```python
import torch

def _softmax(x, axis=-1):
    return torch.softmax(x, dim=axis)

def _gelu(x):
    return torch.nn.functional.gelu(x, approximate='tanh')

def _silu(x):
    return torch.nn.functional.silu(x)

def _sigmoid(x):
    return torch.sigmoid(x)

def _layernorm(x, axis=-1, eps=1e-05):
    m = x.mean(dim=axis, keepdim=True)
    v = x.var(dim=axis, keepdim=True, unbiased=False)
    return (x - m) / torch.sqrt(v + eps)

def _scatter_add(tgt, idx, src, axis=0):
    return tgt.clone().index_add_(axis, idx, src)

def _segment_sum(src, seg, n):
    out = torch.zeros((n,) + src.shape[1:], dtype=src.dtype)
    return out.index_add_(0, seg, src)

def _pad(x, pw):
    flat = tuple((v for pair in reversed(pw) for v in pair))
    return torch.nn.functional.pad(x, flat)
kernel_name = 'parallel_sum__memory_aug_recurrent_mem_tr__geglu_ffn__prompt'

def setup_inputs():

    _w = lambda a: (lambda t: t.to(torch.bfloat16) if t.is_floating_point() else t)(torch.as_tensor(a))

    def _A_setup():
        B, S, Dm = (1, 2048, 512)
        H, Dh = (32, 128)
        M, seg = (8, 32)
        seg = min(seg, S)
        S = S // seg * seg
        return {'x': _w((lambda *s: torch.randn(*s, dtype=torch.bfloat16) * 0.05)(B, S, Dm)), 'mem0': _w((lambda *s: torch.randn(*s, dtype=torch.bfloat16) * 1.6)(B, M, Dm)), 'Wq': _w((lambda *s: torch.randn(*s, dtype=torch.bfloat16) * 0.8)(Dm, H, Dh) * Dm ** (-0.5)), 'Wk': _w((lambda *s: torch.randn(*s, dtype=torch.bfloat16) * 0.8)(Dm, H, Dh) * Dm ** (-0.5)), 'Wv': _w((lambda *s: torch.randn(*s, dtype=torch.bfloat16) * 1.6)(Dm, H, Dh) * Dm ** (-0.5)), 'Wo': _w((lambda *s: torch.randn(*s, dtype=torch.bfloat16) * 1.6)(H, Dh, Dm) * (H * Dh) ** (-0.5)), 'seg_len': seg}
    _A_in = _A_setup()

    def _B_setup():
        B, S, D, F = (1, 2048, 512, 1792)
        return {'x': _w((lambda *s: torch.randn(*s, dtype=torch.bfloat16) * 0.05)(B, S, D)), 'w_gate': _w((lambda *s: torch.randn(*s, dtype=torch.bfloat16) * 0.05)(D, F)), 'w_up': _w((lambda *s: torch.randn(*s, dtype=torch.bfloat16) * 0.05)(D, F)), 'w_down': _w((lambda *s: torch.randn(*s, dtype=torch.bfloat16) * 0.05)(F, D))}
    _B_in = _B_setup()
    return {'A_x': _A_in['x'], 'A_mem0': _A_in['mem0'], 'A_Wq': _A_in['Wq'], 'A_Wk': _A_in['Wk'], 'A_Wv': _A_in['Wv'], 'A_Wo': _A_in['Wo'], 'A_seg_len': _A_in['seg_len'], 'B_x': _B_in['x'], 'B_w_gate': _B_in['w_gate'], 'B_w_up': _B_in['w_up'], 'B_w_down': _B_in['w_down']}

def reference(A_x, A_mem0, A_Wq, A_Wk, A_Wv, A_Wo, A_seg_len, B_x, B_w_gate, B_w_up, B_w_down):

    def _A_ref(x, mem0, Wq, Wk, Wv, Wo, seg_len):
        B, S, Dm = x.shape
        H, Dh = (Wq.shape[1], Wq.shape[2])
        M = mem0.shape[1]
        scale = Dh ** (-0.5)
        n_seg = S // seg_len
        L = M + seg_len
        idx = torch.arange(L)
        mask = (idx[:, None] < M) | (idx[:, None] >= idx[None, :])
        mem = mem0
        outs = []
        for s in range(n_seg):
            xs = x[:, s * seg_len:(s + 1) * seg_len, :].clone()
            h = torch.concatenate([mem, xs], axis=1)
            q = torch.einsum('btd,dhe->bhte', h, Wq)
            k = torch.einsum('btd,dhe->bhte', h, Wk)
            v = torch.einsum('btd,dhe->bhte', h, Wv)
            scores = torch.einsum('bhqe,bhke->bhqk', q, k) * scale
            scores = torch.where(mask[None, None], scores, -1000000000.0)
            p = _softmax(scores, axis=-1)
            o = torch.einsum('bhqk,bhke->bhqe', p, v)
            o = torch.einsum('bhte,hed->btd', o, Wo)
            mem = o[:, :M, :].clone()
            outs.append(o[:, M:, :])
        return torch.concatenate(outs, axis=1)

    def _B_ref(x, w_gate, w_up, w_down):
        return _gelu(x @ w_gate) * (x @ w_up) @ w_down
    x = A_x
    xn = _layernorm(x, axis=-1)
    return x + _A_ref(xn, A_mem0, A_Wq, A_Wk, A_Wv, A_Wo, A_seg_len) + _B_ref(xn, B_w_gate, B_w_up, B_w_down)
```

```cpp
#include <hip/hip_runtime.h>
#include <hip/hip_bf16.h>

typedef unsigned u32;
typedef u32 v2u __attribute__((ext_vector_type(2)));
typedef u32 v4u __attribute__((ext_vector_type(4)));
typedef u32 v8u __attribute__((ext_vector_type(8)));
typedef u32 v16u __attribute__((ext_vector_type(16)));
typedef u32 v32u __attribute__((ext_vector_type(32)));
typedef float v4f __attribute__((ext_vector_type(4)));
typedef __bf16 v8bf __attribute__((ext_vector_type(8)));
typedef __bf16 v2bf __attribute__((ext_vector_type(2)));
typedef float v2f __attribute__((ext_vector_type(2)));
typedef __amdgpu_buffer_rsrc_t rsrc_t;
#define DEV __device__ __forceinline__
#define LAS __attribute__((address_space(3)))
#define GAS __attribute__((address_space(1)))

#define SWAP 0
#define ROT 1
constexpr int NSEG = 64, FF = 1792;
constexpr u32 MB = 1u << 20;
constexpr u32 O_CTL = 0;
constexpr u32 O_F = 4096;
constexpr u32 O_FFN = O_F + 32 * 256;
constexpr u32 O_DONE = O_FFN + 16 * 128;
constexpr u32 O_LN = O_DONE + 128 * 128;
constexpr u32 O_DBG = 65536;
constexpr u32 O_XN = MB / 2, O_H = 4 * MB;
constexpr u32 RS = 65536, HS = 64 * RS, O_REC = 16 * MB;
constexpr u32 O_EX = O_REC + 132 * MB;
constexpr u32 WS_BYTES = 158 * MB;
constexpr u32 OOB = 0x7ffffff0u;
constexpr int AUX_SC1 = 16;
constexpr u32 SPIN_CAP = 1u << 20;

struct Params {
  const u32* x; const u32* mem0; const u32* wq; const u32* wk; const u32* wv; const u32* wo;
  const u32* wg; const u32* wu; const u32* wd; u32* out; unsigned char* ws;
  int dA, dB, mA, mB, vis, pen;
};

DEV u32 rel(u32 b) { return ((b >> 13) << 14) | (b & 8191u); }
DEV u32 pk(float a, float b) { v2f f = {a, b}; return __builtin_bit_cast(u32, __builtin_convertvector(f, v2bf)); }
DEV float bfr(float a) { return __uint_as_float(pk(a, 0.f) << 16); }
DEV float lo16(u32 a) { return __uint_as_float(a << 16); }
DEV float hi16(u32 a) { return __uint_as_float(a & 0xffff0000u); }
DEV float quad_sum(float s) {
  const auto a = __builtin_amdgcn_permlane16_swap(__float_as_uint(s), __float_as_uint(s), false, false);
  s = __uint_as_float(a[0]) + __uint_as_float(a[1]);
  const auto c = __builtin_amdgcn_permlane32_swap(__float_as_uint(s), __float_as_uint(s), false, false);
  return __uint_as_float(c[0]) + __uint_as_float(c[1]);
}
DEV rsrc_t mk(const void* p, u32 bytes) { return __builtin_amdgcn_make_buffer_rsrc((void*)p, 0, bytes, 0x00020000); }
DEV u32 al_off(const unsigned char* ws) { return (0u - (u32)(size_t)ws) & (2 * MB - 1); }
DEV u32 ex_off(const unsigned char* ws) { return O_EX + al_off(ws); }
DEV void drain() { asm volatile("s_waitcnt vmcnt(0)" ::: "memory"); }

DEV void publish(const Params& p, u32 off, u32 val, int tid) {
  const u32 tag = *(const u32*)(p.ws + O_CTL) + 1u;
  drain();
  __syncthreads();
  __builtin_amdgcn_raw_buffer_store_b64((v2u){val, tag}, mk(p.ws, WS_BYTES), tid < 8 ? off + tid * 8 : OOB, 0, AUX_SC1);
}
DEV void layer_norm(const Params& p, int i, int tid, int lane, int w) {
  const int tile = w * 48 + i;
  if (tile < 128) {
    const rsrc_t rx = mk(p.x, 2048 * 1024), ws = mk(p.ws, WS_BYTES);
    v4u xin[16];
#pragma unroll
    for (int ks = 0; ks < 16; ++ks) xin[ks] = __builtin_amdgcn_raw_buffer_load_b128(rx, (lane & 15) * 1024 + (lane >> 4) * 16 + ks * 64, tile * 16384, 0);
    v2f f[64], s = {0.f, 0.f};
#pragma unroll
    for (int j = 0; j < 64; ++j) { f[j] = (v2f){lo16(xin[j >> 2][j & 3]), hi16(xin[j >> 2][j & 3])}; s += f[j]; }
    const float mean = quad_sum(s.x + s.y) * (1.f / 512.f);
    v2f q = {0.f, 0.f};
#pragma unroll
    for (int j = 0; j < 64; ++j) { const v2f d = f[j] - mean; q += d * d; }
    const float inv = 1.f / bfr(sqrtf(bfr(bfr(quad_sum(q.x + q.y) * (1.f / 512.f)) + 1e-5f))), mb = bfr(mean);
#pragma unroll
    for (int ks = 0; ks < 16; ++ks) {
      v4u o;
#pragma unroll
      for (int j = 0; j < 4; ++j) { const v2f r = (f[4 * ks + j] - mb) * inv; o[j] = pk(r.x, r.y); }
      __builtin_amdgcn_raw_buffer_store_b128(o, ws, O_XN + tile * 16384 + lane * 16 + ks * 1024, 0, AUX_SC1);
    }
  }
  publish(p, O_LN + i * 128, 1u, tid);
}
template <int KIND>
DEV void probe(const Params& p, int tid, int lane, int w) {
  constexpr int NP = KIND ? 4 : 2;
  u32 best[NP];
#pragma unroll
  for (int i = 0; i < NP; ++i) best[i] = ~0u;
  if (w == 0) {
    const rsrc_t ws = mk(p.ws, WS_BYTES);
#pragma unroll 1
    for (int r2 = 0; r2 < 12 && (r2 < (KIND ? 5 : 3) || min(best[0], best[NP - 1]) > 620 || min(best[0], best[NP - 1]) + 100 > max(best[0], best[NP - 1])); ++r2)
#pragma unroll
      for (int i = 0; i < NP; ++i) {
        const u32 off = KIND ? 0x1f1000u + (i & 1) * 16384 + (i >> 1) * 2 * MB : 0x1fd000u + i * 8192;
        drain();
        const u32 t0 = (u32)__builtin_amdgcn_s_memtime();
        asm volatile("s_waitcnt lgkmcnt(0)" ::: "memory");
        __builtin_amdgcn_raw_buffer_store_b128((v4u){0u, 0u, 0u, 0u}, ws, lane < 8 ? ex_off(p.ws) + off + lane * 16 : OOB, 0, AUX_SC1);
        drain();
        const u32 t1 = (u32)__builtin_amdgcn_s_memtime();
        best[i] = min(best[i], t1 - t0);
      }
  }
  publish(p, O_LN + (48 + KIND) * 128, (best[0] < best[NP - 1] ? best[0] < best[1] : best[NP - 2] < best[NP - 1]) ? 1u : 0u, tid);
}

DEV void put_srd(v16u& d, int i, const void* p, u32 bytes) {
  d[4 * i] = (u32)(size_t)p; d[4 * i + 1] = (u32)((size_t)p >> 32) & 0xffffu; d[4 * i + 2] = bytes; d[4 * i + 3] = 0x00020000u;
}
__global__ void __launch_bounds__(256, 1) k_main(Params p) {
  {
    u32 dead;
    asm volatile("s_getpc_b64 s[20:21]\n.Lpf0:\n\tv_lshlrev_b32 %0, 7, %1\n\tv_min_u32 %0, Lend-.Lpf0, %0\n\tglobal_load_dword v255, %0, s[20:21]"
                 : "=&v"(dead) : "v"(threadIdx.x + ((blockIdx.x & 8) << 5)) : "s20", "s21", "v255", "memory");
  }
  const int b = blockIdx.x ^ SWAP, x = b & 7, y = b >> 3, xx = x & 3;
  const int tid = threadIdx.x, lane = tid & 63, w = __builtin_amdgcn_readfirstlane(tid >> 6);
  const int n = lane & 15, g = lane >> 4;
  int role = x < 4 ? 0 : y < 8 ? 4 : y < 16 ? 5 : y < 28 ? 7 : (y < 31 || xx < 2) ? 6 : 8;
  if (role == 8) { if (xx == 2) probe<0>(p, tid, lane, w); else probe<1>(p, tid, lane, w); }
  if (role == 7) layer_norm(p, (y - 16) * 4 + xx, tid, lane, w);
  u32 vc[40], sc[16], s84 = 0;
#pragma unroll
  for (int i = 0; i < 40; ++i) vc[i] = 0;
#pragma unroll
  for (int i = 0; i < 16; ++i) sc[i] = 0;
  const void* pa = p.wq; const void* pb = p.wk; const void* pc = p.wv; const void* pd = p.wo;
  u32 na = 4 * MB, nb = 4 * MB, nc = 4 * MB;
  const u32 scr = (role == 0 ? 0u : 98304u) + w * 8192;
  const u32 sw128 = scr + (lane >> 3) * 160 + (lane & 7) * 16, sr128 = scr + (lane >> 2) * 160 + (lane & 3) * 8;
  const u32 sw64 = scr + (lane >> 2) * 96 + (lane & 3) * 16, sr64 = scr + (lane >> 2) * 96 + (lane & 3) * 8;
  const u32 row128 = 8 * (lane >> 5) + ((lane >> 3) & 3), row64 = 8 * g + ((lane >> 2) & 3);
  const u32 lead = tid < 8 ? tid * 8 : OOB;
  const int hd = role == 0 ? x * 8 + (y >> 2) : xx * 8 + (y & 7);
  const u32 rec = O_REC + al_off(p.ws), rn = rec + hd * HS, rf = rn + 8192, dlt = 8192;
  vc[12] = O_LN + min(lane, 48) * 128;
  const u32 o_kx = rf + (hd & 3) * 16384, o_qx = rf + ((hd + 1) & 3) * 16384, o_ym = rf + ((hd + 2) & 3) * 16384, o_px = rf + ((hd + 3) & 3) * 16384;
  if (role == 0) {
    const int h = x * 8 + (y >> 2), c = y & 3, j = c * 32 + h;
    const u32 ex = ex_off(p.ws);
    const int c2 = (lane >> 3) & 1, i8 = lane & 7, cr = c & ROT;
#pragma unroll
    for (int t = 0; t < 4; ++t) vc[t] = (i8 >> 1) == t ? ((i8 & 1) ? 0x3f800000u : 0x00003f80u) : 0u;
    vc[4] = (128 * w + 4 * (g >> 1) + (g & 1)) * 8192 + h * 256 + (n ^ (2 * g)) * 16;
    vc[5] = (128 * w + g) * 8192 + h * 256 + (n ^ g) * 16;
    vc[6] = (h * 128 + 32 * w + g) * 1024 + c * 256 + (n ^ (2 * g)) * 16;
    vc[7] = w * 4096 + (lane >> 2) * 256 + ((2 * w) ^ ((lane >> 2) & 7)) * 32 + (lane & 3) * 8;
    vc[8] = w * 4096 + n * 256 + ((4 * w + g) ^ n) * 16;
#pragma unroll
    for (int t = 0; t < 4; ++t) vc[24 + t] = 32768 + (t ^ w) * 2048 + lane * 16;
    vc[16] = cr << 18; vc[17] = w == 1 ? (j >> 3) * 16384 + (j & 7) * 512 + lane * 8 : OOB;
    vc[36] = (lane * 16) ^ ((cr & 1) * 512); vc[37] = OOB;
    vc[13] = (p.mB & 0xffff) | (p.mA << 16); vc[14] = tid * 16; vc[15] = (u32)(8 * c + i8 - 16 * c2 - 4 * g);
    const u32 irow = (16 * w + n) * 32;
    vc[20] = (n & 2) ? 0xEEEEu : 0x4444u; vc[21] = lane * 16; vc[22] = lane * 8;
    vc[23] = (g >> 1) * 256 + (i8 >> 1) * 64 + (g & 1) * 32 + (i8 & 1) * 16 + c2 * 8;
    vc[28] = 40960 + irow + (((g >> 1) ^ (n >> 2)) * 2 + (g & 1)) * 4; vc[29] = 43008 + irow + (g ^ (n >> 2)) * 8; vc[30] = (lane ^ g) * 8;
    const u32 qo = c2 * 2048 + (g * 16 + 8 * (c & 1) + i8) * 16;
    vc[31] = w == 0 ? rel((c * 32 + (lane >> 2)) * 2048 + h * 64 + (lane & 3) * 16) : w == 1 ? qo : w == 2 ? (lane & 1) * 128 : lane * 32;
    vc[32] = w == 0 ? (g < 2 ? (n >> 2) * 16384 + j * 64 + (n & 3) * 16 + g * 8 : OOB) : w == 1 ? (c2 == 0 ? (c >> 1) * 1024 + (g * 16 + 8 * (c & 1) + i8) * 16 : (i8 & 3) == 0 ? 2048 + c * 128 + g * 32 + i8 * 4 : OOB) : (w == 2 && lane < 8) ? lane * 8 : OOB;
    vc[34] = lane < 4 ? (y * 4 + x) * 32 + lane * 8 : OOB;
    vc[35] = w * 8192 + lane * 16;
    sc[0] = ex + 0x180000u; sc[1] = ex; sc[2] = ex + rel(j * 2048 + w * 512);
    sc[3] = rn + c * 16384 + w * 2048;
    sc[4] = w == 1 ? o_qx + (c >> 1) * 4096 : ex + 0x1c0000u;
    sc[5] = o_ym + (4 * c + w) * 512;
    sc[6] = o_px;
    sc[7] = p.pen | (p.vis << 16);
    sc[8] = O_F + h * 256;
    sc[9] = w == 3 ? ex + 0x1c0000u : O_DONE + (h * 4 + c) * 128;
    sc[10] = p.dA; sc[11] = p.dB; sc[12] = dlt; sc[13] = x * 16384 + w * 2048 + (cr >> 1) * 1024; sc[14] = w * 4096;
    s84 = o_kx;
    {
      const int ga = (j >> 2) * 2 + ((n >> 1) & 1), row = 2 * (j & 3) + (n & 1);
      const size_t ad = (size_t)p.mem0 + row * 1024 + (32 * (ga >> 2) + 16 * (g & 1) + 4 * (ga & 3)) * 2;
      vc[18] = (u32)ad; vc[19] = (u32)(ad >> 32);
    }
    role = w;
  } else if (role == 4) {
    const int h = xx * 8 + y;
    pa = w < 2 ? p.wq : p.wk;
    vc[0] = row128 * 8192 + h * 256 + (w & 1) * 128 + (lane & 7) * 16; vc[1] = sw128; vc[2] = sr128; vc[3] = lane * 16; vc[4] = lead; vc[5] = OOB;
    sc[0] = O_XN + w * 8192; sc[1] = (w < 2 ? o_qx : o_kx) + (w & 1) * 1024; sc[2] = O_F + h * 256; sc[3] = w * 8192; sc[4] = 0u - dlt;
  } else if (role == 6) {
    const int id = (y - 28) * 4 + xx;
    pa = p.wg; pb = p.wu; na = nb = 512 * FF * 2;
    vc[0] = row64 * (FF * 2) + id * 256 + w * 64 + (lane & 3) * 16; vc[1] = sw64; vc[2] = sr64; vc[3] = lane * 16; vc[4] = lead; vc[5] = OOB;
    sc[0] = O_XN + w * 8192; sc[1] = O_H + (id * 4 + w) * 2048; sc[2] = O_FFN + id * 128; sc[3] = w * 8192;
  } else if (role == 5) {
    const int h = xx * 8 + y - 8;
    pa = p.wv; pb = p.wo;
    vc[0] = row64 * 8192 + h * 256 + w * 64 + (lane & 3) * 16; vc[1] = sw64; vc[2] = sr64; vc[3] = lane * 16; vc[4] = lead;
    vc[5] = (h * 128 + (lane >> 3)) * 1024 + w * 256 + (lane & 7) * 16; vc[6] = sw128; vc[7] = sr128;
    vc[8] = 131072 + w * 1024 + lane * 16; vc[9] = 131072 + lane * 16;
    sc[0] = O_XN + w * 8192; sc[1] = rn + w * 16384; sc[2] = O_F + h * 256 + 128; sc[3] = w * 8192; sc[4] = dlt;
  } else if (role == 7) {
    const int grp = y < 24 ? xx : 4 + (xx >> 1), kap = y < 24 ? (y & 7) : (xx & 1) * 4 + (y & 3);
    pa = p.wd; pb = p.x; pc = p.out; na = FF * 512 * 2; nb = nc = 2048 * 512 * 2;
    vc[0] = (w * 448 + (lane >> 3)) * 1024 + kap * 128 + (lane & 7) * 16; vc[1] = sw128; vc[2] = sr128; vc[3] = lane * 16; vc[4] = (lane & 31) * 16 + (lane >> 5) * 8;
    vc[5] = (n >> 3) * 128 + g * 32 + (n & 7) * 4;
    vc[6] = min(lane, 13) * 128; vc[7] = (lane & 7) * 256; vc[8] = (lane & 31) * 128;
    vc[9] = w * 8704 + n * 272 + g * 16; vc[10] = (8 * w + (lane >> 3)) * 272 + (lane & 7) * 32;
    vc[11] = (8 * w + (lane >> 3)) * 1024 + kap * 128 + (lane & 7) * 16;
    const u32 sh = rec + grp * RS + w * 8 * HS;
    sc[0] = grp; sc[1] = O_H + (grp * 56 + w * 14) * 2048; sc[2] = sh + (kap >> 1) * 16384 + (kap & 1) * 4096; sc[3] = sh; sc[4] = 0xaaaau;
    sc[5] = sh + kap * 1024; sc[6] = O_FFN; sc[7] = O_F + w * 2048 + 128; sc[8] = O_DONE + w * 4096;
    sc[9] = grp * 32768; vc[13] = (grp == 63 % 6 && kap == 0 && tid == 0) ? O_CTL : OOB;
  }
  sc[15] = role;
  v16u scv, srds; v32u v0; v8u v1;
#pragma unroll
  for (int i = 0; i < 16; ++i) scv[i] = __builtin_amdgcn_readfirstlane(sc[i]);
  put_srd(srds, 0, pa, na); put_srd(srds, 1, pb, nb); put_srd(srds, 2, pc, nc); put_srd(srds, 3, pd, 4 * MB);
#pragma unroll
  for (int i = 0; i < 16; ++i) srds[i] = __builtin_amdgcn_readfirstlane(srds[i]);
  s84 = __builtin_amdgcn_readfirstlane(s84);
#pragma unroll
  for (int i = 0; i < 8; ++i) v1[i] = vc[i];
#pragma unroll
  for (int i = 0; i < 32; ++i) v0[i] = vc[8 + i];
  const v4u srd = {(u32)(size_t)p.ws, (u32)((size_t)p.ws >> 32) & 0xffffu, WS_BYTES, 0x00020000u};
  asm volatile(
      "s_nop 4\n"
      "s_load_dword s85, s[8:9], 0x0\n"
      "s_mov_b32 s36, 0\n"
      "s_mov_b32 s37, 0\n"
      "s_cmp_lt_u32 s27, 4\n"
      "s_cbranch_scc1 LC_init\n"
      "s_cmp_eq_u32 s27, 4\n"
      "s_cbranch_scc1 LP0_init\n"
      "s_cmp_eq_u32 s27, 5\n"
      "s_cbranch_scc1 LV_init\n"
      "s_cmp_eq_u32 s27, 6\n"
      "s_cbranch_scc1 LP1_init\n"
      "s_cmp_eq_u32 s27, 7\n"
      "s_cbranch_scc1 LO_init\n"
      "s_branch Lend\n"
      "LC_init:\n"
      "v_readfirstlane_b32 s31, v104\n"
      "v_xor_b32 v97, 0x80, v92\n"
      "v_xor_b32 v98, 0x80, v94\n"
      "v_xor_b32 v99, 64, v93\n"
      "v_xor_b32 v100, 128, v93\n"
      "v_xor_b32 v81, 192, v93\n"
      "s_add_u32 m0, s26, 0x18000\n"
      "s_mov_b32 s28, 0x0\n"
      "buffer_load_dwordx4 v94, s[80:83], s28 offen lds\n"
      "s_add_u32 m0, s26, 0x18400\n"
      "s_mov_b32 s28, 0x2000\n"
      "buffer_load_dwordx4 v98, s[80:83], s28 offen lds\n"
      "s_add_u32 m0, s26, 0x18800\n"
      "s_mov_b32 s28, 0x4000\n"
      "buffer_load_dwordx4 v94, s[80:83], s28 offen lds\n"
      "s_add_u32 m0, s26, 0x18c00\n"
      "s_mov_b32 s28, 0x6000\n"
      "buffer_load_dwordx4 v98, s[80:83], s28 offen lds\n"
      "s_add_u32 m0, s26, 0x1c000\n"
      "s_mov_b32 s28, 0x1000\n"
      "buffer_load_dwordx4 v94, s[80:83], s28 offen lds\n"
      "s_add_u32 m0, s26, 0x1c400\n"
      "s_mov_b32 s28, 0x3000\n"
      "buffer_load_dwordx4 v98, s[80:83], s28 offen lds\n"
      "s_add_u32 m0, s26, 0x1c800\n"
      "s_mov_b32 s28, 0x5000\n"
      "buffer_load_dwordx4 v94, s[80:83], s28 offen lds\n"
      "s_add_u32 m0, s26, 0x1cc00\n"
      "s_mov_b32 s28, 0x7000\n"
      "buffer_load_dwordx4 v98, s[80:83], s28 offen lds\n"
      "global_load_dwordx2 v[82:83], v[106:107], off\n"
      "s_add_u32 m0, s26, 0x0\n"
      "s_xor_b32 s28, s31, 0x0\n"
      "buffer_load_dwordx4 v93, s[76:79], s28 offen lds\n"
      "s_add_u32 m0, s26, 0x400\n"
      "s_xor_b32 s28, s31, 0x20000\n"
      "buffer_load_dwordx4 v99, s[76:79], s28 offen lds\n"
      "s_add_u32 m0, s26, 0x800\n"
      "s_xor_b32 s28, s31, 0x10000\n"
      "buffer_load_dwordx4 v100, s[76:79], s28 offen lds\n"
      "s_add_u32 m0, s26, 0xc00\n"
      "s_xor_b32 s28, s31, 0x30000\n"
      "buffer_load_dwordx4 v81, s[76:79], s28 offen lds\n"
      "s_add_u32 m0, s26, 0x4000\n"
      "s_xor_b32 s28, s31, 0x0\n"
      "buffer_load_dwordx4 v92, s[68:71], s28 offen lds\n"
      "s_add_u32 m0, s26, 0x4400\n"
      "s_xor_b32 s28, s31, 0x20000\n"
      "buffer_load_dwordx4 v97, s[68:71], s28 offen lds\n"
      "s_add_u32 m0, s26, 0x4800\n"
      "s_xor_b32 s28, s31, 0x10000\n"
      "buffer_load_dwordx4 v92, s[68:71], s28 offen lds\n"
      "s_add_u32 m0, s26, 0x4c00\n"
      "s_xor_b32 s28, s31, 0x30000\n"
      "buffer_load_dwordx4 v97, s[68:71], s28 offen lds\n"
      "s_add_u32 m0, s26, 0x8000\n"
      "s_xor_b32 s28, s31, 0x0\n"
      "buffer_load_dwordx4 v92, s[72:75], s28 offen lds\n"
      "s_add_u32 m0, s26, 0x8400\n"
      "s_xor_b32 s28, s31, 0x20000\n"
      "buffer_load_dwordx4 v97, s[72:75], s28 offen lds\n"
      "s_add_u32 m0, s26, 0x8800\n"
      "s_xor_b32 s28, s31, 0x10000\n"
      "buffer_load_dwordx4 v92, s[72:75], s28 offen lds\n"
      "s_add_u32 m0, s26, 0x8c00\n"
      "s_xor_b32 s28, s31, 0x30000\n"
      "buffer_load_dwordx4 v97, s[72:75], s28 offen lds\n"
      "s_add_u32 m0, s26, 0xc000\n"
      "s_xor_b32 s28, s31, 0x8000\n"
      "buffer_load_dwordx4 v93, s[76:79], s28 offen lds\n"
      "s_add_u32 m0, s26, 0xc400\n"
      "s_xor_b32 s28, s31, 0x28000\n"
      "buffer_load_dwordx4 v99, s[76:79], s28 offen lds\n"
      "s_add_u32 m0, s26, 0xc800\n"
      "s_xor_b32 s28, s31, 0x18000\n"
      "buffer_load_dwordx4 v100, s[76:79], s28 offen lds\n"
      "s_add_u32 m0, s26, 0xcc00\n"
      "s_xor_b32 s28, s31, 0x38000\n"
      "buffer_load_dwordx4 v81, s[76:79], s28 offen lds\n"
      "s_add_u32 m0, s26, 0x10000\n"
      "s_xor_b32 s28, s31, 0x4000\n"
      "buffer_load_dwordx4 v92, s[68:71], s28 offen lds\n"
      "s_add_u32 m0, s26, 0x10400\n"
      "s_xor_b32 s28, s31, 0x24000\n"
      "buffer_load_dwordx4 v97, s[68:71], s28 offen lds\n"
      "s_add_u32 m0, s26, 0x10800\n"
      "s_xor_b32 s28, s31, 0x14000\n"
      "buffer_load_dwordx4 v92, s[68:71], s28 offen lds\n"
      "s_add_u32 m0, s26, 0x10c00\n"
      "s_xor_b32 s28, s31, 0x34000\n"
      "buffer_load_dwordx4 v97, s[68:71], s28 offen lds\n"
      "s_add_u32 m0, s26, 0x14000\n"
      "s_xor_b32 s28, s31, 0x4000\n"
      "buffer_load_dwordx4 v92, s[72:75], s28 offen lds\n"
      "s_add_u32 m0, s26, 0x14400\n"
      "s_xor_b32 s28, s31, 0x24000\n"
      "buffer_load_dwordx4 v97, s[72:75], s28 offen lds\n"
      "s_add_u32 m0, s26, 0x14800\n"
      "s_xor_b32 s28, s31, 0x14000\n"
      "buffer_load_dwordx4 v92, s[72:75], s28 offen lds\n"
      "s_add_u32 m0, s26, 0x14c00\n"
      "s_xor_b32 s28, s31, 0x34000\n"
      "buffer_load_dwordx4 v97, s[72:75], s28 offen lds\n"
      "v_mov_b32 v84, -1\n"
      "v_mov_b32 v85, -1\n"
      "s_cmp_eq_u32 s27, 0\n"
      "s_cselect_b64 vcc, -1, 0\n"
      "v_mov_b32 v86, 0x7ffffff0\n"
      "v_cndmask_b32 v86, v86, v120, vcc\n"
      "s_waitcnt vmcnt(24) lgkmcnt(0)\n"
      "s_add_u32 s85, s85, 1\n"
      "s_barrier\n"
      "v_xor_b32 v48, 0x0, v95\n"
      "v_xor_b32 v49, 0x20, v95\n"
      "v_xor_b32 v50, 0x1000, v95\n"
      "v_xor_b32 v51, 0x1020, v95\n"
      "v_xor_b32 v52, 0x2000, v95\n"
      "v_xor_b32 v53, 0x2020, v95\n"
      "v_xor_b32 v54, 0x3000, v95\n"
      "v_xor_b32 v55, 0x3020, v95\n"
      "v_add_u32 v48, 0x18000, v48\n"
      "v_add_u32 v49, 0x18000, v49\n"
      "v_add_u32 v50, 0x18000, v50\n"
      "v_add_u32 v51, 0x18000, v51\n"
      "v_add_u32 v52, 0x18000, v52\n"
      "v_add_u32 v53, 0x18000, v53\n"
      "v_add_u32 v54, 0x18000, v54\n"
      "v_add_u32 v55, 0x18000, v55\n"
      "ds_read_b64_tr_b16 v[0:1], v48 offset:0\n"
      "ds_read_b64_tr_b16 v[2:3], v48 offset:16384\n"
      "ds_read_b64_tr_b16 v[4:5], v49 offset:0\n"
      "ds_read_b64_tr_b16 v[6:7], v49 offset:16384\n"
      "s_waitcnt lgkmcnt(4)\n"
      "ds_read_b64_tr_b16 v[8:9], v50 offset:0\n"
      "ds_read_b64_tr_b16 v[10:11], v50 offset:16384\n"
      "ds_read_b64_tr_b16 v[12:13], v51 offset:0\n"
      "ds_read_b64_tr_b16 v[14:15], v51 offset:16384\n"
      "s_waitcnt lgkmcnt(4)\n"
      "ds_read_b64_tr_b16 v[16:17], v52 offset:0\n"
      "ds_read_b64_tr_b16 v[18:19], v52 offset:16384\n"
      "ds_read_b64_tr_b16 v[20:21], v53 offset:0\n"
      "ds_read_b64_tr_b16 v[22:23], v53 offset:16384\n"
      "s_waitcnt lgkmcnt(4)\n"
      "ds_read_b64_tr_b16 v[24:25], v54 offset:0\n"
      "ds_read_b64_tr_b16 v[26:27], v54 offset:16384\n"
      "ds_read_b64_tr_b16 v[28:29], v55 offset:0\n"
      "ds_read_b64_tr_b16 v[30:31], v55 offset:16384\n"
      "s_waitcnt lgkmcnt(4)\n"
      "s_waitcnt vmcnt(12) lgkmcnt(0)\n"
      "s_barrier\n"
      "v_xor_b32 v56, 0x0, v96\n"
      "v_xor_b32 v57, 0x40, v96\n"
      "v_xor_b32 v58, 0x80, v96\n"
      "v_xor_b32 v59, 0xc0, v96\n"
      "ds_read_b128 v[32:35], v56 offset:0\n"
      "ds_read_b128 v[36:39], v57 offset:0\n"
      "ds_read_b128 v[40:43], v58 offset:0\n"
      "v_xor_b32 v60, 0x1000, v96\n"
      "v_xor_b32 v61, 0x1040, v96\n"
      "v_xor_b32 v62, 0x1080, v96\n"
      "v_xor_b32 v63, 0x10c0, v96\n"
      "v_xor_b32 v48, 0x0, v95\n"
      "v_xor_b32 v49, 0x20, v95\n"
      "v_xor_b32 v50, 0x1000, v95\n"
      "v_xor_b32 v51, 0x1020, v95\n"
      "v_xor_b32 v52, 0x2000, v95\n"
      "v_xor_b32 v53, 0x2020, v95\n"
      "v_xor_b32 v54, 0x3000, v95\n"
      "v_xor_b32 v55, 0x3020, v95\n"
      "s_waitcnt lgkmcnt(2)\n"
      "v_mfma_f32_16x16x32_bf16 v[64:67], v[32:35], v[0:3], 0\n"
      "s_add_u32 s28, s14, 0x0\n"
      "buffer_store_dwordx2 v[84:85], v110, s[8:11], s28 offen sc1\n"
      "v_mfma_f32_16x16x32_bf16 v[68:71], v[32:35], v[4:7], 0\n"
      "ds_read_b128 v[44:47], v59 offset:0\n"
      "ds_read_b64_tr_b16 v[128:129], v48 offset:16384\n"
      "s_waitcnt lgkmcnt(3)\n"
      "v_mfma_f32_16x16x32_bf16 v[64:67], v[36:39], v[8:11], v[64:67]\n"
      "s_add_u32 s28, s14, 0x80000\n"
      "buffer_store_dwordx2 v[84:85], v110, s[8:11], s28 offen sc1\n"
      "v_mfma_f32_16x16x32_bf16 v[68:71], v[36:39], v[12:15], v[68:71]\n"
      "v_xor_b32 v56, 0x2000, v96\n"
      "v_xor_b32 v57, 0x2040, v96\n"
      "v_xor_b32 v58, 0x2080, v96\n"
      "v_xor_b32 v59, 0x20c0, v96\n"
      "ds_read_b128 v[32:35], v60 offset:0\n"
      "ds_read_b64_tr_b16 v[136:137], v49 offset:16384\n"
      "s_waitcnt lgkmcnt(4)\n"
      "v_mfma_f32_16x16x32_bf16 v[64:67], v[40:43], v[16:19], v[64:67]\n"
      "s_add_u32 s28, s14, 0x100000\n"
      "buffer_store_dwordx2 v[84:85], v110, s[8:11], s28 offen sc1\n"
      "v_mfma_f32_16x16x32_bf16 v[68:71], v[40:43], v[20:23], v[68:71]\n"
      "ds_read_b128 v[36:39], v61 offset:0\n"
      "ds_read_b64_tr_b16 a[0:1], v50 offset:16384\n"
      "s_waitcnt lgkmcnt(5)\n"
      "v_mfma_f32_16x16x32_bf16 v[64:67], v[44:47], v[24:27], v[64:67]\n"
      "s_add_u32 s28, s12, 0x0\n"
      "buffer_store_dwordx2 v[82:83], v86, s[8:11], s28 offen sc1\n"
      "v_mfma_f32_16x16x32_bf16 v[68:71], v[44:47], v[28:31], v[68:71]\n"
      "ds_read_b128 v[40:43], v62 offset:0\n"
      "ds_read_b64_tr_b16 a[8:9], v51 offset:16384\n"
      "s_waitcnt lgkmcnt(5)\n"
      "v_mfma_f32_16x16x32_bf16 v[72:75], v[32:35], v[0:3], 0\n"
      "s_add_u32 m0, s26, 0x18000\n"
      "s_xor_b32 s28, s31, 0x40000\n"
      "buffer_load_dwordx4 v93, s[76:79], s28 offen lds\n"
      "v_mfma_f32_16x16x32_bf16 v[76:79], v[32:35], v[4:7], 0\n"
      "ds_read_b128 v[44:47], v63 offset:0\n"
      "ds_read_b64_tr_b16 a[32:33], v52 offset:16384\n"
      "s_waitcnt lgkmcnt(5)\n"
      "v_mfma_f32_16x16x32_bf16 v[72:75], v[36:39], v[8:11], v[72:75]\n"
      "s_add_u32 m0, s26, 0x18400\n"
      "s_xor_b32 s28, s31, 0x60000\n"
      "buffer_load_dwordx4 v99, s[76:79], s28 offen lds\n"
      "v_mfma_f32_16x16x32_bf16 v[76:79], v[36:39], v[12:15], v[76:79]\n"
      "v_xor_b32 v60, 0x3000, v96\n"
      "v_xor_b32 v61, 0x3040, v96\n"
      "v_xor_b32 v62, 0x3080, v96\n"
      "v_xor_b32 v63, 0x30c0, v96\n"
      "ds_read_b128 v[32:35], v56 offset:0\n"
      "ds_read_b64_tr_b16 a[40:41], v53 offset:16384\n"
      "s_waitcnt lgkmcnt(5)\n"
      "v_mfma_f32_16x16x32_bf16 v[72:75], v[40:43], v[16:19], v[72:75]\n"
      "s_add_u32 m0, s26, 0x18800\n"
      "s_xor_b32 s28, s31, 0x50000\n"
      "buffer_load_dwordx4 v100, s[76:79], s28 offen lds\n"
      "v_mfma_f32_16x16x32_bf16 v[76:79], v[40:43], v[20:23], v[76:79]\n"
      "ds_read_b128 v[36:39], v57 offset:0\n"
      "ds_read_b64_tr_b16 a[64:65], v54 offset:16384\n"
      "v_cvt_pk_bf16_f32 v80, v64, v65\n"
      "v_accvgpr_write_b32 a224, v80\n"
      "v_cvt_pk_bf16_f32 v80, v66, v67\n"
      "v_accvgpr_write_b32 a226, v80\n"
      "v_cvt_pk_bf16_f32 v80, v68, v69\n"
      "v_accvgpr_write_b32 a232, v80\n"
      "v_cvt_pk_bf16_f32 v80, v70, v71\n"
      "v_accvgpr_write_b32 a234, v80\n"
      "s_waitcnt lgkmcnt(5)\n"
      "v_mfma_f32_16x16x32_bf16 v[72:75], v[44:47], v[24:27], v[72:75]\n"
      "s_add_u32 m0, s26, 0x18c00\n"
      "s_xor_b32 s28, s31, 0x70000\n"
      "buffer_load_dwordx4 v81, s[76:79], s28 offen lds\n"
      "v_mfma_f32_16x16x32_bf16 v[76:79], v[44:47], v[28:31], v[76:79]\n"
      "ds_read_b128 v[40:43], v58 offset:0\n"
      "ds_read_b64_tr_b16 a[72:73], v55 offset:16384\n"
      "s_waitcnt lgkmcnt(5)\n"
      "v_mfma_f32_16x16x32_bf16 v[64:67], v[32:35], v[0:3], 0\n"
      "s_add_u32 m0, s26, 0x1c000\n"
      "s_xor_b32 s28, s31, 0x40000\n"
      "buffer_load_dwordx4 v92, s[68:71], s28 offen lds\n"
      "v_mfma_f32_16x16x32_bf16 v[68:71], v[32:35], v[4:7], 0\n"
      "ds_read_b128 v[44:47], v59 offset:0\n"
      "ds_read_b64_tr_b16 a[96:97], v48 offset:32768\n"
      "s_waitcnt lgkmcnt(5)\n"
      "v_mfma_f32_16x16x32_bf16 v[64:67], v[36:39], v[8:11], v[64:67]\n"
      "s_add_u32 m0, s26, 0x1c400\n"
      "s_xor_b32 s28, s31, 0x60000\n"
      "buffer_load_dwordx4 v97, s[68:71], s28 offen lds\n"
      "v_mfma_f32_16x16x32_bf16 v[68:71], v[36:39], v[12:15], v[68:71]\n"
      "ds_read_b128 v[32:35], v60 offset:0\n"
      "ds_read_b64_tr_b16 a[104:105], v49 offset:32768\n"
      "s_waitcnt lgkmcnt(5)\n"
      "v_mfma_f32_16x16x32_bf16 v[64:67], v[40:43], v[16:19], v[64:67]\n"
      "s_add_u32 m0, s26, 0x1c800\n"
      "s_xor_b32 s28, s31, 0x50000\n"
      "buffer_load_dwordx4 v92, s[68:71], s28 offen lds\n"
      "v_mfma_f32_16x16x32_bf16 v[68:71], v[40:43], v[20:23], v[68:71]\n"
      "ds_read_b128 v[36:39], v61 offset:0\n"
      "ds_read_b64_tr_b16 a[128:129], v50 offset:32768\n"
      "v_cvt_pk_bf16_f32 v160, v72, v73\n"
      "v_cvt_pk_bf16_f32 v162, v74, v75\n"
      "v_cvt_pk_bf16_f32 v168, v76, v77\n"
      "v_cvt_pk_bf16_f32 v170, v78, v79\n"
      "s_waitcnt lgkmcnt(5)\n"
      "v_mfma_f32_16x16x32_bf16 v[64:67], v[44:47], v[24:27], v[64:67]\n"
      "s_add_u32 m0, s26, 0x1cc00\n"
      "s_xor_b32 s28, s31, 0x70000\n"
      "buffer_load_dwordx4 v97, s[68:71], s28 offen lds\n"
      "v_mfma_f32_16x16x32_bf16 v[68:71], v[44:47], v[28:31], v[68:71]\n"
      "ds_read_b128 v[40:43], v62 offset:0\n"
      "ds_read_b64_tr_b16 a[136:137], v51 offset:32768\n"
      "s_waitcnt lgkmcnt(5)\n"
      "v_mfma_f32_16x16x32_bf16 v[72:75], v[32:35], v[0:3], 0\n"
      "s_add_u32 m0, s26, 0x20000\n"
      "s_xor_b32 s28, s31, 0x40000\n"
      "buffer_load_dwordx4 v92, s[72:75], s28 offen lds\n"
      "v_mfma_f32_16x16x32_bf16 v[76:79], v[32:35], v[4:7], 0\n"
      "ds_read_b128 v[44:47], v63 offset:0\n"
      "ds_read_b64_tr_b16 a[160:161], v52 offset:32768\n"
      "s_waitcnt lgkmcnt(5)\n"
      "v_mfma_f32_16x16x32_bf16 v[72:75], v[36:39], v[8:11], v[72:75]\n"
      "s_add_u32 m0, s26, 0x20400\n"
      "s_xor_b32 s28, s31, 0x60000\n"
      "buffer_load_dwordx4 v97, s[72:75], s28 offen lds\n"
      "v_mfma_f32_16x16x32_bf16 v[76:79], v[36:39], v[12:15], v[76:79]\n"
      "ds_read_b64_tr_b16 a[168:169], v53 offset:32768\n"
      "s_waitcnt lgkmcnt(4)\n"
      "v_mfma_f32_16x16x32_bf16 v[72:75], v[40:43], v[16:19], v[72:75]\n"
      "s_add_u32 m0, s26, 0x20800\n"
      "s_xor_b32 s28, s31, 0x50000\n"
      "buffer_load_dwordx4 v92, s[72:75], s28 offen lds\n"
      "v_mfma_f32_16x16x32_bf16 v[76:79], v[40:43], v[20:23], v[76:79]\n"
      "ds_read_b64_tr_b16 a[192:193], v54 offset:32768\n"
      "v_cvt_pk_bf16_f32 v192, v64, v65\n"
      "v_cvt_pk_bf16_f32 v194, v66, v67\n"
      "v_cvt_pk_bf16_f32 v200, v68, v69\n"
      "v_cvt_pk_bf16_f32 v202, v70, v71\n"
      "s_waitcnt lgkmcnt(3)\n"
      "v_mfma_f32_16x16x32_bf16 v[72:75], v[44:47], v[24:27], v[72:75]\n"
      "s_add_u32 m0, s26, 0x20c00\n"
      "s_xor_b32 s28, s31, 0x70000\n"
      "buffer_load_dwordx4 v97, s[72:75], s28 offen lds\n"
      "v_mfma_f32_16x16x32_bf16 v[76:79], v[44:47], v[28:31], v[76:79]\n"
      "ds_read_b64_tr_b16 a[200:201], v55 offset:32768\n"
      "s_waitcnt vmcnt(16) lgkmcnt(0)\n"
      "s_barrier\n"
      "v_xor_b32 v56, 0x0, v96\n"
      "v_xor_b32 v57, 0x40, v96\n"
      "v_xor_b32 v58, 0x80, v96\n"
      "v_xor_b32 v59, 0xc0, v96\n"
      "ds_read_b128 v[32:35], v56 offset:49152\n"
      "ds_read_b128 v[36:39], v57 offset:49152\n"
      "ds_read_b128 v[40:43], v58 offset:49152\n"
      "v_xor_b32 v60, 0x1000, v96\n"
      "v_xor_b32 v61, 0x1040, v96\n"
      "v_xor_b32 v62, 0x1080, v96\n"
      "v_xor_b32 v63, 0x10c0, v96\n"
      "v_cvt_pk_bf16_f32 v224, v72, v73\n"
      "v_cvt_pk_bf16_f32 v226, v74, v75\n"
      "v_cvt_pk_bf16_f32 v232, v76, v77\n"
      "v_cvt_pk_bf16_f32 v234, v78, v79\n"
      "v_xor_b32 v48, 0x0, v95\n"
      "v_xor_b32 v49, 0x20, v95\n"
      "v_xor_b32 v50, 0x1000, v95\n"
      "v_xor_b32 v51, 0x1020, v95\n"
      "v_xor_b32 v52, 0x2000, v95\n"
      "v_xor_b32 v53, 0x2020, v95\n"
      "v_xor_b32 v54, 0x3000, v95\n"
      "v_xor_b32 v55, 0x3020, v95\n"
      "v_add_u32 v48, 0xc000, v48\n"
      "v_add_u32 v49, 0xc000, v49\n"
      "v_add_u32 v50, 0xc000, v50\n"
      "v_add_u32 v51, 0xc000, v51\n"
      "v_add_u32 v52, 0xc000, v52\n"
      "v_add_u32 v53, 0xc000, v53\n"
      "v_add_u32 v54, 0xc000, v54\n"
      "v_add_u32 v55, 0xc000, v55\n"
      "s_waitcnt lgkmcnt(2)\n"
      "v_mfma_f32_16x16x32_bf16 v[64:67], v[32:35], v[0:3], 0\n"
      "s_add_u32 s28, s12, 0x10000\n"
      "buffer_store_dwordx2 v[84:85], v86, s[8:11], s28 offen sc1\n"
      "v_mfma_f32_16x16x32_bf16 v[68:71], v[32:35], v[4:7], 0\n"
      "ds_read_b128 v[44:47], v59 offset:49152\n"
      "ds_read_b64_tr_b16 v[130:131], v48 offset:16384\n"
      "s_waitcnt lgkmcnt(3)\n"
      "v_mfma_f32_16x16x32_bf16 v[64:67], v[36:39], v[8:11], v[64:67]\n"
      "s_add_u32 s28, s12, 0x20000\n"
      "buffer_store_dwordx2 v[84:85], v86, s[8:11], s28 offen sc1\n"
      "v_mfma_f32_16x16x32_bf16 v[68:71], v[36:39], v[12:15], v[68:71]\n"
      "v_xor_b32 v56, 0x2000, v96\n"
      "v_xor_b32 v57, 0x2040, v96\n"
      "v_xor_b32 v58, 0x2080, v96\n"
      "v_xor_b32 v59, 0x20c0, v96\n"
      "ds_read_b128 v[32:35], v60 offset:49152\n"
      "ds_read_b64_tr_b16 v[138:139], v49 offset:16384\n"
      "s_waitcnt lgkmcnt(4)\n"
      "v_mfma_f32_16x16x32_bf16 v[64:67], v[40:43], v[16:19], v[64:67]\n"
      "s_add_u32 s28, s12, 0x30000\n"
      "buffer_store_dwordx2 v[84:85], v86, s[8:11], s28 offen sc1\n"
      "v_mfma_f32_16x16x32_bf16 v[68:71], v[40:43], v[20:23], v[68:71]\n"
      "ds_read_b128 v[36:39], v61 offset:49152\n"
      "ds_read_b64_tr_b16 a[2:3], v50 offset:16384\n"
      "s_waitcnt lgkmcnt(5)\n"
      "v_mfma_f32_16x16x32_bf16 v[64:67], v[44:47], v[24:27], v[64:67]\n"
      "s_add_u32 s28, s13, 0x1c0000\n"
      "buffer_store_dwordx2 v[84:85], v105, s[8:11], s28 offen sc1\n"
      "v_mfma_f32_16x16x32_bf16 v[68:71], v[44:47], v[28:31], v[68:71]\n"
      "ds_read_b128 v[40:43], v62 offset:49152\n"
      "ds_read_b64_tr_b16 a[10:11], v51 offset:16384\n"
      "s_waitcnt lgkmcnt(5)\n"
      "v_mfma_f32_16x16x32_bf16 v[72:75], v[32:35], v[0:3], 0\n"
      "s_add_u32 m0, s26, 0x0\n"
      "s_xor_b32 s28, s31, 0x48000\n"
      "buffer_load_dwordx4 v93, s[76:79], s28 offen lds\n"
      "v_mfma_f32_16x16x32_bf16 v[76:79], v[32:35], v[4:7], 0\n"
      "ds_read_b128 v[44:47], v63 offset:49152\n"
      "ds_read_b64_tr_b16 a[34:35], v52 offset:16384\n"
      "s_waitcnt lgkmcnt(5)\n"
      "v_mfma_f32_16x16x32_bf16 v[72:75], v[36:39], v[8:11], v[72:75]\n"
      "s_add_u32 m0, s26, 0x400\n"
      "s_xor_b32 s28, s31, 0x68000\n"
      "buffer_load_dwordx4 v99, s[76:79], s28 offen lds\n"
      "v_mfma_f32_16x16x32_bf16 v[76:79], v[36:39], v[12:15], v[76:79]\n"
      "v_xor_b32 v60, 0x3000, v96\n"
      "v_xor_b32 v61, 0x3040, v96\n"
      "v_xor_b32 v62, 0x3080, v96\n"
      "v_xor_b32 v63, 0x30c0, v96\n"
      "ds_read_b128 v[32:35], v56 offset:49152\n"
      "ds_read_b64_tr_b16 a[42:43], v53 offset:16384\n"
      "s_waitcnt lgkmcnt(5)\n"
      "v_mfma_f32_16x16x32_bf16 v[72:75], v[40:43], v[16:19], v[72:75]\n"
      "s_add_u32 m0, s26, 0x800\n"
      "s_xor_b32 s28, s31, 0x58000\n"
      "buffer_load_dwordx4 v100, s[76:79], s28 offen lds\n"
      "v_mfma_f32_16x16x32_bf16 v[76:79], v[40:43], v[20:23], v[76:79]\n"
      "ds_read_b128 v[36:39], v57 offset:49152\n"
      "ds_read_b64_tr_b16 a[66:67], v54 offset:16384\n"
      "v_cvt_pk_bf16_f32 v80, v64, v65\n"
      "v_accvgpr_write_b32 a225, v80\n"
      "v_cvt_pk_bf16_f32 v80, v66, v67\n"
      "v_accvgpr_write_b32 a227, v80\n"
      "v_cvt_pk_bf16_f32 v80, v68, v69\n"
      "v_accvgpr_write_b32 a233, v80\n"
      "v_cvt_pk_bf16_f32 v80, v70, v71\n"
      "v_accvgpr_write_b32 a235, v80\n"
      "s_waitcnt lgkmcnt(5)\n"
      "v_mfma_f32_16x16x32_bf16 v[72:75], v[44:47], v[24:27], v[72:75]\n"
      "s_add_u32 m0, s26, 0xc00\n"
      "s_xor_b32 s28, s31, 0x78000\n"
      "buffer_load_dwordx4 v81, s[76:79], s28 offen lds\n"
      "v_mfma_f32_16x16x32_bf16 v[76:79], v[44:47], v[28:31], v[76:79]\n"
      "ds_read_b128 v[40:43], v58 offset:49152\n"
      "ds_read_b64_tr_b16 a[74:75], v55 offset:16384\n"
      "s_waitcnt lgkmcnt(5)\n"
      "v_mfma_f32_16x16x32_bf16 v[64:67], v[32:35], v[0:3], 0\n"
      "s_add_u32 m0, s26, 0x4000\n"
      "s_xor_b32 s28, s31, 0x44000\n"
      "buffer_load_dwordx4 v92, s[68:71], s28 offen lds\n"
      "v_mfma_f32_16x16x32_bf16 v[68:71], v[32:35], v[4:7], 0\n"
      "ds_read_b128 v[44:47], v59 offset:49152\n"
      "ds_read_b64_tr_b16 a[98:99], v48 offset:32768\n"
      "s_waitcnt lgkmcnt(5)\n"
      "v_mfma_f32_16x16x32_bf16 v[64:67], v[36:39], v[8:11], v[64:67]\n"
      "s_add_u32 m0, s26, 0x4400\n"
      "s_xor_b32 s28, s31, 0x64000\n"
      "buffer_load_dwordx4 v97, s[68:71], s28 offen lds\n"
      "v_mfma_f32_16x16x32_bf16 v[68:71], v[36:39], v[12:15], v[68:71]\n"
      "ds_read_b128 v[32:35], v60 offset:49152\n"
      "ds_read_b64_tr_b16 a[106:107], v49 offset:32768\n"
      "s_waitcnt lgkmcnt(5)\n"
      "v_mfma_f32_16x16x32_bf16 v[64:67], v[40:43], v[16:19], v[64:67]\n"
      "s_add_u32 m0, s26, 0x4800\n"
      "s_xor_b32 s28, s31, 0x54000\n"
      "buffer_load_dwordx4 v92, s[68:71], s28 offen lds\n"
      "v_mfma_f32_16x16x32_bf16 v[68:71], v[40:43], v[20:23], v[68:71]\n"
      "ds_read_b128 v[36:39], v61 offset:49152\n"
      "ds_read_b64_tr_b16 a[130:131], v50 offset:32768\n"
      "v_cvt_pk_bf16_f32 v161, v72, v73\n"
      "v_cvt_pk_bf16_f32 v163, v74, v75\n"
      "v_cvt_pk_bf16_f32 v169, v76, v77\n"
      "v_cvt_pk_bf16_f32 v171, v78, v79\n"
      "s_waitcnt lgkmcnt(5)\n"
      "v_mfma_f32_16x16x32_bf16 v[64:67], v[44:47], v[24:27], v[64:67]\n"
      "s_add_u32 m0, s26, 0x4c00\n"
      "s_xor_b32 s28, s31, 0x74000\n"
      "buffer_load_dwordx4 v97, s[68:71], s28 offen lds\n"
      "v_mfma_f32_16x16x32_bf16 v[68:71], v[44:47], v[28:31], v[68:71]\n"
      "ds_read_b128 v[40:43], v62 offset:49152\n"
      "ds_read_b64_tr_b16 a[138:139], v51 offset:32768\n"
      "s_waitcnt lgkmcnt(5)\n"
      "v_mfma_f32_16x16x32_bf16 v[72:75], v[32:35], v[0:3], 0\n"
      "s_add_u32 m0, s26, 0x8000\n"
      "s_xor_b32 s28, s31, 0x44000\n"
      "buffer_load_dwordx4 v92, s[72:75], s28 offen lds\n"
      "v_mfma_f32_16x16x32_bf16 v[76:79], v[32:35], v[4:7], 0\n"
      "ds_read_b128 v[44:47], v63 offset:49152\n"
      "ds_read_b64_tr_b16 a[162:163], v52 offset:32768\n"
      "s_waitcnt lgkmcnt(5)\n"
      "v_mfma_f32_16x16x32_bf16 v[72:75], v[36:39], v[8:11], v[72:75]\n"
      "s_add_u32 m0, s26, 0x8400\n"
      "s_xor_b32 s28, s31, 0x64000\n"
      "buffer_load_dwordx4 v97, s[72:75], s28 offen lds\n"
      "v_mfma_f32_16x16x32_bf16 v[76:79], v[36:39], v[12:15], v[76:79]\n"
      "ds_read_b64_tr_b16 a[170:171], v53 offset:32768\n"
      "s_waitcnt lgkmcnt(4)\n"
      "v_mfma_f32_16x16x32_bf16 v[72:75], v[40:43], v[16:19], v[72:75]\n"
      "s_add_u32 m0, s26, 0x8800\n"
      "s_xor_b32 s28, s31, 0x54000\n"
      "buffer_load_dwordx4 v92, s[72:75], s28 offen lds\n"
      "v_mfma_f32_16x16x32_bf16 v[76:79], v[40:43], v[20:23], v[76:79]\n"
      "ds_read_b64_tr_b16 a[194:195], v54 offset:32768\n"
      "v_cvt_pk_bf16_f32 v193, v64, v65\n"
      "v_cvt_pk_bf16_f32 v195, v66, v67\n"
      "v_cvt_pk_bf16_f32 v201, v68, v69\n"
      "v_cvt_pk_bf16_f32 v203, v70, v71\n"
      "s_waitcnt lgkmcnt(3)\n"
      "v_mfma_f32_16x16x32_bf16 v[72:75], v[44:47], v[24:27], v[72:75]\n"
      "s_add_u32 m0, s26, 0x8c00\n"
      "s_xor_b32 s28, s31, 0x74000\n"
      "buffer_load_dwordx4 v97, s[72:75], s28 offen lds\n"
      "v_mfma_f32_16x16x32_bf16 v[76:79], v[44:47], v[28:31], v[76:79]\n"
      "ds_read_b64_tr_b16 a[202:203], v55 offset:32768\n"
      "s_waitcnt vmcnt(16) lgkmcnt(0)\n"
      "s_barrier\n"
      "v_xor_b32 v56, 0x0, v96\n"
      "v_xor_b32 v57, 0x40, v96\n"
      "v_xor_b32 v58, 0x80, v96\n"
      "v_xor_b32 v59, 0xc0, v96\n"
      "v_add_u32 v56, 0x18000, v56\n"
      "v_add_u32 v57, 0x18000, v57\n"
      "v_add_u32 v58, 0x18000, v58\n"
      "v_add_u32 v59, 0x18000, v59\n"
      "ds_read_b128 v[32:35], v56 offset:0\n"
      "ds_read_b128 v[36:39], v57 offset:0\n"
      "ds_read_b128 v[40:43], v58 offset:0\n"
      "v_xor_b32 v60, 0x1000, v96\n"
      "v_xor_b32 v61, 0x1040, v96\n"
      "v_xor_b32 v62, 0x1080, v96\n"
      "v_xor_b32 v63, 0x10c0, v96\n"
      "v_add_u32 v60, 0x18000, v60\n"
      "v_add_u32 v61, 0x18000, v61\n"
      "v_add_u32 v62, 0x18000, v62\n"
      "v_add_u32 v63, 0x18000, v63\n"
      "v_cvt_pk_bf16_f32 v225, v72, v73\n"
      "v_cvt_pk_bf16_f32 v227, v74, v75\n"
      "v_cvt_pk_bf16_f32 v233, v76, v77\n"
      "v_cvt_pk_bf16_f32 v235, v78, v79\n"
      "v_xor_b32 v48, 0x0, v95\n"
      "v_xor_b32 v49, 0x20, v95\n"
      "v_xor_b32 v50, 0x1000, v95\n"
      "v_xor_b32 v51, 0x1020, v95\n"
      "v_xor_b32 v52, 0x2000, v95\n"
      "v_xor_b32 v53, 0x2020, v95\n"
      "v_xor_b32 v54, 0x3000, v95\n"
      "v_xor_b32 v55, 0x3020, v95\n"
      "v_add_u32 v48, 0x18000, v48\n"
      "v_add_u32 v49, 0x18000, v49\n"
      "v_add_u32 v50, 0x18000, v50\n"
      "v_add_u32 v51, 0x18000, v51\n"
      "v_add_u32 v52, 0x18000, v52\n"
      "v_add_u32 v53, 0x18000, v53\n"
      "v_add_u32 v54, 0x18000, v54\n"
      "v_add_u32 v55, 0x18000, v55\n"
      "s_waitcnt lgkmcnt(2)\n"
      "v_mfma_f32_16x16x32_bf16 v[64:67], v[32:35], v[0:3], 0\n"
      "s_add_u32 s28, s14, 0x200000\n"
      "buffer_store_dwordx2 v[84:85], v110, s[8:11], s28 offen sc1\n"
      "v_mfma_f32_16x16x32_bf16 v[68:71], v[32:35], v[4:7], 0\n"
      "ds_read_b128 v[44:47], v59 offset:0\n"
      "ds_read_b64_tr_b16 v[132:133], v48 offset:16384\n"
      "s_waitcnt lgkmcnt(3)\n"
      "v_mfma_f32_16x16x32_bf16 v[64:67], v[36:39], v[8:11], v[64:67]\n"
      "s_add_u32 s28, s14, 0x280000\n"
      "buffer_store_dwordx2 v[84:85], v110, s[8:11], s28 offen sc1\n"
      "v_mfma_f32_16x16x32_bf16 v[68:71], v[36:39], v[12:15], v[68:71]\n"
      "v_xor_b32 v56, 0x2000, v96\n"
      "v_xor_b32 v57, 0x2040, v96\n"
      "v_xor_b32 v58, 0x2080, v96\n"
      "v_xor_b32 v59, 0x20c0, v96\n"
      "v_add_u32 v56, 0x18000, v56\n"
      "v_add_u32 v57, 0x18000, v57\n"
      "v_add_u32 v58, 0x18000, v58\n"
      "v_add_u32 v59, 0x18000, v59\n"
      "ds_read_b128 v[32:35], v60 offset:0\n"
      "ds_read_b64_tr_b16 v[140:141], v49 offset:16384\n"
      "s_waitcnt lgkmcnt(4)\n"
      "v_mfma_f32_16x16x32_bf16 v[64:67], v[40:43], v[16:19], v[64:67]\n"
      "s_add_u32 s28, s14, 0x300000\n"
      "buffer_store_dwordx2 v[84:85], v110, s[8:11], s28 offen sc1\n"
      "v_mfma_f32_16x16x32_bf16 v[68:71], v[40:43], v[20:23], v[68:71]\n"
      "ds_read_b128 v[36:39], v61 offset:0\n"
      "ds_read_b64_tr_b16 a[4:5], v50 offset:16384\n"
      "s_waitcnt lgkmcnt(5)\n"
      "v_mfma_f32_16x16x32_bf16 v[64:67], v[44:47], v[24:27], v[64:67]\n"
      "s_add_u32 s28, s12, 0x200000\n"
      "buffer_store_dwordx2 v[82:83], v86, s[8:11], s28 offen sc1\n"
      "v_mfma_f32_16x16x32_bf16 v[68:71], v[44:47], v[28:31], v[68:71]\n"
      "ds_read_b128 v[40:43], v62 offset:0\n"
      "ds_read_b64_tr_b16 a[12:13], v51 offset:16384\n"
      "s_waitcnt lgkmcnt(5)\n"
      "v_mfma_f32_16x16x32_bf16 v[72:75], v[32:35], v[0:3], 0\n"
      "s_add_u32 m0, s26, 0xc000\n"
      "s_xor_b32 s28, s31, 0x80000\n"
      "buffer_load_dwordx4 v93, s[76:79], s28 offen lds\n"
      "v_mfma_f32_16x16x32_bf16 v[76:79], v[32:35], v[4:7], 0\n"
      "ds_read_b128 v[44:47], v63 offset:0\n"
      "ds_read_b64_tr_b16 a[36:37], v52 offset:16384\n"
      "s_waitcnt lgkmcnt(5)\n"
      "v_mfma_f32_16x16x32_bf16 v[72:75], v[36:39], v[8:11], v[72:75]\n"
      "s_add_u32 m0, s26, 0xc400\n"
      "s_xor_b32 s28, s31, 0xa0000\n"
      "buffer_load_dwordx4 v99, s[76:79], s28 offen lds\n"
      "v_mfma_f32_16x16x32_bf16 v[76:79], v[36:39], v[12:15], v[76:79]\n"
      "v_xor_b32 v60, 0x3000, v96\n"
      "v_xor_b32 v61, 0x3040, v96\n"
      "v_xor_b32 v62, 0x3080, v96\n"
      "v_xor_b32 v63, 0x30c0, v96\n"
      "v_add_u32 v60, 0x18000, v60\n"
      "v_add_u32 v61, 0x18000, v61\n"
      "v_add_u32 v62, 0x18000, v62\n"
      "v_add_u32 v63, 0x18000, v63\n"
      "ds_read_b128 v[32:35], v56 offset:0\n"
      "ds_read_b64_tr_b16 a[44:45], v53 offset:16384\n"
      "s_waitcnt lgkmcnt(5)\n"
      "v_mfma_f32_16x16x32_bf16 v[72:75], v[40:43], v[16:19], v[72:75]\n"
      "s_add_u32 m0, s26, 0xc800\n"
      "s_xor_b32 s28, s31, 0x90000\n"
      "buffer_load_dwordx4 v100, s[76:79], s28 offen lds\n"
      "v_mfma_f32_16x16x32_bf16 v[76:79], v[40:43], v[20:23], v[76:79]\n"
      "ds_read_b128 v[36:39], v57 offset:0\n"
      "ds_read_b64_tr_b16 a[68:69], v54 offset:16384\n"
      "v_cvt_pk_bf16_f32 v80, v64, v65\n"
      "v_accvgpr_write_b32 a228, v80\n"
      "v_cvt_pk_bf16_f32 v80, v66, v67\n"
      "v_accvgpr_write_b32 a230, v80\n"
      "v_cvt_pk_bf16_f32 v80, v68, v69\n"
      "v_accvgpr_write_b32 a236, v80\n"
      "v_cvt_pk_bf16_f32 v80, v70, v71\n"
      "v_accvgpr_write_b32 a238, v80\n"
      "s_waitcnt lgkmcnt(5)\n"
      "v_mfma_f32_16x16x32_bf16 v[72:75], v[44:47], v[24:27], v[72:75]\n"
      "s_add_u32 m0, s26, 0xcc00\n"
      "s_xor_b32 s28, s31, 0xb0000\n"
      "buffer_load_dwordx4 v81, s[76:79], s28 offen lds\n"
      "v_mfma_f32_16x16x32_bf16 v[76:79], v[44:47], v[28:31], v[76:79]\n"
      "ds_read_b128 v[40:43], v58 offset:0\n"
      "ds_read_b64_tr_b16 a[76:77], v55 offset:16384\n"
      "s_waitcnt lgkmcnt(5)\n"
      "v_mfma_f32_16x16x32_bf16 v[64:67], v[32:35], v[0:3], 0\n"
      "s_add_u32 m0, s26, 0x10000\n"
      "s_xor_b32 s28, s31, 0x80000\n"
      "buffer_load_dwordx4 v92, s[68:71], s28 offen lds\n"
      "v_mfma_f32_16x16x32_bf16 v[68:71], v[32:35], v[4:7], 0\n"
      "ds_read_b128 v[44:47], v59 offset:0\n"
      "ds_read_b64_tr_b16 a[100:101], v48 offset:32768\n"
      "s_waitcnt lgkmcnt(5)\n"
      "v_mfma_f32_16x16x32_bf16 v[64:67], v[36:39], v[8:11], v[64:67]\n"
      "s_add_u32 m0, s26, 0x10400\n"
      "s_xor_b32 s28, s31, 0xa0000\n"
      "buffer_load_dwordx4 v97, s[68:71], s28 offen lds\n"
      "v_mfma_f32_16x16x32_bf16 v[68:71], v[36:39], v[12:15], v[68:71]\n"
      "ds_read_b128 v[32:35], v60 offset:0\n"
      "ds_read_b64_tr_b16 a[108:109], v49 offset:32768\n"
      "s_waitcnt lgkmcnt(5)\n"
      "v_mfma_f32_16x16x32_bf16 v[64:67], v[40:43], v[16:19], v[64:67]\n"
      "s_add_u32 m0, s26, 0x10800\n"
      "s_xor_b32 s28, s31, 0x90000\n"
      "buffer_load_dwordx4 v92, s[68:71], s28 offen lds\n"
      "v_mfma_f32_16x16x32_bf16 v[68:71], v[40:43], v[20:23], v[68:71]\n"
      "ds_read_b128 v[36:39], v61 offset:0\n"
      "ds_read_b64_tr_b16 a[132:133], v50 offset:32768\n"
      "v_cvt_pk_bf16_f32 v164, v72, v73\n"
      "v_cvt_pk_bf16_f32 v166, v74, v75\n"
      "v_cvt_pk_bf16_f32 v172, v76, v77\n"
      "v_cvt_pk_bf16_f32 v174, v78, v79\n"
      "s_waitcnt lgkmcnt(5)\n"
      "v_mfma_f32_16x16x32_bf16 v[64:67], v[44:47], v[24:27], v[64:67]\n"
      "s_add_u32 m0, s26, 0x10c00\n"
      "s_xor_b32 s28, s31, 0xb0000\n"
      "buffer_load_dwordx4 v97, s[68:71], s28 offen lds\n"
      "v_mfma_f32_16x16x32_bf16 v[68:71], v[44:47], v[28:31], v[68:71]\n"
      "ds_read_b128 v[40:43], v62 offset:0\n"
      "ds_read_b64_tr_b16 a[140:141], v51 offset:32768\n"
      "s_waitcnt lgkmcnt(5)\n"
      "v_mfma_f32_16x16x32_bf16 v[72:75], v[32:35], v[0:3], 0\n"
      "s_add_u32 m0, s26, 0x14000\n"
      "s_xor_b32 s28, s31, 0x80000\n"
      "buffer_load_dwordx4 v92, s[72:75], s28 offen lds\n"
      "v_mfma_f32_16x16x32_bf16 v[76:79], v[32:35], v[4:7], 0\n"
      "ds_read_b128 v[44:47], v63 offset:0\n"
      "ds_read_b64_tr_b16 a[164:165], v52 offset:32768\n"
      "s_waitcnt lgkmcnt(5)\n"
      "v_mfma_f32_16x16x32_bf16 v[72:75], v[36:39], v[8:11], v[72:75]\n"
      "s_add_u32 m0, s26, 0x14400\n"
      "s_xor_b32 s28, s31, 0xa0000\n"
      "buffer_load_dwordx4 v97, s[72:75], s28 offen lds\n"
      "v_mfma_f32_16x16x32_bf16 v[76:79], v[36:39], v[12:15], v[76:79]\n"
      "ds_read_b64_tr_b16 a[172:173], v53 offset:32768\n"
      "s_waitcnt lgkmcnt(4)\n"
      "v_mfma_f32_16x16x32_bf16 v[72:75], v[40:43], v[16:19], v[72:75]\n"
      "s_add_u32 m0, s26, 0x14800\n"
      "s_xor_b32 s28, s31, 0x90000\n"
      "buffer_load_dwordx4 v92, s[72:75], s28 offen lds\n"
      "v_mfma_f32_16x16x32_bf16 v[76:79], v[40:43], v[20:23], v[76:79]\n"
      "ds_read_b64_tr_b16 a[196:197], v54 offset:32768\n"
      "v_cvt_pk_bf16_f32 v196, v64, v65\n"
      "v_cvt_pk_bf16_f32 v198, v66, v67\n"
      "v_cvt_pk_bf16_f32 v204, v68, v69\n"
      "v_cvt_pk_bf16_f32 v206, v70, v71\n"
      "s_waitcnt lgkmcnt(3)\n"
      "v_mfma_f32_16x16x32_bf16 v[72:75], v[44:47], v[24:27], v[72:75]\n"
      "s_add_u32 m0, s26, 0x14c00\n"
      "s_xor_b32 s28, s31, 0xb0000\n"
      "buffer_load_dwordx4 v97, s[72:75], s28 offen lds\n"
      "v_mfma_f32_16x16x32_bf16 v[76:79], v[44:47], v[28:31], v[76:79]\n"
      "ds_read_b64_tr_b16 a[204:205], v55 offset:32768\n"
      "s_waitcnt vmcnt(16) lgkmcnt(0)\n"
      "s_barrier\n"
      "v_xor_b32 v56, 0x0, v96\n"
      "v_xor_b32 v57, 0x40, v96\n"
      "v_xor_b32 v58, 0x80, v96\n"
      "v_xor_b32 v59, 0xc0, v96\n"
      "ds_read_b128 v[32:35], v56 offset:0\n"
      "ds_read_b128 v[36:39], v57 offset:0\n"
      "ds_read_b128 v[40:43], v58 offset:0\n"
      "v_xor_b32 v60, 0x1000, v96\n"
      "v_xor_b32 v61, 0x1040, v96\n"
      "v_xor_b32 v62, 0x1080, v96\n"
      "v_xor_b32 v63, 0x10c0, v96\n"
      "v_cvt_pk_bf16_f32 v228, v72, v73\n"
      "v_cvt_pk_bf16_f32 v230, v74, v75\n"
      "v_cvt_pk_bf16_f32 v236, v76, v77\n"
      "v_cvt_pk_bf16_f32 v238, v78, v79\n"
      "v_xor_b32 v48, 0x0, v95\n"
      "v_xor_b32 v49, 0x20, v95\n"
      "v_xor_b32 v50, 0x1000, v95\n"
      "v_xor_b32 v51, 0x1020, v95\n"
      "v_xor_b32 v52, 0x2000, v95\n"
      "v_xor_b32 v53, 0x2020, v95\n"
      "v_xor_b32 v54, 0x3000, v95\n"
      "v_xor_b32 v55, 0x3020, v95\n"
      "s_waitcnt lgkmcnt(2)\n"
      "v_mfma_f32_16x16x32_bf16 v[64:67], v[32:35], v[0:3], 0\n"
      "s_add_u32 s28, s12, 0x210000\n"
      "buffer_store_dwordx2 v[84:85], v86, s[8:11], s28 offen sc1\n"
      "v_mfma_f32_16x16x32_bf16 v[68:71], v[32:35], v[4:7], 0\n"
      "ds_read_b128 v[44:47], v59 offset:0\n"
      "ds_read_b64_tr_b16 v[134:135], v48 offset:16384\n"
      "s_waitcnt lgkmcnt(3)\n"
      "v_mfma_f32_16x16x32_bf16 v[64:67], v[36:39], v[8:11], v[64:67]\n"
      "s_add_u32 s28, s12, 0x220000\n"
      "buffer_store_dwordx2 v[84:85], v86, s[8:11], s28 offen sc1\n"
      "v_mfma_f32_16x16x32_bf16 v[68:71], v[36:39], v[12:15], v[68:71]\n"
      "v_xor_b32 v56, 0x2000, v96\n"
      "v_xor_b32 v57, 0x2040, v96\n"
      "v_xor_b32 v58, 0x2080, v96\n"
      "v_xor_b32 v59, 0x20c0, v96\n"
      "ds_read_b128 v[32:35], v60 offset:0\n"
      "ds_read_b64_tr_b16 v[142:143], v49 offset:16384\n"
      "s_waitcnt lgkmcnt(4)\n"
      "v_mfma_f32_16x16x32_bf16 v[64:67], v[40:43], v[16:19], v[64:67]\n"
      "s_add_u32 s28, s12, 0x230000\n"
      "buffer_store_dwordx2 v[84:85], v86, s[8:11], s28 offen sc1\n"
      "v_mfma_f32_16x16x32_bf16 v[68:71], v[40:43], v[20:23], v[68:71]\n"
      "ds_read_b128 v[36:39], v61 offset:0\n"
      "ds_read_b64_tr_b16 a[6:7], v50 offset:16384\n"
      "s_waitcnt lgkmcnt(5)\n"
      "v_mfma_f32_16x16x32_bf16 v[64:67], v[44:47], v[24:27], v[64:67]\n"
      "s_add_u32 s28, s13, 0x3c0000\n"
      "buffer_store_dwordx2 v[84:85], v105, s[8:11], s28 offen sc1\n"
      "v_mfma_f32_16x16x32_bf16 v[68:71], v[44:47], v[28:31], v[68:71]\n"
      "ds_read_b128 v[40:43], v62 offset:0\n"
      "ds_read_b64_tr_b16 a[14:15], v51 offset:16384\n"
      "s_waitcnt lgkmcnt(5)\n"
      "v_mfma_f32_16x16x32_bf16 v[72:75], v[32:35], v[0:3], 0\n"
      "s_add_u32 m0, s26, 0x18000\n"
      "s_xor_b32 s28, s31, 0x88000\n"
      "buffer_load_dwordx4 v93, s[76:79], s28 offen lds\n"
      "v_mfma_f32_16x16x32_bf16 v[76:79], v[32:35], v[4:7], 0\n"
      "ds_read_b128 v[44:47], v63 offset:0\n"
      "ds_read_b64_tr_b16 a[38:39], v52 offset:16384\n"
      "s_waitcnt lgkmcnt(5)\n"
      "v_mfma_f32_16x16x32_bf16 v[72:75], v[36:39], v[8:11], v[72:75]\n"
      "s_add_u32 m0, s26, 0x18400\n"
      "s_xor_b32 s28, s31, 0xa8000\n"
      "buffer_load_dwordx4 v99, s[76:79], s28 offen lds\n"
      "v_mfma_f32_16x16x32_bf16 v[76:79], v[36:39], v[12:15], v[76:79]\n"
      "v_xor_b32 v60, 0x3000, v96\n"
      "v_xor_b32 v61, 0x3040, v96\n"
      "v_xor_b32 v62, 0x3080, v96\n"
      "v_xor_b32 v63, 0x30c0, v96\n"
      "ds_read_b128 v[32:35], v56 offset:0\n"
      "ds_read_b64_tr_b16 a[46:47], v53 offset:16384\n"
      "s_waitcnt lgkmcnt(5)\n"
      "v_mfma_f32_16x16x32_bf16 v[72:75], v[40:43], v[16:19], v[72:75]\n"
      "s_add_u32 m0, s26, 0x18800\n"
      "s_xor_b32 s28, s31, 0x98000\n"
      "buffer_load_dwordx4 v100, s[76:79], s28 offen lds\n"
      "v_mfma_f32_16x16x32_bf16 v[76:79], v[40:43], v[20:23], v[76:79]\n"
      "ds_read_b128 v[36:39], v57 offset:0\n"
      "ds_read_b64_tr_b16 a[70:71], v54 offset:16384\n"
      "v_cvt_pk_bf16_f32 v80, v64, v65\n"
      "v_accvgpr_write_b32 a229, v80\n"
      "v_cvt_pk_bf16_f32 v80, v66, v67\n"
      "v_accvgpr_write_b32 a231, v80\n"
      "v_cvt_pk_bf16_f32 v80, v68, v69\n"
      "v_accvgpr_write_b32 a237, v80\n"
      "v_cvt_pk_bf16_f32 v80, v70, v71\n"
      "v_accvgpr_write_b32 a239, v80\n"
      "s_waitcnt lgkmcnt(5)\n"
      "v_mfma_f32_16x16x32_bf16 v[72:75], v[44:47], v[24:27], v[72:75]\n"
      "s_add_u32 m0, s26, 0x18c00\n"
      "s_xor_b32 s28, s31, 0xb8000\n"
      "buffer_load_dwordx4 v81, s[76:79], s28 offen lds\n"
      "v_mfma_f32_16x16x32_bf16 v[76:79], v[44:47], v[28:31], v[76:79]\n"
      "ds_read_b128 v[40:43], v58 offset:0\n"
      "ds_read_b64_tr_b16 a[78:79], v55 offset:16384\n"
      "s_waitcnt lgkmcnt(5)\n"
      "v_mfma_f32_16x16x32_bf16 v[64:67], v[32:35], v[0:3], 0\n"
      "s_add_u32 m0, s26, 0x1c000\n"
      "s_xor_b32 s28, s31, 0x84000\n"
      "buffer_load_dwordx4 v92, s[68:71], s28 offen lds\n"
      "v_mfma_f32_16x16x32_bf16 v[68:71], v[32:35], v[4:7], 0\n"
      "ds_read_b128 v[44:47], v59 offset:0\n"
      "ds_read_b64_tr_b16 a[102:103], v48 offset:32768\n"
      "s_waitcnt lgkmcnt(5)\n"
      "v_mfma_f32_16x16x32_bf16 v[64:67], v[36:39], v[8:11], v[64:67]\n"
      "s_add_u32 m0, s26, 0x1c400\n"
      "s_xor_b32 s28, s31, 0xa4000\n"
      "buffer_load_dwordx4 v97, s[68:71], s28 offen lds\n"
      "v_mfma_f32_16x16x32_bf16 v[68:71], v[36:39], v[12:15], v[68:71]\n"
      "ds_read_b128 v[32:35], v60 offset:0\n"
      "ds_read_b64_tr_b16 a[110:111], v49 offset:32768\n"
      "s_waitcnt lgkmcnt(5)\n"
      "v_mfma_f32_16x16x32_bf16 v[64:67], v[40:43], v[16:19], v[64:67]\n"
      "s_add_u32 m0, s26, 0x1c800\n"
      "s_xor_b32 s28, s31, 0x94000\n"
      "buffer_load_dwordx4 v92, s[68:71], s28 offen lds\n"
      "v_mfma_f32_16x16x32_bf16 v[68:71], v[40:43], v[20:23], v[68:71]\n"
      "ds_read_b128 v[36:39], v61 offset:0\n"
      "ds_read_b64_tr_b16 a[134:135], v50 offset:32768\n"
      "v_cvt_pk_bf16_f32 v165, v72, v73\n"
      "v_cvt_pk_bf16_f32 v167, v74, v75\n"
      "v_cvt_pk_bf16_f32 v173, v76, v77\n"
      "v_cvt_pk_bf16_f32 v175, v78, v79\n"
      "s_waitcnt lgkmcnt(5)\n"
      "v_mfma_f32_16x16x32_bf16 v[64:67], v[44:47], v[24:27], v[64:67]\n"
      "s_add_u32 m0, s26, 0x1cc00\n"
      "s_xor_b32 s28, s31, 0xb4000\n"
      "buffer_load_dwordx4 v97, s[68:71], s28 offen lds\n"
      "v_mfma_f32_16x16x32_bf16 v[68:71], v[44:47], v[28:31], v[68:71]\n"
      "ds_read_b128 v[40:43], v62 offset:0\n"
      "ds_read_b64_tr_b16 a[142:143], v51 offset:32768\n"
      "s_waitcnt lgkmcnt(5)\n"
      "v_mfma_f32_16x16x32_bf16 v[72:75], v[32:35], v[0:3], 0\n"
      "s_add_u32 m0, s26, 0x20000\n"
      "s_xor_b32 s28, s31, 0x84000\n"
      "buffer_load_dwordx4 v92, s[72:75], s28 offen lds\n"
      "v_mfma_f32_16x16x32_bf16 v[76:79], v[32:35], v[4:7], 0\n"
      "ds_read_b128 v[44:47], v63 offset:0\n"
      "ds_read_b64_tr_b16 a[166:167], v52 offset:32768\n"
      "s_waitcnt lgkmcnt(5)\n"
      "v_mfma_f32_16x16x32_bf16 v[72:75], v[36:39], v[8:11], v[72:75]\n"
      "s_add_u32 m0, s26, 0x20400\n"
      "s_xor_b32 s28, s31, 0xa4000\n"
      "buffer_load_dwordx4 v97, s[72:75], s28 offen lds\n"
      "v_mfma_f32_16x16x32_bf16 v[76:79], v[36:39], v[12:15], v[76:79]\n"
      "ds_read_b64_tr_b16 a[174:175], v53 offset:32768\n"
      "s_waitcnt lgkmcnt(4)\n"
      "v_mfma_f32_16x16x32_bf16 v[72:75], v[40:43], v[16:19], v[72:75]\n"
      "s_add_u32 m0, s26, 0x20800\n"
      "s_xor_b32 s28, s31, 0x94000\n"
      "buffer_load_dwordx4 v92, s[72:75], s28 offen lds\n"
      "v_mfma_f32_16x16x32_bf16 v[76:79], v[40:43], v[20:23], v[76:79]\n"
      "ds_read_b64_tr_b16 a[198:199], v54 offset:32768\n"
      "v_cvt_pk_bf16_f32 v197, v64, v65\n"
      "v_cvt_pk_bf16_f32 v199, v66, v67\n"
      "v_cvt_pk_bf16_f32 v205, v68, v69\n"
      "v_cvt_pk_bf16_f32 v207, v70, v71\n"
      "s_waitcnt lgkmcnt(3)\n"
      "v_mfma_f32_16x16x32_bf16 v[72:75], v[44:47], v[24:27], v[72:75]\n"
      "s_add_u32 m0, s26, 0x20c00\n"
      "s_xor_b32 s28, s31, 0xb4000\n"
      "buffer_load_dwordx4 v97, s[72:75], s28 offen lds\n"
      "v_mfma_f32_16x16x32_bf16 v[76:79], v[44:47], v[28:31], v[76:79]\n"
      "ds_read_b64_tr_b16 a[206:207], v55 offset:32768\n"
      "s_waitcnt vmcnt(16) lgkmcnt(0)\n"
      "s_barrier\n"
      "v_xor_b32 v56, 0x0, v96\n"
      "v_xor_b32 v57, 0x40, v96\n"
      "v_xor_b32 v58, 0x80, v96\n"
      "v_xor_b32 v59, 0xc0, v96\n"
      "ds_read_b128 v[32:35], v56 offset:49152\n"
      "ds_read_b128 v[36:39], v57 offset:49152\n"
      "ds_read_b128 v[40:43], v58 offset:49152\n"
      "v_xor_b32 v60, 0x1000, v96\n"
      "v_xor_b32 v61, 0x1040, v96\n"
      "v_xor_b32 v62, 0x1080, v96\n"
      "v_xor_b32 v63, 0x10c0, v96\n"
      "v_cvt_pk_bf16_f32 v229, v72, v73\n"
      "v_cvt_pk_bf16_f32 v231, v74, v75\n"
      "v_cvt_pk_bf16_f32 v237, v76, v77\n"
      "v_cvt_pk_bf16_f32 v239, v78, v79\n"
      "v_xor_b32 v48, 0x0, v95\n"
      "v_xor_b32 v49, 0x20, v95\n"
      "v_xor_b32 v50, 0x1000, v95\n"
      "v_xor_b32 v51, 0x1020, v95\n"
      "v_xor_b32 v52, 0x2000, v95\n"
      "v_xor_b32 v53, 0x2020, v95\n"
      "v_xor_b32 v54, 0x3000, v95\n"
      "v_xor_b32 v55, 0x3020, v95\n"
      "v_add_u32 v48, 0xc000, v48\n"
      "v_add_u32 v49, 0xc000, v49\n"
      "v_add_u32 v50, 0xc000, v50\n"
      "v_add_u32 v51, 0xc000, v51\n"
      "v_add_u32 v52, 0xc000, v52\n"
      "v_add_u32 v53, 0xc000, v53\n"
      "v_add_u32 v54, 0xc000, v54\n"
      "v_add_u32 v55, 0xc000, v55\n"
      "s_waitcnt lgkmcnt(2)\n"
      "v_mfma_f32_16x16x32_bf16 v[64:67], v[32:35], v[0:3], 0\n"
      "s_add_u32 m0, s26, 0x0\n"
      "s_xor_b32 s28, s31, 0xc0000\n"
      "buffer_load_dwordx4 v93, s[76:79], s28 offen lds\n"
      "v_mfma_f32_16x16x32_bf16 v[68:71], v[32:35], v[4:7], 0\n"
      "ds_read_b128 v[44:47], v59 offset:49152\n"
      "ds_read_b64_tr_b16 v[144:145], v48 offset:16384\n"
      "s_waitcnt lgkmcnt(3)\n"
      "v_mfma_f32_16x16x32_bf16 v[64:67], v[36:39], v[8:11], v[64:67]\n"
      "s_add_u32 m0, s26, 0x400\n"
      "s_xor_b32 s28, s31, 0xe0000\n"
      "buffer_load_dwordx4 v99, s[76:79], s28 offen lds\n"
      "v_mfma_f32_16x16x32_bf16 v[68:71], v[36:39], v[12:15], v[68:71]\n"
      "v_xor_b32 v56, 0x2000, v96\n"
      "v_xor_b32 v57, 0x2040, v96\n"
      "v_xor_b32 v58, 0x2080, v96\n"
      "v_xor_b32 v59, 0x20c0, v96\n"
      "ds_read_b128 v[32:35], v60 offset:49152\n"
      "ds_read_b64_tr_b16 v[152:153], v49 offset:16384\n"
      "s_waitcnt lgkmcnt(4)\n"
      "v_mfma_f32_16x16x32_bf16 v[64:67], v[40:43], v[16:19], v[64:67]\n"
      "s_add_u32 m0, s26, 0x800\n"
      "s_xor_b32 s28, s31, 0xd0000\n"
      "buffer_load_dwordx4 v100, s[76:79], s28 offen lds\n"
      "v_mfma_f32_16x16x32_bf16 v[68:71], v[40:43], v[20:23], v[68:71]\n"
      "ds_read_b128 v[36:39], v61 offset:49152\n"
      "ds_read_b64_tr_b16 a[16:17], v50 offset:16384\n"
      "s_waitcnt lgkmcnt(5)\n"
      "v_mfma_f32_16x16x32_bf16 v[64:67], v[44:47], v[24:27], v[64:67]\n"
      "s_add_u32 m0, s26, 0xc00\n"
      "s_xor_b32 s28, s31, 0xf0000\n"
      "buffer_load_dwordx4 v81, s[76:79], s28 offen lds\n"
      "v_mfma_f32_16x16x32_bf16 v[68:71], v[44:47], v[28:31], v[68:71]\n"
      "ds_read_b128 v[40:43], v62 offset:49152\n"
      "ds_read_b64_tr_b16 a[24:25], v51 offset:16384\n"
      "s_waitcnt lgkmcnt(5)\n"
      "v_mfma_f32_16x16x32_bf16 v[72:75], v[32:35], v[0:3], 0\n"
      "s_add_u32 m0, s26, 0x4000\n"
      "s_xor_b32 s28, s31, 0xc0000\n"
      "buffer_load_dwordx4 v92, s[68:71], s28 offen lds\n"
      "v_mfma_f32_16x16x32_bf16 v[76:79], v[32:35], v[4:7], 0\n"
      "ds_read_b128 v[44:47], v63 offset:49152\n"
      "ds_read_b64_tr_b16 a[48:49], v52 offset:16384\n"
      "s_waitcnt lgkmcnt(5)\n"
      "v_mfma_f32_16x16x32_bf16 v[72:75], v[36:39], v[8:11], v[72:75]\n"
      "s_add_u32 m0, s26, 0x4400\n"
      "s_xor_b32 s28, s31, 0xe0000\n"
      "buffer_load_dwordx4 v97, s[68:71], s28 offen lds\n"
      "v_mfma_f32_16x16x32_bf16 v[76:79], v[36:39], v[12:15], v[76:79]\n"
      "v_xor_b32 v60, 0x3000, v96\n"
      "v_xor_b32 v61, 0x3040, v96\n"
      "v_xor_b32 v62, 0x3080, v96\n"
      "v_xor_b32 v63, 0x30c0, v96\n"
      "ds_read_b128 v[32:35], v56 offset:49152\n"
      "ds_read_b64_tr_b16 a[56:57], v53 offset:16384\n"
      "s_waitcnt lgkmcnt(5)\n"
      "v_mfma_f32_16x16x32_bf16 v[72:75], v[40:43], v[16:19], v[72:75]\n"
      "s_add_u32 m0, s26, 0x4800\n"
      "s_xor_b32 s28, s31, 0xd0000\n"
      "buffer_load_dwordx4 v92, s[68:71], s28 offen lds\n"
      "v_mfma_f32_16x16x32_bf16 v[76:79], v[40:43], v[20:23], v[76:79]\n"
      "ds_read_b128 v[36:39], v57 offset:49152\n"
      "ds_read_b64_tr_b16 a[80:81], v54 offset:16384\n"
      "v_cvt_pk_bf16_f32 v80, v64, v65\n"
      "v_accvgpr_write_b32 a240, v80\n"
      "v_cvt_pk_bf16_f32 v80, v66, v67\n"
      "v_accvgpr_write_b32 a242, v80\n"
      "v_cvt_pk_bf16_f32 v80, v68, v69\n"
      "v_accvgpr_write_b32 a248, v80\n"
      "v_cvt_pk_bf16_f32 v80, v70, v71\n"
      "v_accvgpr_write_b32 a250, v80\n"
      "s_waitcnt lgkmcnt(5)\n"
      "v_mfma_f32_16x16x32_bf16 v[72:75], v[44:47], v[24:27], v[72:75]\n"
      "s_add_u32 m0, s26, 0x4c00\n"
      "s_xor_b32 s28, s31, 0xf0000\n"
      "buffer_load_dwordx4 v97, s[68:71], s28 offen lds\n"
      "v_mfma_f32_16x16x32_bf16 v[76:79], v[44:47], v[28:31], v[76:79]\n"
      "ds_read_b128 v[40:43], v58 offset:49152\n"
      "ds_read_b64_tr_b16 a[88:89], v55 offset:16384\n"
      "s_waitcnt lgkmcnt(5)\n"
      "v_mfma_f32_16x16x32_bf16 v[64:67], v[32:35], v[0:3], 0\n"
      "s_add_u32 m0, s26, 0x8000\n"
      "s_xor_b32 s28, s31, 0xc0000\n"
      "buffer_load_dwordx4 v92, s[72:75], s28 offen lds\n"
      "v_mfma_f32_16x16x32_bf16 v[68:71], v[32:35], v[4:7], 0\n"
      "ds_read_b128 v[44:47], v59 offset:49152\n"
      "ds_read_b64_tr_b16 a[112:113], v48 offset:32768\n"
      "s_waitcnt lgkmcnt(5)\n"
      "v_mfma_f32_16x16x32_bf16 v[64:67], v[36:39], v[8:11], v[64:67]\n"
      "s_add_u32 m0, s26, 0x8400\n"
      "s_xor_b32 s28, s31, 0xe0000\n"
      "buffer_load_dwordx4 v97, s[72:75], s28 offen lds\n"
      "v_mfma_f32_16x16x32_bf16 v[68:71], v[36:39], v[12:15], v[68:71]\n"
      "ds_read_b128 v[32:35], v60 offset:49152\n"
      "ds_read_b64_tr_b16 a[120:121], v49 offset:32768\n"
      "s_waitcnt lgkmcnt(5)\n"
      "v_mfma_f32_16x16x32_bf16 v[64:67], v[40:43], v[16:19], v[64:67]\n"
      "s_add_u32 m0, s26, 0x8800\n"
      "s_xor_b32 s28, s31, 0xd0000\n"
      "buffer_load_dwordx4 v92, s[72:75], s28 offen lds\n"
      "v_mfma_f32_16x16x32_bf16 v[68:71], v[40:43], v[20:23], v[68:71]\n"
      "ds_read_b128 v[36:39], v61 offset:49152\n"
      "ds_read_b64_tr_b16 a[144:145], v50 offset:32768\n"
      "v_cvt_pk_bf16_f32 v176, v72, v73\n"
      "v_cvt_pk_bf16_f32 v178, v74, v75\n"
      "v_cvt_pk_bf16_f32 v184, v76, v77\n"
      "v_cvt_pk_bf16_f32 v186, v78, v79\n"
      "s_waitcnt lgkmcnt(5)\n"
      "v_mfma_f32_16x16x32_bf16 v[64:67], v[44:47], v[24:27], v[64:67]\n"
      "s_add_u32 m0, s26, 0x8c00\n"
      "s_xor_b32 s28, s31, 0xf0000\n"
      "buffer_load_dwordx4 v97, s[72:75], s28 offen lds\n"
      "v_mfma_f32_16x16x32_bf16 v[68:71], v[44:47], v[28:31], v[68:71]\n"
      "ds_read_b128 v[40:43], v62 offset:49152\n"
      "ds_read_b64_tr_b16 a[152:153], v51 offset:32768\n"
      "s_waitcnt lgkmcnt(5)\n"
      "v_mfma_f32_16x16x32_bf16 v[72:75], v[32:35], v[0:3], 0\n"
      "v_mfma_f32_16x16x32_bf16 v[76:79], v[32:35], v[4:7], 0\n"
      "ds_read_b128 v[44:47], v63 offset:49152\n"
      "ds_read_b64_tr_b16 a[176:177], v52 offset:32768\n"
      "s_waitcnt lgkmcnt(5)\n"
      "v_mfma_f32_16x16x32_bf16 v[72:75], v[36:39], v[8:11], v[72:75]\n"
      "v_mfma_f32_16x16x32_bf16 v[76:79], v[36:39], v[12:15], v[76:79]\n"
      "ds_read_b64_tr_b16 a[184:185], v53 offset:32768\n"
      "s_waitcnt lgkmcnt(4)\n"
      "v_mfma_f32_16x16x32_bf16 v[72:75], v[40:43], v[16:19], v[72:75]\n"
      "v_mfma_f32_16x16x32_bf16 v[76:79], v[40:43], v[20:23], v[76:79]\n"
      "ds_read_b64_tr_b16 a[208:209], v54 offset:32768\n"
      "v_cvt_pk_bf16_f32 v208, v64, v65\n"
      "v_cvt_pk_bf16_f32 v210, v66, v67\n"
      "v_cvt_pk_bf16_f32 v216, v68, v69\n"
      "v_cvt_pk_bf16_f32 v218, v70, v71\n"
      "s_waitcnt lgkmcnt(3)\n"
      "v_mfma_f32_16x16x32_bf16 v[72:75], v[44:47], v[24:27], v[72:75]\n"
      "v_mfma_f32_16x16x32_bf16 v[76:79], v[44:47], v[28:31], v[76:79]\n"
      "ds_read_b64_tr_b16 a[216:217], v55 offset:32768\n"
      "s_waitcnt vmcnt(12) lgkmcnt(0)\n"
      "s_barrier\n"
      "v_mov_b32 v84, -2\n"
      "v_mov_b32 v85, s85\n"
      "s_cmp_lg_u32 s27, 2\n"
      "s_cbranch_scc1 LC_nr\n"
      "buffer_store_dwordx2 v[84:85], v120, s[8:11], s21 offen sc1\n"
      "LC_nr:\n"
      "v_xor_b32 v56, 0x0, v96\n"
      "v_xor_b32 v57, 0x40, v96\n"
      "v_xor_b32 v58, 0x80, v96\n"
      "v_xor_b32 v59, 0xc0, v96\n"
      "v_add_u32 v56, 0x18000, v56\n"
      "v_add_u32 v57, 0x18000, v57\n"
      "v_add_u32 v58, 0x18000, v58\n"
      "v_add_u32 v59, 0x18000, v59\n"
      "ds_read_b128 v[32:35], v56 offset:0\n"
      "ds_read_b128 v[36:39], v57 offset:0\n"
      "ds_read_b128 v[40:43], v58 offset:0\n"
      "v_xor_b32 v60, 0x1000, v96\n"
      "v_xor_b32 v61, 0x1040, v96\n"
      "v_xor_b32 v62, 0x1080, v96\n"
      "v_xor_b32 v63, 0x10c0, v96\n"
      "v_add_u32 v60, 0x18000, v60\n"
      "v_add_u32 v61, 0x18000, v61\n"
      "v_add_u32 v62, 0x18000, v62\n"
      "v_add_u32 v63, 0x18000, v63\n"
      "v_cvt_pk_bf16_f32 v240, v72, v73\n"
      "v_cvt_pk_bf16_f32 v242, v74, v75\n"
      "v_cvt_pk_bf16_f32 v248, v76, v77\n"
      "v_cvt_pk_bf16_f32 v250, v78, v79\n"
      "v_xor_b32 v48, 0x0, v95\n"
      "v_xor_b32 v49, 0x20, v95\n"
      "v_xor_b32 v50, 0x1000, v95\n"
      "v_xor_b32 v51, 0x1020, v95\n"
      "v_xor_b32 v52, 0x2000, v95\n"
      "v_xor_b32 v53, 0x2020, v95\n"
      "v_xor_b32 v54, 0x3000, v95\n"
      "v_xor_b32 v55, 0x3020, v95\n"
      "v_add_u32 v48, 0x18000, v48\n"
      "v_add_u32 v49, 0x18000, v49\n"
      "v_add_u32 v50, 0x18000, v50\n"
      "v_add_u32 v51, 0x18000, v51\n"
      "v_add_u32 v52, 0x18000, v52\n"
      "v_add_u32 v53, 0x18000, v53\n"
      "v_add_u32 v54, 0x18000, v54\n"
      "v_add_u32 v55, 0x18000, v55\n"
      "s_waitcnt lgkmcnt(2)\n"
      "v_mfma_f32_16x16x32_bf16 v[64:67], v[32:35], v[0:3], 0\n"
      "s_add_u32 m0, s26, 0xc000\n"
      "s_xor_b32 s28, s31, 0xc8000\n"
      "buffer_load_dwordx4 v93, s[76:79], s28 offen lds\n"
      "v_mfma_f32_16x16x32_bf16 v[68:71], v[32:35], v[4:7], 0\n"
      "ds_read_b128 v[44:47], v59 offset:0\n"
      "ds_read_b64_tr_b16 v[146:147], v48 offset:16384\n"
      "s_waitcnt lgkmcnt(3)\n"
      "v_mfma_f32_16x16x32_bf16 v[64:67], v[36:39], v[8:11], v[64:67]\n"
      "s_add_u32 m0, s26, 0xc400\n"
      "s_xor_b32 s28, s31, 0xe8000\n"
      "buffer_load_dwordx4 v99, s[76:79], s28 offen lds\n"
      "v_mfma_f32_16x16x32_bf16 v[68:71], v[36:39], v[12:15], v[68:71]\n"
      "v_xor_b32 v56, 0x2000, v96\n"
      "v_xor_b32 v57, 0x2040, v96\n"
      "v_xor_b32 v58, 0x2080, v96\n"
      "v_xor_b32 v59, 0x20c0, v96\n"
      "v_add_u32 v56, 0x18000, v56\n"
      "v_add_u32 v57, 0x18000, v57\n"
      "v_add_u32 v58, 0x18000, v58\n"
      "v_add_u32 v59, 0x18000, v59\n"
      "ds_read_b128 v[32:35], v60 offset:0\n"
      "ds_read_b64_tr_b16 v[154:155], v49 offset:16384\n"
      "s_waitcnt lgkmcnt(4)\n"
      "v_mfma_f32_16x16x32_bf16 v[64:67], v[40:43], v[16:19], v[64:67]\n"
      "s_add_u32 m0, s26, 0xc800\n"
      "s_xor_b32 s28, s31, 0xd8000\n"
      "buffer_load_dwordx4 v100, s[76:79], s28 offen lds\n"
      "v_mfma_f32_16x16x32_bf16 v[68:71], v[40:43], v[20:23], v[68:71]\n"
      "ds_read_b128 v[36:39], v61 offset:0\n"
      "ds_read_b64_tr_b16 a[18:19], v50 offset:16384\n"
      "s_waitcnt lgkmcnt(5)\n"
      "v_mfma_f32_16x16x32_bf16 v[64:67], v[44:47], v[24:27], v[64:67]\n"
      "s_add_u32 m0, s26, 0xcc00\n"
      "s_xor_b32 s28, s31, 0xf8000\n"
      "buffer_load_dwordx4 v81, s[76:79], s28 offen lds\n"
      "v_mfma_f32_16x16x32_bf16 v[68:71], v[44:47], v[28:31], v[68:71]\n"
      "ds_read_b128 v[40:43], v62 offset:0\n"
      "ds_read_b64_tr_b16 a[26:27], v51 offset:16384\n"
      "s_waitcnt lgkmcnt(5)\n"
      "v_mfma_f32_16x16x32_bf16 v[72:75], v[32:35], v[0:3], 0\n"
      "s_add_u32 m0, s26, 0x10000\n"
      "s_xor_b32 s28, s31, 0xc4000\n"
      "buffer_load_dwordx4 v92, s[68:71], s28 offen lds\n"
      "v_mfma_f32_16x16x32_bf16 v[76:79], v[32:35], v[4:7], 0\n"
      "ds_read_b128 v[44:47], v63 offset:0\n"
      "ds_read_b64_tr_b16 a[50:51], v52 offset:16384\n"
      "s_waitcnt lgkmcnt(5)\n"
      "v_mfma_f32_16x16x32_bf16 v[72:75], v[36:39], v[8:11], v[72:75]\n"
      "s_add_u32 m0, s26, 0x10400\n"
      "s_xor_b32 s28, s31, 0xe4000\n"
      "buffer_load_dwordx4 v97, s[68:71], s28 offen lds\n"
      "v_mfma_f32_16x16x32_bf16 v[76:79], v[36:39], v[12:15], v[76:79]\n"
      "v_xor_b32 v60, 0x3000, v96\n"
      "v_xor_b32 v61, 0x3040, v96\n"
      "v_xor_b32 v62, 0x3080, v96\n"
      "v_xor_b32 v63, 0x30c0, v96\n"
      "v_add_u32 v60, 0x18000, v60\n"
      "v_add_u32 v61, 0x18000, v61\n"
      "v_add_u32 v62, 0x18000, v62\n"
      "v_add_u32 v63, 0x18000, v63\n"
      "ds_read_b128 v[32:35], v56 offset:0\n"
      "ds_read_b64_tr_b16 a[58:59], v53 offset:16384\n"
      "s_waitcnt lgkmcnt(5)\n"
      "v_mfma_f32_16x16x32_bf16 v[72:75], v[40:43], v[16:19], v[72:75]\n"
      "s_add_u32 m0, s26, 0x10800\n"
      "s_xor_b32 s28, s31, 0xd4000\n"
      "buffer_load_dwordx4 v92, s[68:71], s28 offen lds\n"
      "v_mfma_f32_16x16x32_bf16 v[76:79], v[40:43], v[20:23], v[76:79]\n"
      "ds_read_b128 v[36:39], v57 offset:0\n"
      "ds_read_b64_tr_b16 a[82:83], v54 offset:16384\n"
      "v_cvt_pk_bf16_f32 v80, v64, v65\n"
      "v_accvgpr_write_b32 a241, v80\n"
      "v_cvt_pk_bf16_f32 v80, v66, v67\n"
      "v_accvgpr_write_b32 a243, v80\n"
      "v_cvt_pk_bf16_f32 v80, v68, v69\n"
      "v_accvgpr_write_b32 a249, v80\n"
      "v_cvt_pk_bf16_f32 v80, v70, v71\n"
      "v_accvgpr_write_b32 a251, v80\n"
      "s_waitcnt lgkmcnt(5)\n"
      "v_mfma_f32_16x16x32_bf16 v[72:75], v[44:47], v[24:27], v[72:75]\n"
      "s_add_u32 m0, s26, 0x10c00\n"
      "s_xor_b32 s28, s31, 0xf4000\n"
      "buffer_load_dwordx4 v97, s[68:71], s28 offen lds\n"
      "v_mfma_f32_16x16x32_bf16 v[76:79], v[44:47], v[28:31], v[76:79]\n"
      "ds_read_b128 v[40:43], v58 offset:0\n"
      "ds_read_b64_tr_b16 a[90:91], v55 offset:16384\n"
      "s_waitcnt lgkmcnt(5)\n"
      "v_mfma_f32_16x16x32_bf16 v[64:67], v[32:35], v[0:3], 0\n"
      "s_add_u32 m0, s26, 0x14000\n"
      "s_xor_b32 s28, s31, 0xc4000\n"
      "buffer_load_dwordx4 v92, s[72:75], s28 offen lds\n"
      "v_mfma_f32_16x16x32_bf16 v[68:71], v[32:35], v[4:7], 0\n"
      "ds_read_b128 v[44:47], v59 offset:0\n"
      "ds_read_b64_tr_b16 a[114:115], v48 offset:32768\n"
      "s_waitcnt lgkmcnt(5)\n"
      "v_mfma_f32_16x16x32_bf16 v[64:67], v[36:39], v[8:11], v[64:67]\n"
      "s_add_u32 m0, s26, 0x14400\n"
      "s_xor_b32 s28, s31, 0xe4000\n"
      "buffer_load_dwordx4 v97, s[72:75], s28 offen lds\n"
      "v_mfma_f32_16x16x32_bf16 v[68:71], v[36:39], v[12:15], v[68:71]\n"
      "ds_read_b128 v[32:35], v60 offset:0\n"
      "ds_read_b64_tr_b16 a[122:123], v49 offset:32768\n"
      "s_waitcnt lgkmcnt(5)\n"
      "v_mfma_f32_16x16x32_bf16 v[64:67], v[40:43], v[16:19], v[64:67]\n"
      "s_add_u32 m0, s26, 0x14800\n"
      "s_xor_b32 s28, s31, 0xd4000\n"
      "buffer_load_dwordx4 v92, s[72:75], s28 offen lds\n"
      "v_mfma_f32_16x16x32_bf16 v[68:71], v[40:43], v[20:23], v[68:71]\n"
      "ds_read_b128 v[36:39], v61 offset:0\n"
      "ds_read_b64_tr_b16 a[146:147], v50 offset:32768\n"
      "v_cvt_pk_bf16_f32 v177, v72, v73\n"
      "v_cvt_pk_bf16_f32 v179, v74, v75\n"
      "v_cvt_pk_bf16_f32 v185, v76, v77\n"
      "v_cvt_pk_bf16_f32 v187, v78, v79\n"
      "s_waitcnt lgkmcnt(5)\n"
      "v_mfma_f32_16x16x32_bf16 v[64:67], v[44:47], v[24:27], v[64:67]\n"
      "s_add_u32 m0, s26, 0x14c00\n"
      "s_xor_b32 s28, s31, 0xf4000\n"
      "buffer_load_dwordx4 v97, s[72:75], s28 offen lds\n"
      "v_mfma_f32_16x16x32_bf16 v[68:71], v[44:47], v[28:31], v[68:71]\n"
      "ds_read_b128 v[40:43], v62 offset:0\n"
      "ds_read_b64_tr_b16 a[154:155], v51 offset:32768\n"
      "s_waitcnt lgkmcnt(5)\n"
      "v_mfma_f32_16x16x32_bf16 v[72:75], v[32:35], v[0:3], 0\n"
      "v_mfma_f32_16x16x32_bf16 v[76:79], v[32:35], v[4:7], 0\n"
      "ds_read_b128 v[44:47], v63 offset:0\n"
      "ds_read_b64_tr_b16 a[178:179], v52 offset:32768\n"
      "s_waitcnt lgkmcnt(5)\n"
      "v_mfma_f32_16x16x32_bf16 v[72:75], v[36:39], v[8:11], v[72:75]\n"
      "v_mfma_f32_16x16x32_bf16 v[76:79], v[36:39], v[12:15], v[76:79]\n"
      "ds_read_b64_tr_b16 a[186:187], v53 offset:32768\n"
      "s_waitcnt lgkmcnt(4)\n"
      "v_mfma_f32_16x16x32_bf16 v[72:75], v[40:43], v[16:19], v[72:75]\n"
      "v_mfma_f32_16x16x32_bf16 v[76:79], v[40:43], v[20:23], v[76:79]\n"
      "ds_read_b64_tr_b16 a[210:211], v54 offset:32768\n"
      "v_cvt_pk_bf16_f32 v209, v64, v65\n"
      "v_cvt_pk_bf16_f32 v211, v66, v67\n"
      "v_cvt_pk_bf16_f32 v217, v68, v69\n"
      "v_cvt_pk_bf16_f32 v219, v70, v71\n"
      "s_waitcnt lgkmcnt(3)\n"
      "v_mfma_f32_16x16x32_bf16 v[72:75], v[44:47], v[24:27], v[72:75]\n"
      "v_mfma_f32_16x16x32_bf16 v[76:79], v[44:47], v[28:31], v[76:79]\n"
      "ds_read_b64_tr_b16 a[218:219], v55 offset:32768\n"
      "s_waitcnt vmcnt(12) lgkmcnt(0)\n"
      "s_barrier\n"
      "v_xor_b32 v56, 0x0, v96\n"
      "v_xor_b32 v57, 0x40, v96\n"
      "v_xor_b32 v58, 0x80, v96\n"
      "v_xor_b32 v59, 0xc0, v96\n"
      "ds_read_b128 v[32:35], v56 offset:0\n"
      "ds_read_b128 v[36:39], v57 offset:0\n"
      "ds_read_b128 v[40:43], v58 offset:0\n"
      "v_xor_b32 v60, 0x1000, v96\n"
      "v_xor_b32 v61, 0x1040, v96\n"
      "v_xor_b32 v62, 0x1080, v96\n"
      "v_xor_b32 v63, 0x10c0, v96\n"
      "v_cvt_pk_bf16_f32 v241, v72, v73\n"
      "v_cvt_pk_bf16_f32 v243, v74, v75\n"
      "v_cvt_pk_bf16_f32 v249, v76, v77\n"
      "v_cvt_pk_bf16_f32 v251, v78, v79\n"
      "v_xor_b32 v48, 0x0, v95\n"
      "v_xor_b32 v49, 0x20, v95\n"
      "v_xor_b32 v50, 0x1000, v95\n"
      "v_xor_b32 v51, 0x1020, v95\n"
      "v_xor_b32 v52, 0x2000, v95\n"
      "v_xor_b32 v53, 0x2020, v95\n"
      "v_xor_b32 v54, 0x3000, v95\n"
      "v_xor_b32 v55, 0x3020, v95\n"
      "s_waitcnt lgkmcnt(2)\n"
      "v_mfma_f32_16x16x32_bf16 v[64:67], v[32:35], v[0:3], 0\n"
      "v_mfma_f32_16x16x32_bf16 v[68:71], v[32:35], v[4:7], 0\n"
      "ds_read_b128 v[44:47], v59 offset:0\n"
      "ds_read_b64_tr_b16 v[148:149], v48 offset:16384\n"
      "s_waitcnt lgkmcnt(3)\n"
      "v_mfma_f32_16x16x32_bf16 v[64:67], v[36:39], v[8:11], v[64:67]\n"
      "v_mfma_f32_16x16x32_bf16 v[68:71], v[36:39], v[12:15], v[68:71]\n"
      "v_xor_b32 v56, 0x2000, v96\n"
      "v_xor_b32 v57, 0x2040, v96\n"
      "v_xor_b32 v58, 0x2080, v96\n"
      "v_xor_b32 v59, 0x20c0, v96\n"
      "ds_read_b128 v[32:35], v60 offset:0\n"
      "ds_read_b64_tr_b16 v[156:157], v49 offset:16384\n"
      "s_waitcnt lgkmcnt(4)\n"
      "v_mfma_f32_16x16x32_bf16 v[64:67], v[40:43], v[16:19], v[64:67]\n"
      "v_mfma_f32_16x16x32_bf16 v[68:71], v[40:43], v[20:23], v[68:71]\n"
      "ds_read_b128 v[36:39], v61 offset:0\n"
      "ds_read_b64_tr_b16 a[20:21], v50 offset:16384\n"
      "s_waitcnt lgkmcnt(5)\n"
      "v_mfma_f32_16x16x32_bf16 v[64:67], v[44:47], v[24:27], v[64:67]\n"
      "v_mfma_f32_16x16x32_bf16 v[68:71], v[44:47], v[28:31], v[68:71]\n"
      "ds_read_b128 v[40:43], v62 offset:0\n"
      "ds_read_b64_tr_b16 a[28:29], v51 offset:16384\n"
      "s_waitcnt lgkmcnt(5)\n"
      "v_mfma_f32_16x16x32_bf16 v[72:75], v[32:35], v[0:3], 0\n"
      "v_mfma_f32_16x16x32_bf16 v[76:79], v[32:35], v[4:7], 0\n"
      "ds_read_b128 v[44:47], v63 offset:0\n"
      "ds_read_b64_tr_b16 a[52:53], v52 offset:16384\n"
      "s_waitcnt lgkmcnt(5)\n"
      "v_mfma_f32_16x16x32_bf16 v[72:75], v[36:39], v[8:11], v[72:75]\n"
      "v_mfma_f32_16x16x32_bf16 v[76:79], v[36:39], v[12:15], v[76:79]\n"
      "v_xor_b32 v60, 0x3000, v96\n"
      "v_xor_b32 v61, 0x3040, v96\n"
      "v_xor_b32 v62, 0x3080, v96\n"
      "v_xor_b32 v63, 0x30c0, v96\n"
      "ds_read_b128 v[32:35], v56 offset:0\n"
      "ds_read_b64_tr_b16 a[60:61], v53 offset:16384\n"
      "s_waitcnt lgkmcnt(5)\n"
      "v_mfma_f32_16x16x32_bf16 v[72:75], v[40:43], v[16:19], v[72:75]\n"
      "v_mfma_f32_16x16x32_bf16 v[76:79], v[40:43], v[20:23], v[76:79]\n"
      "ds_read_b128 v[36:39], v57 offset:0\n"
      "ds_read_b64_tr_b16 a[84:85], v54 offset:16384\n"
      "v_cvt_pk_bf16_f32 v80, v64, v65\n"
      "v_accvgpr_write_b32 a244, v80\n"
      "v_cvt_pk_bf16_f32 v80, v66, v67\n"
      "v_accvgpr_write_b32 a246, v80\n"
      "v_cvt_pk_bf16_f32 v80, v68, v69\n"
      "v_accvgpr_write_b32 a252, v80\n"
      "v_cvt_pk_bf16_f32 v80, v70, v71\n"
      "v_accvgpr_write_b32 a254, v80\n"
      "s_waitcnt lgkmcnt(5)\n"
      "v_mfma_f32_16x16x32_bf16 v[72:75], v[44:47], v[24:27], v[72:75]\n"
      "v_mfma_f32_16x16x32_bf16 v[76:79], v[44:47], v[28:31], v[76:79]\n"
      "ds_read_b128 v[40:43], v58 offset:0\n"
      "ds_read_b64_tr_b16 a[92:93], v55 offset:16384\n"
      "s_waitcnt lgkmcnt(5)\n"
      "v_mfma_f32_16x16x32_bf16 v[64:67], v[32:35], v[0:3], 0\n"
      "v_mfma_f32_16x16x32_bf16 v[68:71], v[32:35], v[4:7], 0\n"
      "ds_read_b128 v[44:47], v59 offset:0\n"
      "ds_read_b64_tr_b16 a[116:117], v48 offset:32768\n"
      "s_waitcnt lgkmcnt(5)\n"
      "v_mfma_f32_16x16x32_bf16 v[64:67], v[36:39], v[8:11], v[64:67]\n"
      "v_mfma_f32_16x16x32_bf16 v[68:71], v[36:39], v[12:15], v[68:71]\n"
      "ds_read_b128 v[32:35], v60 offset:0\n"
      "ds_read_b64_tr_b16 a[124:125], v49 offset:32768\n"
      "s_waitcnt lgkmcnt(5)\n"
      "v_mfma_f32_16x16x32_bf16 v[64:67], v[40:43], v[16:19], v[64:67]\n"
      "v_mfma_f32_16x16x32_bf16 v[68:71], v[40:43], v[20:23], v[68:71]\n"
      "ds_read_b128 v[36:39], v61 offset:0\n"
      "ds_read_b64_tr_b16 a[148:149], v50 offset:32768\n"
      "v_cvt_pk_bf16_f32 v180, v72, v73\n"
      "v_cvt_pk_bf16_f32 v182, v74, v75\n"
      "v_cvt_pk_bf16_f32 v188, v76, v77\n"
      "v_cvt_pk_bf16_f32 v190, v78, v79\n"
      "s_waitcnt lgkmcnt(5)\n"
      "v_mfma_f32_16x16x32_bf16 v[64:67], v[44:47], v[24:27], v[64:67]\n"
      "v_mfma_f32_16x16x32_bf16 v[68:71], v[44:47], v[28:31], v[68:71]\n"
      "ds_read_b128 v[40:43], v62 offset:0\n"
      "ds_read_b64_tr_b16 a[156:157], v51 offset:32768\n"
      "s_waitcnt lgkmcnt(5)\n"
      "v_mfma_f32_16x16x32_bf16 v[72:75], v[32:35], v[0:3], 0\n"
      "v_mfma_f32_16x16x32_bf16 v[76:79], v[32:35], v[4:7], 0\n"
      "ds_read_b128 v[44:47], v63 offset:0\n"
      "ds_read_b64_tr_b16 a[180:181], v52 offset:32768\n"
      "s_waitcnt lgkmcnt(5)\n"
      "v_mfma_f32_16x16x32_bf16 v[72:75], v[36:39], v[8:11], v[72:75]\n"
      "v_mfma_f32_16x16x32_bf16 v[76:79], v[36:39], v[12:15], v[76:79]\n"
      "ds_read_b64_tr_b16 a[188:189], v53 offset:32768\n"
      "s_waitcnt lgkmcnt(4)\n"
      "v_mfma_f32_16x16x32_bf16 v[72:75], v[40:43], v[16:19], v[72:75]\n"
      "v_mfma_f32_16x16x32_bf16 v[76:79], v[40:43], v[20:23], v[76:79]\n"
      "ds_read_b64_tr_b16 a[212:213], v54 offset:32768\n"
      "v_cvt_pk_bf16_f32 v212, v64, v65\n"
      "v_cvt_pk_bf16_f32 v214, v66, v67\n"
      "v_cvt_pk_bf16_f32 v220, v68, v69\n"
      "v_cvt_pk_bf16_f32 v222, v70, v71\n"
      "s_waitcnt lgkmcnt(3)\n"
      "v_mfma_f32_16x16x32_bf16 v[72:75], v[44:47], v[24:27], v[72:75]\n"
      "v_mfma_f32_16x16x32_bf16 v[76:79], v[44:47], v[28:31], v[76:79]\n"
      "ds_read_b64_tr_b16 a[220:221], v55 offset:32768\n"
      "s_waitcnt vmcnt(0) lgkmcnt(0)\n"
      "s_barrier\n"
      "v_xor_b32 v56, 0x0, v96\n"
      "v_xor_b32 v57, 0x40, v96\n"
      "v_xor_b32 v58, 0x80, v96\n"
      "v_xor_b32 v59, 0xc0, v96\n"
      "ds_read_b128 v[32:35], v56 offset:49152\n"
      "ds_read_b128 v[36:39], v57 offset:49152\n"
      "ds_read_b128 v[40:43], v58 offset:49152\n"
      "v_xor_b32 v60, 0x1000, v96\n"
      "v_xor_b32 v61, 0x1040, v96\n"
      "v_xor_b32 v62, 0x1080, v96\n"
      "v_xor_b32 v63, 0x10c0, v96\n"
      "v_cvt_pk_bf16_f32 v244, v72, v73\n"
      "v_cvt_pk_bf16_f32 v246, v74, v75\n"
      "v_cvt_pk_bf16_f32 v252, v76, v77\n"
      "v_cvt_pk_bf16_f32 v254, v78, v79\n"
      "v_xor_b32 v48, 0x0, v95\n"
      "v_xor_b32 v49, 0x20, v95\n"
      "v_xor_b32 v50, 0x1000, v95\n"
      "v_xor_b32 v51, 0x1020, v95\n"
      "v_xor_b32 v52, 0x2000, v95\n"
      "v_xor_b32 v53, 0x2020, v95\n"
      "v_xor_b32 v54, 0x3000, v95\n"
      "v_xor_b32 v55, 0x3020, v95\n"
      "v_add_u32 v48, 0xc000, v48\n"
      "v_add_u32 v49, 0xc000, v49\n"
      "v_add_u32 v50, 0xc000, v50\n"
      "v_add_u32 v51, 0xc000, v51\n"
      "v_add_u32 v52, 0xc000, v52\n"
      "v_add_u32 v53, 0xc000, v53\n"
      "v_add_u32 v54, 0xc000, v54\n"
      "v_add_u32 v55, 0xc000, v55\n"
      "s_waitcnt lgkmcnt(2)\n"
      "v_mfma_f32_16x16x32_bf16 v[64:67], v[32:35], v[0:3], 0\n"
      "v_and_b32 v93, 0x1f0, v109\n"
      "v_lshlrev_b32 v93, 3, v93\n"
      "v_add_u32 v93, s26, v93\n"
      "v_add_u32 v93, 14336, v93\n"
      "v_and_b32 v94, 16, v109\n"
      "v_lshlrev_b32 v94, 3, v94\n"
      "v_add_u32 v94, 36864, v94\n"
      "v_mov_b32 v98, 0\n"
      "v_mfma_f32_16x16x32_bf16 v[68:71], v[32:35], v[4:7], 0\n"
      "ds_read_b128 v[44:47], v59 offset:49152\n"
      "ds_read_b64_tr_b16 v[150:151], v48 offset:16384\n"
      "s_waitcnt lgkmcnt(3)\n"
      "v_mfma_f32_16x16x32_bf16 v[64:67], v[36:39], v[8:11], v[64:67]\n"
      "s_getpc_b64 s[28:29]\n"
      "LC_here:\n"
      "v_lshlrev_b32 v99, 3, v102\n"
      "v_min_u32 v99, LP0_init-LC_here, v99\n"
      "global_load_dword v100, v99, s[28:29]\n"
      "v_mfma_f32_16x16x32_bf16 v[68:71], v[36:39], v[12:15], v[68:71]\n"
      "v_xor_b32 v56, 0x2000, v96\n"
      "v_xor_b32 v57, 0x2040, v96\n"
      "v_xor_b32 v58, 0x2080, v96\n"
      "v_xor_b32 v59, 0x20c0, v96\n"
      "ds_read_b128 v[32:35], v60 offset:49152\n"
      "ds_read_b64_tr_b16 v[158:159], v49 offset:16384\n"
      "s_waitcnt lgkmcnt(4)\n"
      "v_mfma_f32_16x16x32_bf16 v[64:67], v[40:43], v[16:19], v[64:67]\n"
      "v_mfma_f32_16x16x32_bf16 v[68:71], v[40:43], v[20:23], v[68:71]\n"
      "ds_read_b128 v[36:39], v61 offset:49152\n"
      "ds_read_b64_tr_b16 a[22:23], v50 offset:16384\n"
      "s_waitcnt lgkmcnt(5)\n"
      "v_mfma_f32_16x16x32_bf16 v[64:67], v[44:47], v[24:27], v[64:67]\n"
      "v_mfma_f32_16x16x32_bf16 v[68:71], v[44:47], v[28:31], v[68:71]\n"
      "ds_read_b128 v[40:43], v62 offset:49152\n"
      "ds_read_b64_tr_b16 a[30:31], v51 offset:16384\n"
      "s_waitcnt lgkmcnt(5)\n"
      "v_mfma_f32_16x16x32_bf16 v[72:75], v[32:35], v[0:3], 0\n"
      "v_mfma_f32_16x16x32_bf16 v[76:79], v[32:35], v[4:7], 0\n"
      "ds_read_b128 v[44:47], v63 offset:49152\n"
      "ds_read_b64_tr_b16 a[54:55], v52 offset:16384\n"
      "s_waitcnt lgkmcnt(5)\n"
      "v_mfma_f32_16x16x32_bf16 v[72:75], v[36:39], v[8:11], v[72:75]\n"
      "v_mfma_f32_16x16x32_bf16 v[76:79], v[36:39], v[12:15], v[76:79]\n"
      "v_xor_b32 v60, 0x3000, v96\n"
      "v_xor_b32 v61, 0x3040, v96\n"
      "v_xor_b32 v62, 0x3080, v96\n"
      "v_xor_b32 v63, 0x30c0, v96\n"
      "ds_read_b128 v[32:35], v56 offset:49152\n"
      "ds_read_b64_tr_b16 a[62:63], v53 offset:16384\n"
      "s_waitcnt lgkmcnt(5)\n"
      "v_mfma_f32_16x16x32_bf16 v[72:75], v[40:43], v[16:19], v[72:75]\n"
      "v_mfma_f32_16x16x32_bf16 v[76:79], v[40:43], v[20:23], v[76:79]\n"
      "ds_read_b128 v[36:39], v57 offset:49152\n"
      "ds_read_b64_tr_b16 a[86:87], v54 offset:16384\n"
      "v_cvt_pk_bf16_f32 v80, v64, v65\n"
      "v_accvgpr_write_b32 a245, v80\n"
      "v_cvt_pk_bf16_f32 v80, v66, v67\n"
      "v_accvgpr_write_b32 a247, v80\n"
      "v_cvt_pk_bf16_f32 v80, v68, v69\n"
      "v_accvgpr_write_b32 a253, v80\n"
      "v_cvt_pk_bf16_f32 v80, v70, v71\n"
      "v_accvgpr_write_b32 a255, v80\n"
      "s_waitcnt lgkmcnt(5)\n"
      "v_mfma_f32_16x16x32_bf16 v[72:75], v[44:47], v[24:27], v[72:75]\n"
      "v_mfma_f32_16x16x32_bf16 v[76:79], v[44:47], v[28:31], v[76:79]\n"
      "ds_read_b128 v[40:43], v58 offset:49152\n"
      "ds_read_b64_tr_b16 a[94:95], v55 offset:16384\n"
      "s_waitcnt lgkmcnt(5)\n"
      "v_mfma_f32_16x16x32_bf16 v[64:67], v[32:35], v[0:3], 0\n"
      "buffer_load_dwordx2 v[84:85], v94, s[8:11], 0 offen sc1\n"
      "v_mfma_f32_16x16x32_bf16 v[68:71], v[32:35], v[4:7], 0\n"
      "ds_read_b128 v[44:47], v59 offset:49152\n"
      "ds_read_b64_tr_b16 a[118:119], v48 offset:32768\n"
      "s_waitcnt lgkmcnt(5)\n"
      "v_mfma_f32_16x16x32_bf16 v[64:67], v[36:39], v[8:11], v[64:67]\n"
      "buffer_load_dwordx2 v[86:87], v98, s[8:11], s20 offen sc1\n"
      "v_mfma_f32_16x16x32_bf16 v[68:71], v[36:39], v[12:15], v[68:71]\n"
      "ds_read_b128 v[32:35], v60 offset:49152\n"
      "ds_read_b64_tr_b16 a[126:127], v49 offset:32768\n"
      "s_waitcnt lgkmcnt(5)\n"
      "v_mfma_f32_16x16x32_bf16 v[64:67], v[40:43], v[16:19], v[64:67]\n"
      "buffer_load_dwordx2 v[82:83], v93, s[8:11], 0 offen sc1\n"
      "v_mfma_f32_16x16x32_bf16 v[68:71], v[40:43], v[20:23], v[68:71]\n"
      "ds_read_b128 v[36:39], v61 offset:49152\n"
      "ds_read_b64_tr_b16 a[150:151], v50 offset:32768\n"
      "v_cvt_pk_bf16_f32 v181, v72, v73\n"
      "v_cvt_pk_bf16_f32 v183, v74, v75\n"
      "v_cvt_pk_bf16_f32 v189, v76, v77\n"
      "v_cvt_pk_bf16_f32 v191, v78, v79\n"
      "s_waitcnt lgkmcnt(5)\n"
      "v_mfma_f32_16x16x32_bf16 v[64:67], v[44:47], v[24:27], v[64:67]\n"
      "v_mfma_f32_16x16x32_bf16 v[68:71], v[44:47], v[28:31], v[68:71]\n"
      "ds_read_b128 v[40:43], v62 offset:49152\n"
      "ds_read_b64_tr_b16 a[158:159], v51 offset:32768\n"
      "s_waitcnt lgkmcnt(5)\n"
      "v_mfma_f32_16x16x32_bf16 v[72:75], v[32:35], v[0:3], 0\n"
      "v_mfma_f32_16x16x32_bf16 v[76:79], v[32:35], v[4:7], 0\n"
      "ds_read_b128 v[44:47], v63 offset:49152\n"
      "ds_read_b64_tr_b16 a[182:183], v52 offset:32768\n"
      "s_waitcnt lgkmcnt(5)\n"
      "v_mfma_f32_16x16x32_bf16 v[72:75], v[36:39], v[8:11], v[72:75]\n"
      "v_mfma_f32_16x16x32_bf16 v[76:79], v[36:39], v[12:15], v[76:79]\n"
      "ds_read_b64_tr_b16 a[190:191], v53 offset:32768\n"
      "s_waitcnt lgkmcnt(4)\n"
      "v_mfma_f32_16x16x32_bf16 v[72:75], v[40:43], v[16:19], v[72:75]\n"
      "v_mfma_f32_16x16x32_bf16 v[76:79], v[40:43], v[20:23], v[76:79]\n"
      "ds_read_b64_tr_b16 a[214:215], v54 offset:32768\n"
      "v_cvt_pk_bf16_f32 v213, v64, v65\n"
      "v_cvt_pk_bf16_f32 v215, v66, v67\n"
      "v_cvt_pk_bf16_f32 v221, v68, v69\n"
      "v_cvt_pk_bf16_f32 v223, v70, v71\n"
      "s_waitcnt lgkmcnt(3)\n"
      "v_mfma_f32_16x16x32_bf16 v[72:75], v[44:47], v[24:27], v[72:75]\n"
      "v_mfma_f32_16x16x32_bf16 v[76:79], v[44:47], v[28:31], v[76:79]\n"
      "ds_read_b64_tr_b16 a[222:223], v55 offset:32768\n"
      "s_nop 5\n"
      "v_cvt_pk_bf16_f32 v245, v72, v73\n"
      "v_cvt_pk_bf16_f32 v247, v74, v75\n"
      "v_cvt_pk_bf16_f32 v253, v76, v77\n"
      "v_cvt_pk_bf16_f32 v255, v78, v79\n"
      "s_waitcnt lgkmcnt(0)\n"
      "s_barrier\n"
      "ds_write_b128 v123, v[128:131] offset:0\n"
      "ds_write_b128 v123, v[132:135] offset:1024\n"
      "ds_write_b128 v123, v[136:139] offset:2048\n"
      "ds_write_b128 v123, v[140:143] offset:3072\n"
      "ds_write_b128 v123, v[144:147] offset:4096\n"
      "ds_write_b128 v123, v[148:151] offset:5120\n"
      "ds_write_b128 v123, v[152:155] offset:6144\n"
      "ds_write_b128 v123, v[156:159] offset:7168\n"
      "s_mov_b32 s64, 0x200000\n"
      "s_waitcnt vmcnt(0)\n"
      "s_branch LC_pc\n"
      "LC_p:\n"
      "buffer_load_dwordx2 v[84:85], v94, s[8:11], 0 offen offset:0 sc1\n"
      "s_sub_u32 s64, s64, 1\n"
      "s_cbranch_scc1 LC_pg\n"
      "s_waitcnt vmcnt(0)\n"
      "LC_pc:\n"
      "v_cmp_ne_u32_e64 s[54:55], s85, v85\n"
      "v_cmp_ge_i32 vcc, -1, v84\n"
      "s_or_b64 vcc, vcc, s[54:55]\n"
      "s_cbranch_vccz LC_pk\n"
      "s_sleep 1\n"
      "s_branch LC_p\n"
      "LC_pg:\n"
      "s_mov_b32 s64, 0\n"
      "s_waitcnt vmcnt(0)\n"
      "LC_pk:\n"
      "v_readlane_b32 s55, v84, 0\n"
      "v_readlane_b32 s28, v84, 1\n"
      "s_and_b32 s55, s55, 1\n"
      "s_lshl_b32 s55, s55, 21\n"
      "s_and_b32 s28, s28, 1\n"
      "s_lshl_b32 s28, s28, 14\n"
      "s_xor_b32 s25, s25, s28\n"
      "s_add_u32 s12, s12, s55\n"
      "s_add_u32 s13, s13, s55\n"
      "s_add_u32 s14, s14, s55\n"
      "s_add_u32 s15, s15, s55\n"
      "s_add_u32 s16, s16, s55\n"
      "s_add_u32 s17, s17, s55\n"
      "s_add_u32 s84, s84, s55\n"
      "s_cmp_eq_u32 s27, 3\n"
      "s_cselect_b32 s28, s55, 0\n"
      "s_add_u32 s21, s21, s28\n"
      "s_add_u32 s18, s18, s55\n"
      "s_branch LC_kc\n"
      "LC_k:\n"
      "buffer_load_dwordx2 v[86:87], v98, s[8:11], s20 offen offset:0 sc1\n"
      "s_sub_u32 s64, s64, 1\n"
      "s_cbranch_scc1 LC_kg\n"
      "s_waitcnt vmcnt(0)\n"
      "LC_kc:\n"
      "v_cmp_ne_u32_e64 s[54:55], s85, v87\n"
      "v_cmp_ge_i32 vcc, 0, v86\n"
      "s_or_b64 vcc, vcc, s[54:55]\n"
      "s_cbranch_vccz LC_kk\n"
      "s_sleep 1\n"
      "s_branch LC_k\n"
      "LC_kg:\n"
      "s_mov_b32 s64, 0\n"
      "s_waitcnt vmcnt(0)\n"
      "LC_kk:\n"
      "s_add_u32 s29, s84, 4096\n"
      "buffer_load_dwordx4 v[0:3], v102, s[8:11], s84 offen sc1\n"
      "buffer_load_dwordx4 v[4:7], v102, s[8:11], s29 offen sc1\n"
      "s_branch LC_rc\n"
      "LC_r:\n"
      "buffer_load_dwordx2 v[82:83], v93, s[8:11], 0 offen offset:0 sc1\n"
      "s_sub_u32 s64, s64, 1\n"
      "s_cbranch_scc1 LC_rg\n"
      "s_waitcnt vmcnt(0)\n"
      "LC_rc:\n"
      "v_cmp_ne_u32_e64 s[54:55], s85, v83\n"
      "v_cmp_ge_i32 vcc, -16, v82\n"
      "s_or_b64 vcc, vcc, s[54:55]\n"
      "s_cbranch_vccz LC_rk\n"
      "s_sleep 1\n"
      "s_branch LC_r\n"
      "LC_rg:\n"
      "s_mov_b32 s64, 0\n"
      "s_waitcnt vmcnt(0)\n"
      "LC_rk:\n"
      "s_mov_b32 s28, 0\n"
      "s_mov_b32 s29, 0\n"
      "s_mov_b32 s30, 0x100000\n"
      "s_lshl_b32 s30, s27, 11\n"
      "s_cmp_eq_u32 s27, 1\n"
      "s_cselect_b32 s30, 3072, s30\n"
      "s_add_u32 s84, s84, s30\n"
      "s_add_u32 s84, s84, 0x10000\n"
      "v_readfirstlane_b32 s26, v101\n"
      "s_mov_b32 s65, 0x3e0293ee\n"
      "s_mov_b32 s38, 0xff00ff00\n"
      "s_mov_b32 s39, 0xff00ff00\n"
      "s_xor_b32 s78, s25, 1024\n"
      "s_add_u32 s62, s12, s78\n"
      "s_add_u32 s31, s12, s25\n"
      "s_mov_b32 s63, s14\n"
      "s_add_u32 s79, s12, 0x30000\n"
      "s_mov_b32 s60, s12\n"
      "v_mov_b32 v104, 0x3f803f80\n"
      "v_mov_b32 v105, 0x3f803f80\n"
      "v_mov_b32 v106, 0x3f803f80\n"
      "v_mov_b32 v107, 0x3f803f80\n"
      "v_mov_b32 v126, -1\n"
      "v_mov_b32 v127, -1\n"
      "s_waitcnt vmcnt(0)\n"
      "ds_write_b128 v102, v[0:3] offset:49152\n"
      "ds_write_b128 v102, v[4:7] offset:53248\n"
      "s_cmp_eq_u32 s27, 0\n"
      "s_cbranch_scc1 L0_init\n"
      "s_cmp_eq_u32 s27, 1\n"
      "s_cbranch_scc1 L1_init\n"
      "s_cmp_eq_u32 s27, 2\n"
      "s_cbranch_scc1 L2_init\n"
      "s_branch L3_init\n"
      "L0_init:\n"
      "s_mov_b32 s40, s79\n"
      "s_add_u32 s79, s12, 0x20000\n"
      "s_add_u32 s67, s14, 0xffe00\n"
      "s_mov_b32 s52, s17\n"
      "v_mov_b64 v[32:33], 0\n"
      "v_mov_b64 v[34:35], 0\n"
      "v_mov_b64 v[36:37], 0\n"
      "v_mov_b64 v[38:39], 0\n"
      "v_mov_b64 v[40:41], 0\n"
      "v_mov_b64 v[42:43], 0\n"
      "v_mov_b64 v[44:45], 0\n"
      "v_mov_b64 v[46:47], 0\n"
      "v_mov_b64 v[48:49], 0\n"
      "v_mov_b64 v[50:51], 0\n"
      "v_mov_b64 v[52:53], 0\n"
      "v_mov_b64 v[54:55], 0\n"
      "s_waitcnt vmcnt(0) lgkmcnt(0)\n"
      ".p2align 6\n"
      "L0_loop:\n"
      "s_barrier\n"
      "buffer_load_dwordx4 v[0:3], v124, s[8:11], s31 offen sc1\n"
      "buffer_load_dwordx4 v[4:7], v124, s[8:11], s62 offen sc1\n"
      "s_nop 15\n"
      "buffer_load_dwordx4 v[80:83], v109, s[8:11], s15 offen sc1\n"
      "buffer_load_dwordx2 v[84:85], v109, s[8:11], s15 offen offset:1032 sc1\n"
      "buffer_load_dwordx2 v[86:87], v109, s[8:11], s15 offen offset:1024 sc1\n"
      "s_nop 15\n"
      "buffer_load_dwordx4 v[92:95], v109, s[8:11], s84 offen offset:0 sc1\n"
      "buffer_load_dwordx4 v[96:99], v109, s[8:11], s84 offen offset:1024 sc1\n"
      "buffer_load_dwordx4 v[100:103], v109, s[8:11], s84 offen offset:2048 sc1\n"
      "buffer_store_dwordx2 v[126:127], v120, s[8:11], s79 offen sc1\n"
      "v_mov_b32 v64, v70\n"
      "v_alignbit_b32 v65, v71, v71, 16\n"
      "v_mov_b32 v66, -1\n"
      "v_mov_b32 v67, -1\n"
      "v_mov_b32 v68, s52\n"
      "v_mov_b32 v69, s67\n"
      "v_permlane32_swap_b32 v64, v66\n"
      "v_permlane32_swap_b32 v65, v67\n"
      "v_mov_b32_dpp v68, v69 quad_perm:[0,1,2,3] row_mask:0xc bank_mask:0xf\n"
      "v_add_u32 v68, v68, v109\n"
      "s_waitcnt vmcnt(8)\n"
      "L0_wrA:\n"
      "ds_write_b128 v112, v[0:3]\n"
      "v_smfmac_f32_16x16x64_bf16 v[32:35], v[0:3], v[128:135], v108\n"
      "v_max3_u32 v56, v0, v1, v2\n"
      "v_max_u32 v56, v56, v3\n"
      "v_smfmac_f32_16x16x64_bf16 v[36:39], v[0:3], v[136:143], v108\n"
      "s_waitcnt vmcnt(7)\n"
      "ds_write_b128 v112, v[4:7] offset:1024\n"
      "v_smfmac_f32_16x16x64_bf16 v[40:43], v[0:3], a[96:103], v108\n"
      "v_max3_u32 v56, v56, v4, v5\n"
      "v_max3_u32 v56, v56, v6, v7\n"
      "v_smfmac_f32_16x16x64_bf16 v[44:47], v[0:3], a[104:111], v108\n"
      "v_cmp_eq_u32 vcc, -1, v56\n"
      "v_smfmac_f32_16x16x64_bf16 v[32:35], v[4:7], v[144:151], v108\n"
      "s_nop 3\n"
      "s_cbranch_vccnz L0_retryA\n"
      "L0_okA:\n"
      "s_waitcnt lgkmcnt(0)\n"
      "s_barrier\n"
      "ds_read_b128 v[8:11], v113 offset:0\n"
      "ds_read_b128 v[12:15], v113 offset:1024\n"
      "v_smfmac_f32_16x16x64_bf16 v[36:39], v[4:7], v[152:159], v108\n"
      "ds_read_b128 v[16:19], v114 offset:0\n"
      "ds_read_b128 v[20:23], v114 offset:1024\n"
      "v_smfmac_f32_16x16x64_bf16 v[40:43], v[4:7], a[112:119], v108\n"
      "ds_read_b128 v[24:27], v115 offset:0\n"
      "ds_read_b128 v[28:31], v115 offset:1024\n"
      "v_smfmac_f32_16x16x64_bf16 v[44:47], v[4:7], a[120:127], v108\n"
      "v_smfmac_f32_16x16x64_bf16 v[48:51], v[0:3], a[224:231], v108\n"
      "buffer_store_dwordx4 v[64:67], v68, s[8:11], 0 offen sc1\n"
      "v_smfmac_f32_16x16x64_bf16 v[52:55], v[0:3], a[232:239], v108\n"
      "v_smfmac_f32_16x16x64_bf16 v[48:51], v[4:7], a[240:247], v108\n"
      "v_smfmac_f32_16x16x64_bf16 v[52:55], v[4:7], a[248:255], v108\n"
      "s_waitcnt lgkmcnt(5)\n"
      "v_smfmac_f32_16x16x64_bf16 v[32:35], v[8:11], a[0:7], v108\n"
      "v_smfmac_f32_16x16x64_bf16 v[36:39], v[8:11], a[8:15], v108\n"
      "ds_read_b128 v[128:131], v109 offset:49152\n"
      "v_xor_b32 v76, 16, v116\n"
      "s_waitcnt lgkmcnt(5)\n"
      "v_smfmac_f32_16x16x64_bf16 v[32:35], v[12:15], a[16:23], v108\n"
      "ds_read_b128 v[132:135], v109 offset:50176\n"
      "s_waitcnt lgkmcnt(5)\n"
      "v_smfmac_f32_16x16x64_bf16 v[32:35], v[16:19], a[32:39], v108\n"
      "ds_read_b128 v[136:139], v109 offset:51200\n"
      "s_waitcnt lgkmcnt(5)\n"
      "v_smfmac_f32_16x16x64_bf16 v[32:35], v[20:23], a[48:55], v108\n"
      "ds_read_b128 v[140:143], v109 offset:52224\n"
      "s_waitcnt lgkmcnt(5)\n"
      "v_smfmac_f32_16x16x64_bf16 v[32:35], v[24:27], a[64:71], v108\n"
      "ds_read_b128 v[144:147], v109 offset:53248\n"
      "s_waitcnt lgkmcnt(5)\n"
      "v_smfmac_f32_16x16x64_bf16 v[32:35], v[28:31], a[80:87], v108\n"
      "ds_read_b128 v[148:151], v109 offset:54272\n"
      "s_nop 0\n"
      "v_smfmac_f32_16x16x64_bf16 v[36:39], v[12:15], a[24:31], v108\n"
      "ds_read_b128 v[152:155], v109 offset:55296\n"
      "s_nop 1\n"
      "v_smfmac_f32_16x16x64_bf16 v[36:39], v[16:19], a[40:47], v108\n"
      "ds_read_b128 v[156:159], v109 offset:56320\n"
      "v_add_f32 v32, v32, v34\n"
      "v_smfmac_f32_16x16x64_bf16 v[36:39], v[20:23], a[56:63], v108\n"
      "ds_read_b128 v[72:75], v109 offset:57344\n"
      "v_add_f32 v33, v33, v35\n"
      "v_smfmac_f32_16x16x64_bf16 v[36:39], v[24:27], a[72:79], v108\n"
      "v_mul_f32 v32, s65, v32\n"
      "v_smfmac_f32_16x16x64_bf16 v[36:39], v[28:31], a[88:95], v108\n"
      "s_nop 1\n"
      "v_mul_f32 v33, s65, v33\n"
      "v_smfmac_f32_16x16x64_bf16 v[40:43], v[8:11], a[128:135], v108\n"
      "s_nop 1\n"
      "v_cvt_pk_bf16_f32 v32, v32, v33\n"
      "v_smfmac_f32_16x16x64_bf16 v[40:43], v[12:15], a[144:151], v108\n"
      "ds_write_b32 v116, v32\n"
      "v_add_f32 v36, v36, v38\n"
      "v_add_f32 v37, v37, v39\n"
      "v_smfmac_f32_16x16x64_bf16 v[40:43], v[16:19], a[160:167], v108\n"
      "v_mul_f32 v36, s65, v36\n"
      "v_mul_f32 v37, s65, v37\n"
      "v_smfmac_f32_16x16x64_bf16 v[40:43], v[20:23], a[176:183], v108\n"
      "v_cvt_pk_bf16_f32 v33, v36, v37\n"
      "v_smfmac_f32_16x16x64_bf16 v[40:43], v[24:27], a[192:199], v108\n"
      "ds_write_b32 v76, v33\n"
      "v_xor_b32 v77, 16, v118\n"
      "v_smfmac_f32_16x16x64_bf16 v[40:43], v[28:31], a[208:215], v108\n"
      "s_nop 2\n"
      "v_smfmac_f32_16x16x64_bf16 v[44:47], v[8:11], a[136:143], v108\n"
      "s_nop 2\n"
      "v_smfmac_f32_16x16x64_bf16 v[44:47], v[12:15], a[152:159], v108\n"
      "v_add_f32 v40, v40, v42\n"
      "v_smfmac_f32_16x16x64_bf16 v[44:47], v[16:19], a[168:175], v108\n"
      "s_waitcnt lgkmcnt(0)\n"
      "s_barrier\n"
      "ds_read_b64_tr_b16 v[56:57], v118 offset:40960\n"
      "v_add_f32 v41, v41, v43\n"
      "v_smfmac_f32_16x16x64_bf16 v[44:47], v[20:23], a[184:191], v108\n"
      "ds_read_b64_tr_b16 v[58:59], v118 offset:41472\n"
      "ds_read_b64_tr_b16 v[60:61], v118 offset:41984\n"
      "v_smfmac_f32_16x16x64_bf16 v[44:47], v[24:27], a[200:207], v108\n"
      "ds_read_b64_tr_b16 v[62:63], v118 offset:42496\n"
      "ds_read_b64_tr_b16 v[64:65], v77 offset:40960\n"
      "v_smfmac_f32_16x16x64_bf16 v[44:47], v[28:31], a[216:223], v108\n"
      "ds_read_b64_tr_b16 v[66:67], v77 offset:41472\n"
      "ds_read_b64_tr_b16 v[68:69], v77 offset:41984\n"
      "v_cvt_pk_bf16_f32 v40, v40, v41\n"
      "v_smfmac_f32_16x16x64_bf16 v[48:51], v[8:11], v[160:167], v108\n"
      "ds_read_b64_tr_b16 v[70:71], v77 offset:42496\n"
      "s_nop 2\n"
      "v_add_f32 v44, v44, v46\n"
      "v_add_f32 v45, v45, v47\n"
      "v_cvt_pk_bf16_f32 v41, v44, v45\n"
      "ds_write_b64 v117, v[40:41]\n"
      "v_smfmac_f32_16x16x64_bf16 v[48:51], v[12:15], v[176:183], v108\n"
      "s_waitcnt lgkmcnt(7)\n"
      "v_mfma_f32_16x16x32_bf16 v[32:35], v[128:131], v[56:59], v[72:75]\n"
      "v_mov_b64 v[42:43], 0\n"
      "v_mov_b64 v[46:47], 0\n"
      "v_mfma_f32_16x16x32_bf16 v[36:39], v[152:155], v[56:59], v[72:75]\n"
      "s_waitcnt lgkmcnt(5)\n"
      "v_mov_b64 v[40:41], v[72:73]\n"
      "v_mfma_f32_16x16x32_bf16 v[32:35], v[132:135], v[60:63], v[32:35]\n"
      "v_mfma_f32_16x16x32_bf16 v[36:39], v[156:159], v[60:63], v[36:39]\n"
      "s_waitcnt lgkmcnt(0)\n"
      "s_barrier\n"
      "ds_read_b64_tr_b16 v[72:73], v118 offset:43008\n"
      "ds_read_b64_tr_b16 v[74:75], v118 offset:43520\n"
      "v_mfma_f32_16x16x32_bf16 v[32:35], v[136:139], v[64:67], v[32:35]\n"
      "ds_read_b64_tr_b16 v[76:77], v118 offset:44032\n"
      "ds_read_b64_tr_b16 v[78:79], v118 offset:44544\n"
      "v_mfma_f32_16x16x32_bf16 v[36:39], v[144:147], v[64:67], v[36:39]\n"
      "v_mfma_f32_16x16x32_bf16 v[32:35], v[140:143], v[68:71], v[32:35]\n"
      "v_mfma_f32_16x16x32_bf16 v[36:39], v[148:151], v[68:71], v[36:39]\n"
      "s_nop 2\n"
      "v_smfmac_f32_16x16x64_bf16 v[48:51], v[16:19], v[192:199], v108\n"
      "s_nop 2\n"
      "v_smfmac_f32_16x16x64_bf16 v[48:51], v[20:23], v[208:215], v108\n"
      "v_cndmask_b32_e64 v32, v32, v36, s[38:39]\n"
      "v_smfmac_f32_16x16x64_bf16 v[48:51], v[24:27], v[224:231], v108\n"
      "v_cndmask_b32_e64 v33, v33, v37, s[38:39]\n"
      "s_waitcnt lgkmcnt(2)\n"
      "v_mfma_f32_16x16x32_bf16 v[40:43], v[72:75], v[56:59], v[40:43]\n"
      "s_waitcnt lgkmcnt(0)\n"
      "v_cndmask_b32_e64 v34, v34, v38, s[38:39]\n"
      "v_cndmask_b32_e64 v35, v35, v39, s[38:39]\n"
      "v_mfma_f32_16x16x32_bf16 v[40:43], v[76:79], v[60:63], v[40:43]\n"
      "v_exp_f32 v36, v32\n"
      "v_smfmac_f32_16x16x64_bf16 v[48:51], v[28:31], v[240:247], v108\n"
      "s_nop 1\n"
      "v_exp_f32 v37, v33\n"
      "v_smfmac_f32_16x16x64_bf16 v[52:55], v[8:11], v[168:175], v108\n"
      "v_exp_f32 v38, v34\n"
      "v_smfmac_f32_16x16x64_bf16 v[52:55], v[12:15], v[184:191], v108\n"
      "v_add_f32_dpp v68, v42, v40 row_ror:8 row_mask:0xf bank_mask:0x3\n"
      "v_smfmac_f32_16x16x64_bf16 v[52:55], v[16:19], v[200:207], v108\n"
      "s_memrealtime s[68:69]\n"
      "v_exp_f32 v39, v35\n"
      "v_add_f32_dpp v68, v41, v43 row_ror:8 row_mask:0xf bank_mask:0xc\n"
      "v_smfmac_f32_16x16x64_bf16 v[52:55], v[20:23], v[216:223], v108\n"
      "s_memrealtime s[70:71]\n"
      "v_cvt_pk_bf16_f32 v64, v36, v37\n"
      "v_exp_f32 v69, v68\n"
      "v_smfmac_f32_16x16x64_bf16 v[52:55], v[24:27], v[232:239], v108\n"
      "s_memrealtime s[72:73]\n"
      "v_cvt_pk_bf16_f32 v65, v38, v39\n"
      "v_add_f32 v48, v48, v50\n"
      "v_smfmac_f32_16x16x64_bf16 v[52:55], v[28:31], v[248:255], v108\n"
      "s_memrealtime s[74:75]\n"
      "v_mov_b32_dpp v40, v69 row_ror:8 row_mask:0xf bank_mask:0xf\n"
      "v_mov_b32_dpp v66, v64 row_ror:8 row_mask:0xf bank_mask:0xf\n"
      "v_mov_b32_dpp v67, v65 row_ror:8 row_mask:0xf bank_mask:0xf\n"
      "v_cvt_pk_bf16_f32 v40, v69, v40\n"
      "v_add_f32 v49, v49, v51\n"
      "v_mfma_f32_16x16x32_bf16 v[0:3], v[104:107], v[64:67], 0\n"
      "v_cndmask_b32_e64 v44, v40, 0, s[38:39]\n"
      "v_cndmask_b32_e64 v45, 0, v40, s[38:39]\n"
      "v_add_f32 v52, v52, v54\n"
      "v_add_f32 v53, v53, v55\n"
      "v_mfma_f32_16x16x32_bf16 v[0:3], v[104:107], v[44:47], v[0:3]\n"
      "v_cvt_pk_bf16_f32 v70, v48, v49\n"
      "v_cvt_pk_bf16_f32 v71, v53, v52\n"
      "s_nop 1\n"
      "v_mfma_f32_16x16x32_bf16 v[4:7], v[70:73], v[44:47], 0\n"
      "s_waitcnt vmcnt(5)\n"
      "s_nop 1\n"
      "v_mfma_f32_16x16x32_bf16 v[24:27], v[80:83], v[64:67], v[4:7]\n"
      "s_nop 0\n"
      "v_rcp_f32 v12, v0\n"
      "v_mfma_f32_16x16x32_bf16 v[28:31], v[84:87], v[64:67], v[4:7]\n"
      "s_nop 0\n"
      "v_subrev_u32 v13, 0x21800000, v0\n"
      "v_cmp_le_u32 vcc, 0x3c000000, v13\n"
      "s_nop 0\n"
      "s_nop 0\n"
      "v_mul_f32 v8, v24, v12\n"
      "v_cndmask_b32 v13, v111, v125, vcc\n"
      "v_mul_f32 v9, v25, v12\n"
      "v_mul_f32 v10, v26, v12\n"
      "v_mul_f32 v11, v27, v12\n"
      "v_mul_f32_dpp v8, v28, v12 quad_perm:[0,1,2,3] row_mask:0xf bank_mask:0xc\n"
      "v_mul_f32_dpp v9, v29, v12 quad_perm:[0,1,2,3] row_mask:0xf bank_mask:0xc\n"
      "v_mul_f32_dpp v10, v30, v12 quad_perm:[0,1,2,3] row_mask:0xf bank_mask:0xc\n"
      "v_mul_f32_dpp v11, v31, v12 quad_perm:[0,1,2,3] row_mask:0xf bank_mask:0xc\n"
      "v_cvt_pk_bf16_f32 v14, v8, v9\n"
      "v_cvt_pk_bf16_f32 v15, v10, v11\n"
      "buffer_store_dwordx2 v[14:15], v13, s[8:11], s63 offen sc1\n"
      "s_cbranch_vccnz L0_exact\n"
      "L0_out:\n"
      "s_cmp_eq_u32 s28, 63\n"
      "s_cbranch_scc1 L0_last\n"
      "s_add_u32 s58, s29, s13\n"
      "s_add_u32 s59, s58, 0x10000\n"
      "v_mov_b64 v[32:33], 0\n"
      "v_mov_b64 v[34:35], 0\n"
      "v_mov_b64 v[36:37], 0\n"
      "v_mov_b64 v[38:39], 0\n"
      "v_mov_b64 v[40:41], 0\n"
      "v_mov_b64 v[42:43], 0\n"
      "v_mov_b64 v[44:45], 0\n"
      "v_mov_b64 v[46:47], 0\n"
      "v_mov_b64 v[48:49], 0\n"
      "v_mov_b64 v[50:51], 0\n"
      "v_mov_b64 v[52:53], 0\n"
      "v_mov_b64 v[54:55], 0\n"
      "s_barrier\n"
      "buffer_load_dwordx4 v[16:19], v119, s[8:11], s58 offen sc1\n"
      "buffer_load_dwordx4 v[20:23], v119, s[8:11], s59 offen sc1\n"
      "ds_read_b128 v[128:131], v123 offset:0\n"
      "ds_read_b128 v[132:135], v123 offset:1024\n"
      "ds_read_b128 v[136:139], v123 offset:2048\n"
      "ds_read_b128 v[140:143], v123 offset:3072\n"
      "ds_read_b128 v[144:147], v123 offset:4096\n"
      "ds_read_b128 v[148:151], v123 offset:5120\n"
      "ds_read_b128 v[152:155], v123 offset:6144\n"
      "ds_read_b128 v[156:159], v123 offset:7168\n"
      "s_sub_u32 s67, s63, 512\n"
      "s_mov_b32 s52, s17\n"
      "s_add_u32 s29, s29, 0x80000\n"
      "s_cmp_eq_u32 s29, 0x180000\n"
      "s_cselect_b32 s29, 0, s29\n"
      "s_add_u32 s63, s29, s14\n"
      "s_mov_b32 s79, s40\n"
      "s_mov_b32 s40, s60\n"
      "s_add_u32 s28, s28, 1\n"
      "s_and_b32 s31, s28, 3\n"
      "s_lshl_b32 s31, s31, 16\n"
      "s_add_u32 s31, s31, s12\n"
      "s_mov_b32 s60, s31\n"
      "s_add_u32 s62, s31, s78\n"
      "s_add_u32 s31, s31, s25\n"
      "s_cmp_lt_u32 s28, 63\n"
      "s_cselect_b32 s54, 0x10000, 0\n"
      "s_cmp_eq_u32 s28, 31\n"
      "s_cselect_b32 s55, s24, 0\n"
      "s_add_u32 s84, s84, s54\n"
      "s_sub_u32 s84, s84, s55\n"
      "s_cmp_eq_u32 s28, 32\n"
      "s_cselect_b32 s55, s24, 0\n"
      "s_add_u32 s54, s55, 0x10000\n"
      "s_sub_u32 s55, 0x10000, s55\n"
      "s_add_u32 s15, s15, s54\n"
      "s_add_u32 s17, s17, s55\n"
      "s_waitcnt vmcnt(5) lgkmcnt(2)\n"
      "ds_write_b128 v109, v[92:95] offset:49152\n"
      "ds_write_b128 v109, v[96:99] offset:50176\n"
      "ds_write_b128 v109, v[100:103] offset:51200\n"
      "v_mov_b32 v10, 0\n"
      "s_waitcnt vmcnt(1)\n"
      "L0_redB:\n"
      "v_mfma_f32_16x16x32_bf16 v[0:3], v[88:91], v[16:19], 0\n"
      "v_max3_u32 v4, v16, v17, v18\n"
      "v_max_u32 v4, v4, v19\n"
      "s_waitcnt vmcnt(0)\n"
      "v_mfma_f32_16x16x32_bf16 v[0:3], v[88:91], v[20:23], v[0:3]\n"
      "v_max3_u32 v4, v4, v20, v21\n"
      "v_max3_u32 v4, v4, v22, v23\n"
      "v_cmp_eq_u32 vcc, -1, v4\n"
      "s_nop 3\n"
      "s_cbranch_vccnz L0_retryB\n"
      "L0_okB:\n"
      "v_add_f32_dpp v0, v0, v0 row_ror:8 row_mask:0xf bank_mask:0xf\n"
      "v_add_f32_dpp v1, v1, v1 row_ror:8 row_mask:0xf bank_mask:0xf\n"
      "v_add_f32_dpp v2, v2, v2 row_ror:8 row_mask:0xf bank_mask:0xf\n"
      "v_add_f32_dpp v3, v3, v3 row_ror:8 row_mask:0xf bank_mask:0xf\n"
      "v_add_f32_dpp v0, v0, v0 row_ror:4 row_mask:0xf bank_mask:0xf\n"
      "v_add_f32_dpp v1, v1, v1 row_ror:4 row_mask:0xf bank_mask:0xf\n"
      "v_add_f32_dpp v2, v2, v2 row_ror:4 row_mask:0xf bank_mask:0xf\n"
      "v_add_f32_dpp v3, v3, v3 row_ror:4 row_mask:0xf bank_mask:0xf\n"
      "v_cvt_pk_bf16_f32 v4, v0, v1\n"
      "v_cvt_pk_bf16_f32 v5, v2, v3\n"
      "buffer_store_dwordx2 v[4:5], v120, s[8:11], s60 offen sc1\n"
      "s_memtime s[44:45]\n"
      "s_waitcnt lgkmcnt(0)\n"
      "v_mov_b32 v8, s44\n"
      "ds_write_b32 v10, v8 offset:58368\n"
      "s_waitcnt lgkmcnt(0)\n"
      "s_branch L0_loop\n"
      "L0_retryB:\n"
      "s_sub_u32 s64, s64, 1\n"
      "s_cbranch_scc1 L0_giveB\n"
      "buffer_load_dwordx4 v[16:19], v119, s[8:11], s58 offen sc1\n"
      "buffer_load_dwordx4 v[20:23], v119, s[8:11], s59 offen sc1\n"
      "s_waitcnt vmcnt(0)\n"
      "s_branch L0_redB\n"
      "L0_giveB:\n"
      "s_mov_b32 s64, 0\n"
      "s_nop 7\n"
      "s_branch L0_okB\n"
      "L0_last:\n"
      "s_sub_u32 s67, s63, 512\n"
      "s_mov_b32 s52, s17\n"
      "v_mov_b32 v64, v70\n"
      "v_alignbit_b32 v65, v71, v71, 16\n"
      "v_mov_b32 v66, -1\n"
      "v_mov_b32 v67, -1\n"
      "v_mov_b32 v68, s52\n"
      "v_mov_b32 v69, s67\n"
      "v_permlane32_swap_b32 v64, v66\n"
      "v_permlane32_swap_b32 v65, v67\n"
      "v_mov_b32_dpp v68, v69 quad_perm:[0,1,2,3] row_mask:0xc bank_mask:0xf\n"
      "v_add_u32 v68, v68, v109\n"
      "buffer_store_dwordx4 v[64:67], v68, s[8:11], 0 offen sc1\n"
      "s_waitcnt vmcnt(2)\n"
      "s_barrier\n"
      "s_waitcnt vmcnt(0) lgkmcnt(0)\n"
      "s_barrier\n"
      "s_branch Lend\n"
      "L0_exact:\n"
      "s_nop 7\n"
      "v_max3_f32 v36, v32, v33, v34\n"
      "v_max3_f32 v36, v36, v35, v68\n"
      "s_nop 1\n"
      "v_max_f32_dpp v36, v36, v36 row_ror:8 row_mask:0xf bank_mask:0xf\n"
      "v_mov_b32 v37, v36\n"
      "s_nop 1\n"
      "v_permlane16_swap_b32 v36, v37\n"
      "v_max_f32 v36, v36, v37\n"
      "v_mov_b32 v37, v36\n"
      "s_nop 1\n"
      "v_permlane32_swap_b32 v36, v37\n"
      "v_max_f32 v36, v36, v37\n"
      "v_mov_b32 v41, v36\n"
      "v_sub_f32 v36, v32, v41\n"
      "v_sub_f32 v37, v33, v41\n"
      "v_sub_f32 v38, v34, v41\n"
      "v_sub_f32 v39, v35, v41\n"
      "v_sub_f32 v69, v68, v41\n"
      "v_exp_f32 v36, v36\n"
      "v_exp_f32 v37, v37\n"
      "v_exp_f32 v38, v38\n"
      "v_exp_f32 v39, v39\n"
      "v_exp_f32 v69, v69\n"
      "v_cvt_pk_bf16_f32 v64, v36, v37\n"
      "v_cvt_pk_bf16_f32 v65, v38, v39\n"
      "s_nop 0\n"
      "v_mov_b32_dpp v66, v64 row_ror:8 row_mask:0xf bank_mask:0xf\n"
      "v_mov_b32_dpp v67, v65 row_ror:8 row_mask:0xf bank_mask:0xf\n"
      "v_mov_b32_dpp v40, v69 row_ror:8 row_mask:0xf bank_mask:0xf\n"
      "v_cvt_pk_bf16_f32 v40, v69, v40\n"
      "v_cndmask_b32_e64 v44, v40, 0, s[38:39]\n"
      "v_cndmask_b32_e64 v45, 0, v40, s[38:39]\n"
      "v_mfma_f32_16x16x32_bf16 v[0:3], v[104:107], v[64:67], 0\n"
      "s_nop 0\n"
      "v_mfma_f32_16x16x32_bf16 v[0:3], v[104:107], v[44:47], v[0:3]\n"
      "v_mfma_f32_16x16x32_bf16 v[4:7], v[70:73], v[44:47], 0\n"
      "v_mfma_f32_16x16x32_bf16 v[24:27], v[80:83], v[64:67], v[4:7]\n"
      "v_mfma_f32_16x16x32_bf16 v[28:31], v[84:87], v[64:67], v[4:7]\n"
      "s_nop 4\n"
      "v_rcp_f32 v12, v0\n"
      "v_cndmask_b32 v13, v125, v111, vcc\n"
      "v_mul_f32 v8, v24, v12\n"
      "v_mul_f32 v9, v25, v12\n"
      "v_mul_f32 v10, v26, v12\n"
      "v_mul_f32 v11, v27, v12\n"
      "v_mul_f32_dpp v8, v28, v12 quad_perm:[0,1,2,3] row_mask:0xf bank_mask:0xc\n"
      "v_mul_f32_dpp v9, v29, v12 quad_perm:[0,1,2,3] row_mask:0xf bank_mask:0xc\n"
      "v_mul_f32_dpp v10, v30, v12 quad_perm:[0,1,2,3] row_mask:0xf bank_mask:0xc\n"
      "v_mul_f32_dpp v11, v31, v12 quad_perm:[0,1,2,3] row_mask:0xf bank_mask:0xc\n"
      "v_cvt_pk_bf16_f32 v14, v8, v9\n"
      "v_cvt_pk_bf16_f32 v15, v10, v11\n"
      "buffer_store_dwordx2 v[14:15], v13, s[8:11], s63 offen sc1\n"
      "s_waitcnt vmcnt(1)\n"
      "s_branch L0_out\n"
      "L0_retryA:\n"
      "s_sub_u32 s64, s64, 1\n"
      "s_cbranch_scc1 L0_giveA\n"
      "s_waitcnt lgkmcnt(0)\n"
      "s_nop 7\n"
      "buffer_load_dwordx4 v[0:3], v124, s[8:11], s31 offen sc1\n"
      "buffer_load_dwordx4 v[4:7], v124, s[8:11], s62 offen sc1\n"
      "v_mov_b64 v[32:33], 0\n"
      "v_mov_b64 v[34:35], 0\n"
      "v_mov_b64 v[36:37], 0\n"
      "v_mov_b64 v[38:39], 0\n"
      "v_mov_b64 v[40:41], 0\n"
      "v_mov_b64 v[42:43], 0\n"
      "v_mov_b64 v[44:45], 0\n"
      "v_mov_b64 v[46:47], 0\n"
      "s_waitcnt vmcnt(0)\n"
      "s_branch L0_wrA\n"
      "L0_giveA:\n"
      "s_mov_b32 s64, 0\n"
      "s_branch L0_okA\n"
      "L1_init:\n"
      "v_cmp_le_i32_e64 s[40:41], 0, v103\n"
      "v_cmp_le_i32_e64 s[42:43], 1, v103\n"
      "v_cmp_le_i32_e64 s[44:45], 2, v103\n"
      "v_cmp_le_i32_e64 s[46:47], 3, v103\n"
      "s_add_u32 s67, s14, 0xffe00\n"
      "s_mov_b32 s52, s17\n"
      "s_mov_b32 s79, s18\n"
      "v_mov_b64 v[32:33], 0\n"
      "v_mov_b64 v[34:35], 0\n"
      "v_mov_b64 v[36:37], 0\n"
      "v_mov_b64 v[38:39], 0\n"
      "v_mov_b64 v[40:41], 0\n"
      "v_mov_b64 v[42:43], 0\n"
      "v_mov_b64 v[44:45], 0\n"
      "v_mov_b64 v[46:47], 0\n"
      "v_mov_b64 v[48:49], 0\n"
      "v_mov_b64 v[50:51], 0\n"
      "v_mov_b64 v[52:53], 0\n"
      "v_mov_b64 v[54:55], 0\n"
      "s_waitcnt vmcnt(0) lgkmcnt(0)\n"
      ".p2align 6\n"
      "L1_loop:\n"
      "s_barrier\n"
      "buffer_load_dwordx4 v[0:3], v124, s[8:11], s31 offen sc1\n"
      "buffer_load_dwordx4 v[4:7], v124, s[8:11], s62 offen sc1\n"
      "s_nop 15\n"
      "buffer_load_dwordx4 v[80:83], v109, s[8:11], s15 offen sc1\n"
      "buffer_load_dwordx2 v[84:85], v109, s[8:11], s15 offen offset:1032 sc1\n"
      "buffer_load_dwordx2 v[86:87], v109, s[8:11], s15 offen offset:1024 sc1\n"
      "s_nop 15\n"
      "buffer_load_dwordx4 v[92:95], v109, s[8:11], s84 offen offset:0 sc1\n"
      "buffer_load_dwordx4 v[96:99], v119, s[8:11], s16 offen offset:0 sc1\n"
      "buffer_load_dwordx4 v[100:103], v119, s[8:11], s16 offen offset:1024 sc1\n"
      "buffer_store_dwordx4 v[88:91], v120, s[8:11], s79 offen sc1\n"
      "v_mov_b32 v64, v70\n"
      "v_alignbit_b32 v65, v71, v71, 16\n"
      "v_mov_b32 v66, -1\n"
      "v_mov_b32 v67, -1\n"
      "v_mov_b32 v68, s52\n"
      "v_mov_b32 v69, s67\n"
      "v_permlane32_swap_b32 v64, v66\n"
      "v_permlane32_swap_b32 v65, v67\n"
      "v_mov_b32_dpp v68, v69 quad_perm:[0,1,2,3] row_mask:0xc bank_mask:0xf\n"
      "v_add_u32 v68, v68, v109\n"
      "s_waitcnt vmcnt(8)\n"
      "L1_wrA:\n"
      "ds_write_b128 v112, v[0:3]\n"
      "v_smfmac_f32_16x16x64_bf16 v[32:35], v[0:3], v[128:135], v108\n"
      "v_max3_u32 v56, v0, v1, v2\n"
      "v_max_u32 v56, v56, v3\n"
      "v_smfmac_f32_16x16x64_bf16 v[36:39], v[0:3], v[136:143], v108\n"
      "s_waitcnt vmcnt(7)\n"
      "ds_write_b128 v112, v[4:7] offset:1024\n"
      "v_smfmac_f32_16x16x64_bf16 v[40:43], v[0:3], a[96:103], v108\n"
      "v_max3_u32 v56, v56, v4, v5\n"
      "v_max3_u32 v56, v56, v6, v7\n"
      "v_smfmac_f32_16x16x64_bf16 v[44:47], v[0:3], a[104:111], v108\n"
      "v_cmp_eq_u32 vcc, -1, v56\n"
      "v_smfmac_f32_16x16x64_bf16 v[32:35], v[4:7], v[144:151], v108\n"
      "s_nop 3\n"
      "s_cbranch_vccnz L1_retryA\n"
      "L1_okA:\n"
      "s_waitcnt lgkmcnt(0)\n"
      "s_barrier\n"
      "ds_read_b128 v[8:11], v113 offset:0\n"
      "ds_read_b128 v[12:15], v113 offset:1024\n"
      "v_smfmac_f32_16x16x64_bf16 v[36:39], v[4:7], v[152:159], v108\n"
      "ds_read_b128 v[16:19], v114 offset:0\n"
      "ds_read_b128 v[20:23], v114 offset:1024\n"
      "v_smfmac_f32_16x16x64_bf16 v[40:43], v[4:7], a[112:119], v108\n"
      "ds_read_b128 v[24:27], v115 offset:0\n"
      "ds_read_b128 v[28:31], v115 offset:1024\n"
      "v_smfmac_f32_16x16x64_bf16 v[44:47], v[4:7], a[120:127], v108\n"
      "v_smfmac_f32_16x16x64_bf16 v[48:51], v[0:3], a[224:231], v108\n"
      "v_smfmac_f32_16x16x64_bf16 v[52:55], v[0:3], a[232:239], v108\n"
      "v_smfmac_f32_16x16x64_bf16 v[48:51], v[4:7], a[240:247], v108\n"
      "v_smfmac_f32_16x16x64_bf16 v[52:55], v[4:7], a[248:255], v108\n"
      "s_waitcnt lgkmcnt(5)\n"
      "v_smfmac_f32_16x16x64_bf16 v[32:35], v[8:11], a[0:7], v108\n"
      "buffer_store_dwordx4 v[64:67], v68, s[8:11], 0 offen sc1\n"
      "v_smfmac_f32_16x16x64_bf16 v[36:39], v[8:11], a[8:15], v108\n"
      "ds_read_b128 v[128:131], v109 offset:49152\n"
      "v_xor_b32 v76, 16, v116\n"
      "s_waitcnt lgkmcnt(5)\n"
      "v_smfmac_f32_16x16x64_bf16 v[32:35], v[12:15], a[16:23], v108\n"
      "ds_read_b128 v[132:135], v109 offset:50176\n"
      "s_waitcnt lgkmcnt(5)\n"
      "v_smfmac_f32_16x16x64_bf16 v[32:35], v[16:19], a[32:39], v108\n"
      "ds_read_b128 v[136:139], v109 offset:51200\n"
      "s_waitcnt lgkmcnt(5)\n"
      "v_smfmac_f32_16x16x64_bf16 v[32:35], v[20:23], a[48:55], v108\n"
      "ds_read_b128 v[140:143], v109 offset:52224\n"
      "s_waitcnt lgkmcnt(5)\n"
      "v_smfmac_f32_16x16x64_bf16 v[32:35], v[24:27], a[64:71], v108\n"
      "ds_read_b128 v[144:147], v109 offset:53248\n"
      "s_waitcnt lgkmcnt(5)\n"
      "v_smfmac_f32_16x16x64_bf16 v[32:35], v[28:31], a[80:87], v108\n"
      "ds_read_b128 v[148:151], v109 offset:54272\n"
      "s_nop 0\n"
      "v_smfmac_f32_16x16x64_bf16 v[36:39], v[12:15], a[24:31], v108\n"
      "ds_read_b128 v[152:155], v109 offset:55296\n"
      "s_nop 1\n"
      "v_smfmac_f32_16x16x64_bf16 v[36:39], v[16:19], a[40:47], v108\n"
      "ds_read_b128 v[156:159], v109 offset:56320\n"
      "v_add_f32 v32, v32, v34\n"
      "v_smfmac_f32_16x16x64_bf16 v[36:39], v[20:23], a[56:63], v108\n"
      "ds_read_b128 v[72:75], v109 offset:57344\n"
      "v_add_f32 v33, v33, v35\n"
      "v_smfmac_f32_16x16x64_bf16 v[36:39], v[24:27], a[72:79], v108\n"
      "v_mul_f32 v32, s65, v32\n"
      "v_smfmac_f32_16x16x64_bf16 v[36:39], v[28:31], a[88:95], v108\n"
      "s_nop 1\n"
      "v_mul_f32 v33, s65, v33\n"
      "v_smfmac_f32_16x16x64_bf16 v[40:43], v[8:11], a[128:135], v108\n"
      "s_nop 1\n"
      "v_cvt_pk_bf16_f32 v32, v32, v33\n"
      "v_smfmac_f32_16x16x64_bf16 v[40:43], v[12:15], a[144:151], v108\n"
      "ds_write_b32 v116, v32\n"
      "v_add_f32 v36, v36, v38\n"
      "v_add_f32 v37, v37, v39\n"
      "v_smfmac_f32_16x16x64_bf16 v[40:43], v[16:19], a[160:167], v108\n"
      "v_mul_f32 v36, s65, v36\n"
      "v_mul_f32 v37, s65, v37\n"
      "v_smfmac_f32_16x16x64_bf16 v[40:43], v[20:23], a[176:183], v108\n"
      "v_cvt_pk_bf16_f32 v33, v36, v37\n"
      "v_smfmac_f32_16x16x64_bf16 v[40:43], v[24:27], a[192:199], v108\n"
      "ds_write_b32 v76, v33\n"
      "v_xor_b32 v77, 16, v118\n"
      "v_smfmac_f32_16x16x64_bf16 v[40:43], v[28:31], a[208:215], v108\n"
      "s_nop 2\n"
      "v_smfmac_f32_16x16x64_bf16 v[44:47], v[8:11], a[136:143], v108\n"
      "s_nop 2\n"
      "v_smfmac_f32_16x16x64_bf16 v[44:47], v[12:15], a[152:159], v108\n"
      "v_add_f32 v40, v40, v42\n"
      "v_smfmac_f32_16x16x64_bf16 v[44:47], v[16:19], a[168:175], v108\n"
      "s_waitcnt lgkmcnt(0)\n"
      "s_barrier\n"
      "ds_read_b64_tr_b16 v[56:57], v118 offset:40960\n"
      "v_add_f32 v41, v41, v43\n"
      "v_smfmac_f32_16x16x64_bf16 v[44:47], v[20:23], a[184:191], v108\n"
      "ds_read_b64_tr_b16 v[58:59], v118 offset:41472\n"
      "ds_read_b64_tr_b16 v[60:61], v118 offset:41984\n"
      "v_smfmac_f32_16x16x64_bf16 v[44:47], v[24:27], a[200:207], v108\n"
      "ds_read_b64_tr_b16 v[62:63], v118 offset:42496\n"
      "ds_read_b64_tr_b16 v[64:65], v77 offset:40960\n"
      "v_smfmac_f32_16x16x64_bf16 v[44:47], v[28:31], a[216:223], v108\n"
      "ds_read_b64_tr_b16 v[66:67], v77 offset:41472\n"
      "ds_read_b64_tr_b16 v[68:69], v77 offset:41984\n"
      "v_cvt_pk_bf16_f32 v40, v40, v41\n"
      "v_smfmac_f32_16x16x64_bf16 v[48:51], v[8:11], v[160:167], v108\n"
      "ds_read_b64_tr_b16 v[70:71], v77 offset:42496\n"
      "s_nop 2\n"
      "v_add_f32 v44, v44, v46\n"
      "v_add_f32 v45, v45, v47\n"
      "v_cvt_pk_bf16_f32 v41, v44, v45\n"
      "ds_write_b64 v117, v[40:41]\n"
      "v_smfmac_f32_16x16x64_bf16 v[48:51], v[12:15], v[176:183], v108\n"
      "s_waitcnt lgkmcnt(7)\n"
      "v_mfma_f32_16x16x32_bf16 v[32:35], v[128:131], v[56:59], v[72:75]\n"
      "v_mov_b64 v[42:43], 0\n"
      "v_mov_b64 v[46:47], 0\n"
      "v_mfma_f32_16x16x32_bf16 v[36:39], v[152:155], v[56:59], v[72:75]\n"
      "s_waitcnt lgkmcnt(5)\n"
      "v_mov_b64 v[40:41], v[72:73]\n"
      "v_mfma_f32_16x16x32_bf16 v[32:35], v[132:135], v[60:63], v[32:35]\n"
      "v_mfma_f32_16x16x32_bf16 v[36:39], v[156:159], v[60:63], v[36:39]\n"
      "s_waitcnt lgkmcnt(0)\n"
      "s_barrier\n"
      "ds_read_b64_tr_b16 v[72:73], v118 offset:43008\n"
      "ds_read_b64_tr_b16 v[74:75], v118 offset:43520\n"
      "v_mfma_f32_16x16x32_bf16 v[32:35], v[136:139], v[64:67], v[32:35]\n"
      "ds_read_b64_tr_b16 v[76:77], v118 offset:44032\n"
      "ds_read_b64_tr_b16 v[78:79], v118 offset:44544\n"
      "v_mfma_f32_16x16x32_bf16 v[36:39], v[144:147], v[64:67], v[36:39]\n"
      "v_mfma_f32_16x16x32_bf16 v[32:35], v[140:143], v[68:71], v[32:35]\n"
      "v_mfma_f32_16x16x32_bf16 v[36:39], v[148:151], v[68:71], v[36:39]\n"
      "s_nop 2\n"
      "v_smfmac_f32_16x16x64_bf16 v[48:51], v[16:19], v[192:199], v108\n"
      "s_nop 2\n"
      "v_smfmac_f32_16x16x64_bf16 v[48:51], v[20:23], v[208:215], v108\n"
      "v_cndmask_b32_e64 v32, v32, v36, s[38:39]\n"
      "v_smfmac_f32_16x16x64_bf16 v[48:51], v[24:27], v[224:231], v108\n"
      "v_cndmask_b32_e64 v33, v33, v37, s[38:39]\n"
      "s_waitcnt lgkmcnt(2)\n"
      "v_mfma_f32_16x16x32_bf16 v[40:43], v[72:75], v[56:59], v[40:43]\n"
      "s_waitcnt lgkmcnt(0)\n"
      "v_cndmask_b32_e64 v34, v34, v38, s[38:39]\n"
      "v_cndmask_b32_e64 v35, v35, v39, s[38:39]\n"
      "v_mfma_f32_16x16x32_bf16 v[40:43], v[76:79], v[60:63], v[40:43]\n"
      "v_exp_f32 v36, v32\n"
      "v_smfmac_f32_16x16x64_bf16 v[48:51], v[28:31], v[240:247], v108\n"
      "s_nop 1\n"
      "v_exp_f32 v37, v33\n"
      "v_smfmac_f32_16x16x64_bf16 v[52:55], v[8:11], v[168:175], v108\n"
      "v_exp_f32 v38, v34\n"
      "v_smfmac_f32_16x16x64_bf16 v[52:55], v[12:15], v[184:191], v108\n"
      "v_add_f32_dpp v68, v42, v40 row_ror:8 row_mask:0xf bank_mask:0x3\n"
      "v_smfmac_f32_16x16x64_bf16 v[52:55], v[16:19], v[200:207], v108\n"
      "s_memrealtime s[68:69]\n"
      "v_exp_f32 v39, v35\n"
      "v_add_f32_dpp v68, v41, v43 row_ror:8 row_mask:0xf bank_mask:0xc\n"
      "v_smfmac_f32_16x16x64_bf16 v[52:55], v[20:23], v[216:223], v108\n"
      "s_memrealtime s[70:71]\n"
      "v_cvt_pk_bf16_f32 v64, v36, v37\n"
      "v_exp_f32 v69, v68\n"
      "v_smfmac_f32_16x16x64_bf16 v[52:55], v[24:27], v[232:239], v108\n"
      "s_memrealtime s[72:73]\n"
      "v_cvt_pk_bf16_f32 v65, v38, v39\n"
      "v_add_f32 v48, v48, v50\n"
      "v_smfmac_f32_16x16x64_bf16 v[52:55], v[28:31], v[248:255], v108\n"
      "s_memrealtime s[74:75]\n"
      "v_mov_b32_dpp v40, v69 row_ror:8 row_mask:0xf bank_mask:0xf\n"
      "v_mov_b32_dpp v66, v64 row_ror:8 row_mask:0xf bank_mask:0xf\n"
      "v_mov_b32_dpp v67, v65 row_ror:8 row_mask:0xf bank_mask:0xf\n"
      "v_cvt_pk_bf16_f32 v40, v69, v40\n"
      "v_add_f32 v49, v49, v51\n"
      "v_mfma_f32_16x16x32_bf16 v[0:3], v[104:107], v[64:67], 0\n"
      "v_cndmask_b32_e64 v44, v40, 0, s[38:39]\n"
      "v_cndmask_b32_e64 v45, 0, v40, s[38:39]\n"
      "v_add_f32 v52, v52, v54\n"
      "v_add_f32 v53, v53, v55\n"
      "v_mfma_f32_16x16x32_bf16 v[0:3], v[104:107], v[44:47], v[0:3]\n"
      "v_cvt_pk_bf16_f32 v70, v48, v49\n"
      "v_cvt_pk_bf16_f32 v71, v53, v52\n"
      "s_nop 1\n"
      "v_mfma_f32_16x16x32_bf16 v[4:7], v[70:73], v[44:47], 0\n"
      "s_waitcnt vmcnt(5)\n"
      "s_nop 1\n"
      "v_mfma_f32_16x16x32_bf16 v[24:27], v[80:83], v[64:67], v[4:7]\n"
      "s_nop 0\n"
      "v_rcp_f32 v12, v0\n"
      "v_mfma_f32_16x16x32_bf16 v[28:31], v[84:87], v[64:67], v[4:7]\n"
      "s_nop 0\n"
      "v_subrev_u32 v13, 0x21800000, v0\n"
      "v_cmp_le_u32 vcc, 0x3c000000, v13\n"
      "s_nop 0\n"
      "s_nop 0\n"
      "v_mul_f32 v8, v24, v12\n"
      "v_cndmask_b32 v13, v111, v125, vcc\n"
      "v_mul_f32 v9, v25, v12\n"
      "v_mul_f32 v10, v26, v12\n"
      "v_mul_f32 v11, v27, v12\n"
      "v_mul_f32_dpp v8, v28, v12 quad_perm:[0,1,2,3] row_mask:0xf bank_mask:0xc\n"
      "v_mul_f32_dpp v9, v29, v12 quad_perm:[0,1,2,3] row_mask:0xf bank_mask:0xc\n"
      "v_mul_f32_dpp v10, v30, v12 quad_perm:[0,1,2,3] row_mask:0xf bank_mask:0xc\n"
      "v_mul_f32_dpp v11, v31, v12 quad_perm:[0,1,2,3] row_mask:0xf bank_mask:0xc\n"
      "v_cvt_pk_bf16_f32 v14, v8, v9\n"
      "v_cvt_pk_bf16_f32 v15, v10, v11\n"
      "buffer_store_dwordx2 v[14:15], v13, s[8:11], s63 offen sc1\n"
      "s_cbranch_vccnz L1_exact\n"
      "L1_out:\n"
      "s_waitcnt vmcnt(3)\n"
      "s_nop 1\n"
      "v_mov_b32_dpp v56, v96 row_ror:8 row_mask:0xf bank_mask:0xf\n"
      "v_mov_b32_dpp v57, v97 row_ror:8 row_mask:0xf bank_mask:0xf\n"
      "v_mov_b32_dpp v58, v98 row_ror:8 row_mask:0xf bank_mask:0xf\n"
      "v_mov_b32_dpp v59, v99 row_ror:8 row_mask:0xf bank_mask:0xf\n"
      "v_mov_b32_dpp v60, v100 row_ror:8 row_mask:0xf bank_mask:0xf\n"
      "v_mov_b32_dpp v61, v101 row_ror:8 row_mask:0xf bank_mask:0xf\n"
      "v_mov_b32_dpp v62, v102 row_ror:8 row_mask:0xf bank_mask:0xf\n"
      "v_mov_b32_dpp v63, v103 row_ror:8 row_mask:0xf bank_mask:0xf\n"
      "v_mfma_f32_16x16x32_bf16 v[32:35], v[128:131], v[96:99], 0\n"
      "v_mfma_f32_16x16x32_bf16 v[36:39], v[152:155], v[96:99], 0\n"
      "v_mfma_f32_16x16x32_bf16 v[32:35], v[132:135], v[100:103], v[32:35]\n"
      "v_mfma_f32_16x16x32_bf16 v[36:39], v[156:159], v[100:103], v[36:39]\n"
      "s_cmp_eq_u32 s28, 63\n"
      "s_cbranch_scc1 L1_nw\n"
      "L1_nb:\n"
      "s_barrier\n"
      "v_mfma_f32_16x16x32_bf16 v[32:35], v[136:139], v[56:59], v[32:35]\n"
      "v_mfma_f32_16x16x32_bf16 v[36:39], v[144:147], v[56:59], v[36:39]\n"
      "v_mfma_f32_16x16x32_bf16 v[32:35], v[140:143], v[60:63], v[32:35]\n"
      "v_mfma_f32_16x16x32_bf16 v[36:39], v[148:151], v[60:63], v[36:39]\n"
      "v_mfma_f32_16x16x32_bf16 v[40:43], v[72:75], v[96:99], 0\n"
      "v_mfma_f32_16x16x32_bf16 v[40:43], v[76:79], v[100:103], v[40:43]\n"
      "s_nop 7\n"
      "ds_read_b128 v[128:131], v123 offset:0\n"
      "ds_read_b128 v[132:135], v123 offset:1024\n"
      "ds_read_b128 v[136:139], v123 offset:2048\n"
      "ds_read_b128 v[140:143], v123 offset:3072\n"
      "ds_read_b128 v[144:147], v123 offset:4096\n"
      "ds_read_b128 v[148:151], v123 offset:5120\n"
      "ds_read_b128 v[152:155], v123 offset:6144\n"
      "ds_read_b128 v[156:159], v123 offset:7168\n"
      "ds_write_b128 v109, v[92:95] offset:52224\n"
      "v_mov_b32 v46, 0xd028965f\n"
      "v_cndmask_b32_e64 v32, v32, v36, s[38:39]\n"
      "v_cndmask_b32_e64 v33, v33, v37, s[38:39]\n"
      "v_cndmask_b32_e64 v34, v34, v38, s[38:39]\n"
      "v_cndmask_b32_e64 v35, v35, v39, s[38:39]\n"
      "v_cndmask_b32_e64 v32, v46, v32, s[40:41]\n"
      "v_cndmask_b32_e64 v33, v46, v33, s[42:43]\n"
      "v_cndmask_b32_e64 v34, v46, v34, s[44:45]\n"
      "v_cndmask_b32_e64 v35, v46, v35, s[46:47]\n"
      "v_max3_f32 v36, v32, v33, v34\n"
      "v_add_f32_dpp v38, v42, v40 row_ror:8 row_mask:0xf bank_mask:0x3\n"
      "s_nop 1\n"
      "v_add_f32_dpp v38, v41, v43 row_ror:8 row_mask:0xf bank_mask:0xc\n"
      "v_max3_f32 v36, v36, v35, v38\n"
      "s_nop 1\n"
      "v_max_f32_dpp v36, v36, v36 row_ror:8 row_mask:0xf bank_mask:0xf\n"
      "v_mov_b32 v37, v36\n"
      "s_nop 1\n"
      "v_permlane16_swap_b32 v36, v37\n"
      "v_max_f32 v36, v36, v37\n"
      "v_mov_b32 v37, v36\n"
      "s_nop 1\n"
      "v_permlane32_swap_b32 v36, v37\n"
      "v_max_f32 v36, v36, v37\n"
      "v_mul_f32 v36, s65, v36\n"
      "v_fma_f32 v38, v38, s65, -v36\n"
      "v_fma_f32 v32, v32, s65, -v36\n"
      "v_fma_f32 v33, v33, s65, -v36\n"
      "v_fma_f32 v34, v34, s65, -v36\n"
      "v_fma_f32 v35, v35, s65, -v36\n"
      "v_exp_f32 v38, v38\n"
      "v_exp_f32 v32, v32\n"
      "v_exp_f32 v33, v33\n"
      "v_exp_f32 v34, v34\n"
      "v_exp_f32 v35, v35\n"
      "v_add_f32 v36, v32, v33\n"
      "v_add_f32 v37, v34, v35\n"
      "v_add_f32 v36, v36, v38\n"
      "v_add_f32 v36, v36, v37\n"
      "s_nop 1\n"
      "v_add_f32_dpp v36, v36, v36 row_ror:8 row_mask:0xf bank_mask:0xf\n"
      "v_mov_b32 v37, v36\n"
      "s_nop 1\n"
      "v_permlane16_swap_b32 v36, v37\n"
      "v_add_f32 v36, v36, v37\n"
      "v_mov_b32 v37, v36\n"
      "s_nop 1\n"
      "v_permlane32_swap_b32 v36, v37\n"
      "v_add_f32 v36, v36, v37\n"
      "v_rcp_f32 v36, v36\n"
      "s_nop 0\n"
      "v_mul_f32 v38, v38, v36\n"
      "v_mul_f32 v32, v32, v36\n"
      "v_mul_f32 v33, v33, v36\n"
      "v_mul_f32 v34, v34, v36\n"
      "v_mul_f32 v35, v35, v36\n"
      "v_cvt_pk_bf16_f32 v88, v32, v33\n"
      "v_cvt_pk_bf16_f32 v89, v34, v35\n"
      "v_mov_b32_dpp v39, v38 row_ror:8 row_mask:0xf bank_mask:0xf\n"
      "v_mov_b32_dpp v90, v88 row_ror:8 row_mask:0xf bank_mask:0xf\n"
      "v_mov_b32_dpp v91, v89 row_ror:8 row_mask:0xf bank_mask:0xf\n"
      "v_cvt_pk_bf16_f32 v39, v39, v38\n"
      "s_nop 1\n"
      "v_mov_b32_dpp v88, v39 quad_perm:[0,0,0,0] row_mask:0xf bank_mask:0xc\n"
      "v_mov_b32_dpp v89, v39 quad_perm:[1,1,1,1] row_mask:0xf bank_mask:0xc\n"
      "v_mov_b32_dpp v90, v39 quad_perm:[2,2,2,2] row_mask:0xf bank_mask:0xc\n"
      "v_mov_b32_dpp v91, v39 quad_perm:[3,3,3,3] row_mask:0xf bank_mask:0xc\n"
      "s_cmp_eq_u32 s28, 63\n"
      "s_cbranch_scc1 L1_last\n"
      "v_mov_b64 v[32:33], 0\n"
      "v_mov_b64 v[34:35], 0\n"
      "v_mov_b64 v[36:37], 0\n"
      "v_mov_b64 v[38:39], 0\n"
      "v_mov_b64 v[40:41], 0\n"
      "v_mov_b64 v[42:43], 0\n"
      "v_mov_b64 v[44:45], 0\n"
      "v_mov_b64 v[46:47], 0\n"
      "v_mov_b64 v[48:49], 0\n"
      "v_mov_b64 v[50:51], 0\n"
      "v_mov_b64 v[52:53], 0\n"
      "v_mov_b64 v[54:55], 0\n"
      "s_sub_u32 s67, s63, 512\n"
      "s_mov_b32 s52, s17\n"
      "s_add_u32 s29, s29, 0x80000\n"
      "s_cmp_eq_u32 s29, 0x180000\n"
      "s_cselect_b32 s29, 0, s29\n"
      "s_add_u32 s63, s29, s14\n"
      "s_add_u32 s28, s28, 1\n"
      "s_and_b32 s31, s28, 3\n"
      "s_lshl_b32 s31, s31, 16\n"
      "s_add_u32 s31, s31, s12\n"
      "s_add_u32 s62, s31, s78\n"
      "s_add_u32 s31, s31, s25\n"
      "s_mov_b32 s79, s18\n"
      "s_cmp_lt_u32 s28, 63\n"
      "s_cselect_b32 s54, 0x10000, 0\n"
      "s_cmp_eq_u32 s28, 31\n"
      "s_cselect_b32 s55, s24, 0\n"
      "s_add_u32 s84, s84, s54\n"
      "s_sub_u32 s84, s84, s55\n"
      "s_cmp_eq_u32 s28, 32\n"
      "s_cselect_b32 s55, s24, 0\n"
      "s_add_u32 s54, s55, 0x10000\n"
      "s_sub_u32 s55, 0x10000, s55\n"
      "s_add_u32 s15, s15, s54\n"
      "s_add_u32 s17, s17, s55\n"
      "s_add_u32 s16, s16, s55\n"
      "s_add_u32 s18, s18, s55\n"
      "s_waitcnt vmcnt(1) lgkmcnt(0)\n"
      "s_branch L1_loop\n"
      "L1_nw:\n"
      "s_waitcnt vmcnt(1)\n"
      "s_branch L1_nb\n"
      "L1_last:\n"
      "s_mov_b32 s79, s18\n"
      "buffer_store_dwordx4 v[88:91], v120, s[8:11], s79 offen sc1\n"
      "s_sub_u32 s67, s63, 512\n"
      "s_mov_b32 s52, s17\n"
      "v_mov_b32 v64, v70\n"
      "v_alignbit_b32 v65, v71, v71, 16\n"
      "v_mov_b32 v66, -1\n"
      "v_mov_b32 v67, -1\n"
      "v_mov_b32 v68, s52\n"
      "v_mov_b32 v69, s67\n"
      "v_permlane32_swap_b32 v64, v66\n"
      "v_permlane32_swap_b32 v65, v67\n"
      "v_mov_b32_dpp v68, v69 quad_perm:[0,1,2,3] row_mask:0xc bank_mask:0xf\n"
      "v_add_u32 v68, v68, v109\n"
      "buffer_store_dwordx4 v[64:67], v68, s[8:11], 0 offen sc1\n"
      "s_waitcnt vmcnt(0) lgkmcnt(0)\n"
      "s_barrier\n"
      "s_branch Lend\n"
      "L1_exact:\n"
      "s_nop 7\n"
      "v_max3_f32 v36, v32, v33, v34\n"
      "v_max3_f32 v36, v36, v35, v68\n"
      "s_nop 1\n"
      "v_max_f32_dpp v36, v36, v36 row_ror:8 row_mask:0xf bank_mask:0xf\n"
      "v_mov_b32 v37, v36\n"
      "s_nop 1\n"
      "v_permlane16_swap_b32 v36, v37\n"
      "v_max_f32 v36, v36, v37\n"
      "v_mov_b32 v37, v36\n"
      "s_nop 1\n"
      "v_permlane32_swap_b32 v36, v37\n"
      "v_max_f32 v36, v36, v37\n"
      "v_mov_b32 v41, v36\n"
      "v_sub_f32 v36, v32, v41\n"
      "v_sub_f32 v37, v33, v41\n"
      "v_sub_f32 v38, v34, v41\n"
      "v_sub_f32 v39, v35, v41\n"
      "v_sub_f32 v69, v68, v41\n"
      "v_exp_f32 v36, v36\n"
      "v_exp_f32 v37, v37\n"
      "v_exp_f32 v38, v38\n"
      "v_exp_f32 v39, v39\n"
      "v_exp_f32 v69, v69\n"
      "v_cvt_pk_bf16_f32 v64, v36, v37\n"
      "v_cvt_pk_bf16_f32 v65, v38, v39\n"
      "s_nop 0\n"
      "v_mov_b32_dpp v66, v64 row_ror:8 row_mask:0xf bank_mask:0xf\n"
      "v_mov_b32_dpp v67, v65 row_ror:8 row_mask:0xf bank_mask:0xf\n"
      "v_mov_b32_dpp v40, v69 row_ror:8 row_mask:0xf bank_mask:0xf\n"
      "v_cvt_pk_bf16_f32 v40, v69, v40\n"
      "v_cndmask_b32_e64 v44, v40, 0, s[38:39]\n"
      "v_cndmask_b32_e64 v45, 0, v40, s[38:39]\n"
      "v_mfma_f32_16x16x32_bf16 v[0:3], v[104:107], v[64:67], 0\n"
      "s_nop 0\n"
      "v_mfma_f32_16x16x32_bf16 v[0:3], v[104:107], v[44:47], v[0:3]\n"
      "v_mfma_f32_16x16x32_bf16 v[4:7], v[70:73], v[44:47], 0\n"
      "v_mfma_f32_16x16x32_bf16 v[24:27], v[80:83], v[64:67], v[4:7]\n"
      "v_mfma_f32_16x16x32_bf16 v[28:31], v[84:87], v[64:67], v[4:7]\n"
      "s_nop 4\n"
      "v_rcp_f32 v12, v0\n"
      "v_cndmask_b32 v13, v125, v111, vcc\n"
      "v_mul_f32 v8, v24, v12\n"
      "v_mul_f32 v9, v25, v12\n"
      "v_mul_f32 v10, v26, v12\n"
      "v_mul_f32 v11, v27, v12\n"
      "v_mul_f32_dpp v8, v28, v12 quad_perm:[0,1,2,3] row_mask:0xf bank_mask:0xc\n"
      "v_mul_f32_dpp v9, v29, v12 quad_perm:[0,1,2,3] row_mask:0xf bank_mask:0xc\n"
      "v_mul_f32_dpp v10, v30, v12 quad_perm:[0,1,2,3] row_mask:0xf bank_mask:0xc\n"
      "v_mul_f32_dpp v11, v31, v12 quad_perm:[0,1,2,3] row_mask:0xf bank_mask:0xc\n"
      "v_cvt_pk_bf16_f32 v14, v8, v9\n"
      "v_cvt_pk_bf16_f32 v15, v10, v11\n"
      "buffer_store_dwordx2 v[14:15], v13, s[8:11], s63 offen sc1\n"
      "s_waitcnt vmcnt(1)\n"
      "s_branch L1_out\n"
      "L1_retryA:\n"
      "s_sub_u32 s64, s64, 1\n"
      "s_cbranch_scc1 L1_giveA\n"
      "s_waitcnt lgkmcnt(0)\n"
      "s_nop 7\n"
      "buffer_load_dwordx4 v[0:3], v124, s[8:11], s31 offen sc1\n"
      "buffer_load_dwordx4 v[4:7], v124, s[8:11], s62 offen sc1\n"
      "v_mov_b64 v[32:33], 0\n"
      "v_mov_b64 v[34:35], 0\n"
      "v_mov_b64 v[36:37], 0\n"
      "v_mov_b64 v[38:39], 0\n"
      "v_mov_b64 v[40:41], 0\n"
      "v_mov_b64 v[42:43], 0\n"
      "v_mov_b64 v[44:45], 0\n"
      "v_mov_b64 v[46:47], 0\n"
      "s_waitcnt vmcnt(0)\n"
      "s_branch L1_wrA\n"
      "L1_giveA:\n"
      "s_mov_b32 s64, 0\n"
      "s_branch L1_okA\n"
      "L2_init:\n"
      "s_add_u32 s67, s14, 0xffe00\n"
      "s_mov_b32 s52, s17\n"
      "v_mov_b32 v126, -1\n"
      "v_mov_b32 v127, s85\n"
      "s_mov_b32 s40, 1\n"
      "s_mov_b32 s79, s21\n"
      "v_mov_b64 v[32:33], 0\n"
      "v_mov_b64 v[34:35], 0\n"
      "v_mov_b64 v[36:37], 0\n"
      "v_mov_b64 v[38:39], 0\n"
      "v_mov_b64 v[40:41], 0\n"
      "v_mov_b64 v[42:43], 0\n"
      "v_mov_b64 v[44:45], 0\n"
      "v_mov_b64 v[46:47], 0\n"
      "v_mov_b64 v[48:49], 0\n"
      "v_mov_b64 v[50:51], 0\n"
      "v_mov_b64 v[52:53], 0\n"
      "v_mov_b64 v[54:55], 0\n"
      "ds_write_b128 v109, v[32:35] offset:57344\n"
      "s_waitcnt vmcnt(0) lgkmcnt(0)\n"
      "L2_f0:\n"
      "buffer_load_dwordx2 v[100:101], v119, s[8:11], s20 offen offset:0 sc1\n"
      "s_sub_u32 s64, s64, 1\n"
      "s_cbranch_scc1 L2_f0g\n"
      "s_waitcnt vmcnt(0)\n"
      "L2_f0c:\n"
      "v_lshrrev_b32 v8, 7, v119\n"
      "v_add_u32 v100, v100, v8\n"
      "v_cmp_ne_u32_e64 s[54:55], s85, v101\n"
      "v_cmp_ge_i32 vcc, 1, v100\n"
      "s_or_b64 vcc, vcc, s[54:55]\n"
      "s_cbranch_vccz L2_f0k\n"
      "s_sleep 1\n"
      "s_branch L2_f0\n"
      "L2_f0g:\n"
      "s_mov_b32 s64, 0\n"
      "s_waitcnt vmcnt(0)\n"
      "L2_f0k:\n"
      "s_memtime s[44:45]\n"
      "v_mov_b32 v10, 0\n"
      "s_waitcnt lgkmcnt(0)\n"
      "v_mov_b32 v8, s44\n"
      "ds_write_b32 v10, v8 offset:58368\n"
      "s_waitcnt lgkmcnt(0)\n"
      ".p2align 6\n"
      "L2_loop:\n"
      "s_barrier\n"
      "buffer_load_dwordx4 v[0:3], v124, s[8:11], s31 offen sc1\n"
      "buffer_load_dwordx4 v[4:7], v124, s[8:11], s62 offen sc1\n"
      "s_nop 15\n"
      "buffer_load_dwordx4 v[80:83], v109, s[8:11], s15 offen sc1\n"
      "buffer_load_dwordx2 v[84:85], v109, s[8:11], s15 offen offset:1032 sc1\n"
      "buffer_load_dwordx2 v[86:87], v109, s[8:11], s15 offen offset:1024 sc1\n"
      "s_nop 15\n"
      "buffer_load_dwordx4 v[92:95], v109, s[8:11], s84 offen offset:0 sc1\n"
      "buffer_load_dwordx4 v[96:99], v109, s[8:11], s84 offen offset:1024 sc1\n"
      "buffer_load_dwordx2 v[100:101], v119, s[8:11], s20 offen sc1\n"
      "buffer_store_dwordx2 v[126:127], v120, s[8:11], s79 offen sc1\n"
      "v_mov_b32 v64, v70\n"
      "v_alignbit_b32 v65, v71, v71, 16\n"
      "v_mov_b32 v66, -1\n"
      "v_mov_b32 v67, -1\n"
      "v_mov_b32 v68, s52\n"
      "v_mov_b32 v69, s67\n"
      "v_permlane32_swap_b32 v64, v66\n"
      "v_permlane32_swap_b32 v65, v67\n"
      "v_mov_b32_dpp v68, v69 quad_perm:[0,1,2,3] row_mask:0xc bank_mask:0xf\n"
      "v_add_u32 v68, v68, v109\n"
      "s_waitcnt vmcnt(8)\n"
      "L2_wrA:\n"
      "ds_write_b128 v112, v[0:3]\n"
      "v_smfmac_f32_16x16x64_bf16 v[32:35], v[0:3], v[128:135], v108\n"
      "v_max3_u32 v56, v0, v1, v2\n"
      "v_max_u32 v56, v56, v3\n"
      "v_smfmac_f32_16x16x64_bf16 v[36:39], v[0:3], v[136:143], v108\n"
      "s_waitcnt vmcnt(7)\n"
      "ds_write_b128 v112, v[4:7] offset:1024\n"
      "v_smfmac_f32_16x16x64_bf16 v[40:43], v[0:3], a[96:103], v108\n"
      "v_max3_u32 v56, v56, v4, v5\n"
      "v_max3_u32 v56, v56, v6, v7\n"
      "v_smfmac_f32_16x16x64_bf16 v[44:47], v[0:3], a[104:111], v108\n"
      "v_cmp_eq_u32 vcc, -1, v56\n"
      "v_smfmac_f32_16x16x64_bf16 v[32:35], v[4:7], v[144:151], v108\n"
      "s_nop 3\n"
      "s_cbranch_vccnz L2_retryA\n"
      "L2_okA:\n"
      "s_waitcnt lgkmcnt(0)\n"
      "s_barrier\n"
      "ds_read_b128 v[8:11], v113 offset:0\n"
      "ds_read_b128 v[12:15], v113 offset:1024\n"
      "v_smfmac_f32_16x16x64_bf16 v[36:39], v[4:7], v[152:159], v108\n"
      "ds_read_b128 v[16:19], v114 offset:0\n"
      "ds_read_b128 v[20:23], v114 offset:1024\n"
      "v_smfmac_f32_16x16x64_bf16 v[40:43], v[4:7], a[112:119], v108\n"
      "ds_read_b128 v[24:27], v115 offset:0\n"
      "ds_read_b128 v[28:31], v115 offset:1024\n"
      "v_smfmac_f32_16x16x64_bf16 v[44:47], v[4:7], a[120:127], v108\n"
      "v_smfmac_f32_16x16x64_bf16 v[48:51], v[0:3], a[224:231], v108\n"
      "v_smfmac_f32_16x16x64_bf16 v[52:55], v[0:3], a[232:239], v108\n"
      "v_smfmac_f32_16x16x64_bf16 v[48:51], v[4:7], a[240:247], v108\n"
      "v_smfmac_f32_16x16x64_bf16 v[52:55], v[4:7], a[248:255], v108\n"
      "s_waitcnt lgkmcnt(5)\n"
      "v_smfmac_f32_16x16x64_bf16 v[32:35], v[8:11], a[0:7], v108\n"
      "v_smfmac_f32_16x16x64_bf16 v[36:39], v[8:11], a[8:15], v108\n"
      "ds_read_b128 v[128:131], v109 offset:49152\n"
      "v_xor_b32 v76, 16, v116\n"
      "s_waitcnt lgkmcnt(5)\n"
      "v_smfmac_f32_16x16x64_bf16 v[32:35], v[12:15], a[16:23], v108\n"
      "ds_read_b128 v[132:135], v109 offset:50176\n"
      "s_waitcnt lgkmcnt(5)\n"
      "v_smfmac_f32_16x16x64_bf16 v[32:35], v[16:19], a[32:39], v108\n"
      "ds_read_b128 v[136:139], v109 offset:51200\n"
      "s_waitcnt lgkmcnt(5)\n"
      "v_smfmac_f32_16x16x64_bf16 v[32:35], v[20:23], a[48:55], v108\n"
      "ds_read_b128 v[140:143], v109 offset:52224\n"
      "s_waitcnt lgkmcnt(5)\n"
      "v_smfmac_f32_16x16x64_bf16 v[32:35], v[24:27], a[64:71], v108\n"
      "ds_read_b128 v[144:147], v109 offset:53248\n"
      "s_waitcnt lgkmcnt(5)\n"
      "v_smfmac_f32_16x16x64_bf16 v[32:35], v[28:31], a[80:87], v108\n"
      "ds_read_b128 v[148:151], v109 offset:54272\n"
      "s_nop 0\n"
      "v_smfmac_f32_16x16x64_bf16 v[36:39], v[12:15], a[24:31], v108\n"
      "ds_read_b128 v[152:155], v109 offset:55296\n"
      "s_nop 1\n"
      "v_smfmac_f32_16x16x64_bf16 v[36:39], v[16:19], a[40:47], v108\n"
      "ds_read_b128 v[156:159], v109 offset:56320\n"
      "v_add_f32 v32, v32, v34\n"
      "v_smfmac_f32_16x16x64_bf16 v[36:39], v[20:23], a[56:63], v108\n"
      "ds_read_b128 v[72:75], v109 offset:57344\n"
      "v_add_f32 v33, v33, v35\n"
      "v_smfmac_f32_16x16x64_bf16 v[36:39], v[24:27], a[72:79], v108\n"
      "buffer_store_dwordx4 v[64:67], v68, s[8:11], 0 offen sc1\n"
      "v_mul_f32 v32, s65, v32\n"
      "v_smfmac_f32_16x16x64_bf16 v[36:39], v[28:31], a[88:95], v108\n"
      "s_nop 1\n"
      "v_mul_f32 v33, s65, v33\n"
      "v_smfmac_f32_16x16x64_bf16 v[40:43], v[8:11], a[128:135], v108\n"
      "s_nop 1\n"
      "v_cvt_pk_bf16_f32 v32, v32, v33\n"
      "v_smfmac_f32_16x16x64_bf16 v[40:43], v[12:15], a[144:151], v108\n"
      "ds_write_b32 v116, v32\n"
      "v_add_f32 v36, v36, v38\n"
      "v_add_f32 v37, v37, v39\n"
      "v_smfmac_f32_16x16x64_bf16 v[40:43], v[16:19], a[160:167], v108\n"
      "v_mul_f32 v36, s65, v36\n"
      "v_mul_f32 v37, s65, v37\n"
      "v_smfmac_f32_16x16x64_bf16 v[40:43], v[20:23], a[176:183], v108\n"
      "v_cvt_pk_bf16_f32 v33, v36, v37\n"
      "v_smfmac_f32_16x16x64_bf16 v[40:43], v[24:27], a[192:199], v108\n"
      "ds_write_b32 v76, v33\n"
      "v_xor_b32 v77, 16, v118\n"
      "v_smfmac_f32_16x16x64_bf16 v[40:43], v[28:31], a[208:215], v108\n"
      "s_nop 2\n"
      "v_smfmac_f32_16x16x64_bf16 v[44:47], v[8:11], a[136:143], v108\n"
      "s_nop 2\n"
      "v_smfmac_f32_16x16x64_bf16 v[44:47], v[12:15], a[152:159], v108\n"
      "v_add_f32 v40, v40, v42\n"
      "v_smfmac_f32_16x16x64_bf16 v[44:47], v[16:19], a[168:175], v108\n"
      "s_waitcnt lgkmcnt(0)\n"
      "s_barrier\n"
      "ds_read_b64_tr_b16 v[56:57], v118 offset:40960\n"
      "v_add_f32 v41, v41, v43\n"
      "v_smfmac_f32_16x16x64_bf16 v[44:47], v[20:23], a[184:191], v108\n"
      "ds_read_b64_tr_b16 v[58:59], v118 offset:41472\n"
      "ds_read_b64_tr_b16 v[60:61], v118 offset:41984\n"
      "v_smfmac_f32_16x16x64_bf16 v[44:47], v[24:27], a[200:207], v108\n"
      "ds_read_b64_tr_b16 v[62:63], v118 offset:42496\n"
      "ds_read_b64_tr_b16 v[64:65], v77 offset:40960\n"
      "v_smfmac_f32_16x16x64_bf16 v[44:47], v[28:31], a[216:223], v108\n"
      "ds_read_b64_tr_b16 v[66:67], v77 offset:41472\n"
      "ds_read_b64_tr_b16 v[68:69], v77 offset:41984\n"
      "v_cvt_pk_bf16_f32 v40, v40, v41\n"
      "v_smfmac_f32_16x16x64_bf16 v[48:51], v[8:11], v[160:167], v108\n"
      "ds_read_b64_tr_b16 v[70:71], v77 offset:42496\n"
      "s_nop 2\n"
      "v_add_f32 v44, v44, v46\n"
      "v_add_f32 v45, v45, v47\n"
      "v_cvt_pk_bf16_f32 v41, v44, v45\n"
      "ds_write_b64 v117, v[40:41]\n"
      "v_smfmac_f32_16x16x64_bf16 v[48:51], v[12:15], v[176:183], v108\n"
      "s_waitcnt lgkmcnt(7)\n"
      "v_mfma_f32_16x16x32_bf16 v[32:35], v[128:131], v[56:59], v[72:75]\n"
      "v_mov_b64 v[42:43], 0\n"
      "v_mov_b64 v[46:47], 0\n"
      "v_mfma_f32_16x16x32_bf16 v[36:39], v[152:155], v[56:59], v[72:75]\n"
      "s_waitcnt lgkmcnt(5)\n"
      "v_mov_b64 v[40:41], v[72:73]\n"
      "v_mfma_f32_16x16x32_bf16 v[32:35], v[132:135], v[60:63], v[32:35]\n"
      "v_mfma_f32_16x16x32_bf16 v[36:39], v[156:159], v[60:63], v[36:39]\n"
      "s_waitcnt lgkmcnt(0)\n"
      "s_barrier\n"
      "ds_read_b64_tr_b16 v[72:73], v118 offset:43008\n"
      "ds_read_b64_tr_b16 v[74:75], v118 offset:43520\n"
      "v_mfma_f32_16x16x32_bf16 v[32:35], v[136:139], v[64:67], v[32:35]\n"
      "ds_read_b64_tr_b16 v[76:77], v118 offset:44032\n"
      "ds_read_b64_tr_b16 v[78:79], v118 offset:44544\n"
      "v_mfma_f32_16x16x32_bf16 v[36:39], v[144:147], v[64:67], v[36:39]\n"
      "v_mfma_f32_16x16x32_bf16 v[32:35], v[140:143], v[68:71], v[32:35]\n"
      "v_mfma_f32_16x16x32_bf16 v[36:39], v[148:151], v[68:71], v[36:39]\n"
      "s_nop 2\n"
      "v_smfmac_f32_16x16x64_bf16 v[48:51], v[16:19], v[192:199], v108\n"
      "s_nop 2\n"
      "v_smfmac_f32_16x16x64_bf16 v[48:51], v[20:23], v[208:215], v108\n"
      "v_cndmask_b32_e64 v32, v32, v36, s[38:39]\n"
      "v_smfmac_f32_16x16x64_bf16 v[48:51], v[24:27], v[224:231], v108\n"
      "v_cndmask_b32_e64 v33, v33, v37, s[38:39]\n"
      "s_waitcnt lgkmcnt(2)\n"
      "v_mfma_f32_16x16x32_bf16 v[40:43], v[72:75], v[56:59], v[40:43]\n"
      "s_waitcnt lgkmcnt(0)\n"
      "v_cndmask_b32_e64 v34, v34, v38, s[38:39]\n"
      "v_cndmask_b32_e64 v35, v35, v39, s[38:39]\n"
      "v_mfma_f32_16x16x32_bf16 v[40:43], v[76:79], v[60:63], v[40:43]\n"
      "v_exp_f32 v36, v32\n"
      "v_smfmac_f32_16x16x64_bf16 v[48:51], v[28:31], v[240:247], v108\n"
      "s_nop 1\n"
      "v_exp_f32 v37, v33\n"
      "v_smfmac_f32_16x16x64_bf16 v[52:55], v[8:11], v[168:175], v108\n"
      "v_exp_f32 v38, v34\n"
      "v_smfmac_f32_16x16x64_bf16 v[52:55], v[12:15], v[184:191], v108\n"
      "v_add_f32_dpp v68, v42, v40 row_ror:8 row_mask:0xf bank_mask:0x3\n"
      "v_smfmac_f32_16x16x64_bf16 v[52:55], v[16:19], v[200:207], v108\n"
      "s_memrealtime s[68:69]\n"
      "v_exp_f32 v39, v35\n"
      "v_add_f32_dpp v68, v41, v43 row_ror:8 row_mask:0xf bank_mask:0xc\n"
      "v_smfmac_f32_16x16x64_bf16 v[52:55], v[20:23], v[216:223], v108\n"
      "s_memrealtime s[70:71]\n"
      "v_cvt_pk_bf16_f32 v64, v36, v37\n"
      "v_exp_f32 v69, v68\n"
      "v_smfmac_f32_16x16x64_bf16 v[52:55], v[24:27], v[232:239], v108\n"
      "s_memrealtime s[72:73]\n"
      "v_cvt_pk_bf16_f32 v65, v38, v39\n"
      "v_add_f32 v48, v48, v50\n"
      "v_smfmac_f32_16x16x64_bf16 v[52:55], v[28:31], v[248:255], v108\n"
      "s_memrealtime s[74:75]\n"
      "v_mov_b32_dpp v40, v69 row_ror:8 row_mask:0xf bank_mask:0xf\n"
      "v_mov_b32_dpp v66, v64 row_ror:8 row_mask:0xf bank_mask:0xf\n"
      "v_mov_b32_dpp v67, v65 row_ror:8 row_mask:0xf bank_mask:0xf\n"
      "v_cvt_pk_bf16_f32 v40, v69, v40\n"
      "v_add_f32 v49, v49, v51\n"
      "v_mfma_f32_16x16x32_bf16 v[0:3], v[104:107], v[64:67], 0\n"
      "v_cndmask_b32_e64 v44, v40, 0, s[38:39]\n"
      "v_cndmask_b32_e64 v45, 0, v40, s[38:39]\n"
      "v_add_f32 v52, v52, v54\n"
      "v_add_f32 v53, v53, v55\n"
      "v_mfma_f32_16x16x32_bf16 v[0:3], v[104:107], v[44:47], v[0:3]\n"
      "v_cvt_pk_bf16_f32 v70, v48, v49\n"
      "v_cvt_pk_bf16_f32 v71, v53, v52\n"
      "s_nop 1\n"
      "v_mfma_f32_16x16x32_bf16 v[4:7], v[70:73], v[44:47], 0\n"
      "s_waitcnt vmcnt(5)\n"
      "s_nop 1\n"
      "v_mfma_f32_16x16x32_bf16 v[24:27], v[80:83], v[64:67], v[4:7]\n"
      "s_nop 0\n"
      "v_rcp_f32 v12, v0\n"
      "v_mfma_f32_16x16x32_bf16 v[28:31], v[84:87], v[64:67], v[4:7]\n"
      "s_nop 0\n"
      "v_subrev_u32 v13, 0x21800000, v0\n"
      "v_cmp_le_u32 vcc, 0x3c000000, v13\n"
      "s_nop 0\n"
      "s_nop 0\n"
      "v_mul_f32 v8, v24, v12\n"
      "v_cndmask_b32 v13, v111, v125, vcc\n"
      "v_mul_f32 v9, v25, v12\n"
      "v_mul_f32 v10, v26, v12\n"
      "v_mul_f32 v11, v27, v12\n"
      "v_mul_f32_dpp v8, v28, v12 quad_perm:[0,1,2,3] row_mask:0xf bank_mask:0xc\n"
      "v_mul_f32_dpp v9, v29, v12 quad_perm:[0,1,2,3] row_mask:0xf bank_mask:0xc\n"
      "v_mul_f32_dpp v10, v30, v12 quad_perm:[0,1,2,3] row_mask:0xf bank_mask:0xc\n"
      "v_mul_f32_dpp v11, v31, v12 quad_perm:[0,1,2,3] row_mask:0xf bank_mask:0xc\n"
      "v_cvt_pk_bf16_f32 v14, v8, v9\n"
      "v_cvt_pk_bf16_f32 v15, v10, v11\n"
      "buffer_store_dwordx2 v[14:15], v13, s[8:11], s63 offen sc1\n"
      "s_cbranch_vccnz L2_exact\n"
      "L2_out:\n"
      "s_cmp_eq_u32 s28, 63\n"
      "s_cbranch_scc1 L2_last\n"
      "s_add_u32 s40, s28, 2\n"
      "s_waitcnt vmcnt(3)\n"
      "v_lshrrev_b32 v8, 7, v119\n"
      "v_add_u32 v8, v100, v8\n"
      "v_mov_b32 v9, v101\n"
      "v_cmp_ne_u32_e64 s[54:55], s85, v9\n"
      "v_cmp_ge_i32 vcc, s40, v8\n"
      "s_or_b64 vcc, vcc, s[54:55]\n"
      "ds_read_b32 v24, v109 offset:57344\n"
      "v_max3_f32 v36, v32, v33, v34\n"
      "v_max3_f32 v36, v36, v35, v68\n"
      "s_nop 1\n"
      "v_max_f32_dpp v36, v36, v36 row_ror:8 row_mask:0xf bank_mask:0xf\n"
      "v_mov_b32 v37, v36\n"
      "s_nop 1\n"
      "v_permlane16_swap_b32 v36, v37\n"
      "v_max_f32 v36, v36, v37\n"
      "v_mov_b32 v37, v36\n"
      "s_nop 1\n"
      "v_permlane32_swap_b32 v36, v37\n"
      "v_max_f32 v36, v36, v37\n"
      "s_waitcnt lgkmcnt(0)\n"
      "v_sub_f32 v24, v24, v36\n"
      "v_mov_b32 v25, v24\n"
      "v_mov_b64 v[26:27], v[24:25]\n"
      "ds_write_b128 v109, v[24:27] offset:57344\n"
      "s_cbranch_vccz L2_fr\n"
      "buffer_load_dwordx2 v[100:101], v119, s[8:11], s20 offen sc1\n"
      "L2_fr:\n"
      "s_barrier\n"
      "ds_read_b128 v[128:131], v123 offset:0\n"
      "ds_read_b128 v[132:135], v123 offset:1024\n"
      "ds_read_b128 v[136:139], v123 offset:2048\n"
      "ds_read_b128 v[140:143], v123 offset:3072\n"
      "ds_read_b128 v[144:147], v123 offset:4096\n"
      "ds_read_b128 v[148:151], v123 offset:5120\n"
      "ds_read_b128 v[152:155], v123 offset:6144\n"
      "ds_read_b128 v[156:159], v123 offset:7168\n"
      "v_mov_b64 v[32:33], 0\n"
      "v_mov_b64 v[34:35], 0\n"
      "v_mov_b64 v[36:37], 0\n"
      "v_mov_b64 v[38:39], 0\n"
      "v_mov_b64 v[40:41], 0\n"
      "v_mov_b64 v[42:43], 0\n"
      "v_mov_b64 v[44:45], 0\n"
      "v_mov_b64 v[46:47], 0\n"
      "v_mov_b64 v[48:49], 0\n"
      "v_mov_b64 v[50:51], 0\n"
      "v_mov_b64 v[52:53], 0\n"
      "v_mov_b64 v[54:55], 0\n"
      "s_waitcnt lgkmcnt(0)\n"
      "ds_write_b128 v109, v[92:95] offset:53248\n"
      "ds_write_b128 v109, v[96:99] offset:54272\n"
      "s_sub_u32 s67, s63, 512\n"
      "s_mov_b32 s52, s17\n"
      "s_add_u32 s29, s29, 0x80000\n"
      "s_cmp_eq_u32 s29, 0x180000\n"
      "s_cselect_b32 s29, 0, s29\n"
      "s_add_u32 s63, s29, s14\n"
      "s_add_u32 s28, s28, 1\n"
      "s_and_b32 s31, s28, 3\n"
      "s_lshl_b32 s31, s31, 16\n"
      "s_add_u32 s31, s31, s12\n"
      "s_add_u32 s62, s31, s78\n"
      "s_add_u32 s31, s31, s25\n"
      "s_cmp_lt_u32 s28, 63\n"
      "s_cselect_b32 s54, 0x10000, 0\n"
      "s_cmp_eq_u32 s28, 31\n"
      "s_cselect_b32 s55, s24, 0\n"
      "s_add_u32 s84, s84, s54\n"
      "s_sub_u32 s84, s84, s55\n"
      "v_add_u32 v126, 1, v126\n"
      "s_add_u32 s40, s28, 1\n"
      "s_cmp_eq_u32 s28, 32\n"
      "s_cselect_b32 s55, s24, 0\n"
      "s_add_u32 s54, s55, 0x10000\n"
      "s_sub_u32 s55, 0x10000, s55\n"
      "s_add_u32 s15, s15, s54\n"
      "s_add_u32 s17, s17, s55\n"
      "s_waitcnt vmcnt(0)\n"
      "v_lshrrev_b32 v8, 7, v119\n"
      "v_add_u32 v100, v100, v8\n"
      "v_cmp_ne_u32_e64 s[54:55], s85, v101\n"
      "v_cmp_ge_i32 vcc, s40, v100\n"
      "s_or_b64 vcc, vcc, s[54:55]\n"
      "s_cbranch_vccnz L2_slowf\n"
      "L2_fok:\n"
      "s_waitcnt vmcnt(1) lgkmcnt(0)\n"
      "s_branch L2_loop\n"
      "L2_last:\n"
      "s_sub_u32 s67, s63, 512\n"
      "s_mov_b32 s52, s17\n"
      "v_mov_b32 v64, v70\n"
      "v_alignbit_b32 v65, v71, v71, 16\n"
      "v_mov_b32 v66, -1\n"
      "v_mov_b32 v67, -1\n"
      "v_mov_b32 v68, s52\n"
      "v_mov_b32 v69, s67\n"
      "v_permlane32_swap_b32 v64, v66\n"
      "v_permlane32_swap_b32 v65, v67\n"
      "v_mov_b32_dpp v68, v69 quad_perm:[0,1,2,3] row_mask:0xc bank_mask:0xf\n"
      "v_add_u32 v68, v68, v109\n"
      "buffer_store_dwordx4 v[64:67], v68, s[8:11], 0 offen sc1\n"
      "s_waitcnt vmcnt(2)\n"
      "s_barrier\n"
      "v_mov_b32 v126, 63\n"
      "buffer_store_dwordx2 v[126:127], v120, s[8:11], s21 offen sc1\n"
      "s_waitcnt vmcnt(0) lgkmcnt(0)\n"
      "s_barrier\n"
      "v_mov_b32 v126, 64\n"
      "buffer_store_dwordx2 v[126:127], v120, s[8:11], s21 offen sc1\n"
      "s_branch Lend\n"
      "L2_slowf:\n"
      "s_sub_u32 s64, s64, 1\n"
      "s_cbranch_scc1 L2_giveF\n"
      "buffer_load_dwordx2 v[100:101], v119, s[8:11], s20 offen sc1\n"
      "s_waitcnt vmcnt(0)\n"
      "v_lshrrev_b32 v8, 7, v119\n"
      "v_add_u32 v100, v100, v8\n"
      "v_cmp_ne_u32_e64 s[54:55], s85, v101\n"
      "v_cmp_ge_i32 vcc, s40, v100\n"
      "s_or_b64 vcc, vcc, s[54:55]\n"
      "s_cbranch_vccnz L2_slowf\n"
      "s_branch L2_fok\n"
      "L2_giveF:\n"
      "s_mov_b32 s64, 0\n"
      "s_branch L2_fok\n"
      "L2_exact:\n"
      "s_nop 7\n"
      "v_max3_f32 v36, v32, v33, v34\n"
      "v_max3_f32 v36, v36, v35, v68\n"
      "s_nop 1\n"
      "v_max_f32_dpp v36, v36, v36 row_ror:8 row_mask:0xf bank_mask:0xf\n"
      "v_mov_b32 v37, v36\n"
      "s_nop 1\n"
      "v_permlane16_swap_b32 v36, v37\n"
      "v_max_f32 v36, v36, v37\n"
      "v_mov_b32 v37, v36\n"
      "s_nop 1\n"
      "v_permlane32_swap_b32 v36, v37\n"
      "v_max_f32 v36, v36, v37\n"
      "v_mov_b32 v41, v36\n"
      "v_sub_f32 v36, v32, v41\n"
      "v_sub_f32 v37, v33, v41\n"
      "v_sub_f32 v38, v34, v41\n"
      "v_sub_f32 v39, v35, v41\n"
      "v_sub_f32 v69, v68, v41\n"
      "v_exp_f32 v36, v36\n"
      "v_exp_f32 v37, v37\n"
      "v_exp_f32 v38, v38\n"
      "v_exp_f32 v39, v39\n"
      "v_exp_f32 v69, v69\n"
      "v_cvt_pk_bf16_f32 v64, v36, v37\n"
      "v_cvt_pk_bf16_f32 v65, v38, v39\n"
      "s_nop 0\n"
      "v_mov_b32_dpp v66, v64 row_ror:8 row_mask:0xf bank_mask:0xf\n"
      "v_mov_b32_dpp v67, v65 row_ror:8 row_mask:0xf bank_mask:0xf\n"
      "v_mov_b32_dpp v40, v69 row_ror:8 row_mask:0xf bank_mask:0xf\n"
      "v_cvt_pk_bf16_f32 v40, v69, v40\n"
      "v_cndmask_b32_e64 v44, v40, 0, s[38:39]\n"
      "v_cndmask_b32_e64 v45, 0, v40, s[38:39]\n"
      "v_mfma_f32_16x16x32_bf16 v[0:3], v[104:107], v[64:67], 0\n"
      "s_nop 0\n"
      "v_mfma_f32_16x16x32_bf16 v[0:3], v[104:107], v[44:47], v[0:3]\n"
      "v_mfma_f32_16x16x32_bf16 v[4:7], v[70:73], v[44:47], 0\n"
      "v_mfma_f32_16x16x32_bf16 v[24:27], v[80:83], v[64:67], v[4:7]\n"
      "v_mfma_f32_16x16x32_bf16 v[28:31], v[84:87], v[64:67], v[4:7]\n"
      "s_nop 4\n"
      "v_rcp_f32 v12, v0\n"
      "v_cndmask_b32 v13, v125, v111, vcc\n"
      "v_mul_f32 v8, v24, v12\n"
      "v_mul_f32 v9, v25, v12\n"
      "v_mul_f32 v10, v26, v12\n"
      "v_mul_f32 v11, v27, v12\n"
      "v_mul_f32_dpp v8, v28, v12 quad_perm:[0,1,2,3] row_mask:0xf bank_mask:0xc\n"
      "v_mul_f32_dpp v9, v29, v12 quad_perm:[0,1,2,3] row_mask:0xf bank_mask:0xc\n"
      "v_mul_f32_dpp v10, v30, v12 quad_perm:[0,1,2,3] row_mask:0xf bank_mask:0xc\n"
      "v_mul_f32_dpp v11, v31, v12 quad_perm:[0,1,2,3] row_mask:0xf bank_mask:0xc\n"
      "v_cvt_pk_bf16_f32 v14, v8, v9\n"
      "v_cvt_pk_bf16_f32 v15, v10, v11\n"
      "buffer_store_dwordx2 v[14:15], v13, s[8:11], s63 offen sc1\n"
      "s_waitcnt vmcnt(1)\n"
      "s_branch L2_out\n"
      "L2_retryA:\n"
      "s_sub_u32 s64, s64, 1\n"
      "s_cbranch_scc1 L2_giveA\n"
      "s_waitcnt lgkmcnt(0)\n"
      "s_nop 7\n"
      "buffer_load_dwordx4 v[0:3], v124, s[8:11], s31 offen sc1\n"
      "buffer_load_dwordx4 v[4:7], v124, s[8:11], s62 offen sc1\n"
      "v_mov_b64 v[32:33], 0\n"
      "v_mov_b64 v[34:35], 0\n"
      "v_mov_b64 v[36:37], 0\n"
      "v_mov_b64 v[38:39], 0\n"
      "v_mov_b64 v[40:41], 0\n"
      "v_mov_b64 v[42:43], 0\n"
      "v_mov_b64 v[44:45], 0\n"
      "v_mov_b64 v[46:47], 0\n"
      "s_waitcnt vmcnt(0)\n"
      "s_branch L2_wrA\n"
      "L2_giveA:\n"
      "s_mov_b32 s64, 0\n"
      "s_branch L2_okA\n"
      "L3_init:\n"
      "s_add_u32 s67, s14, 0xffe00\n"
      "s_mov_b32 s52, s17\n"
      "s_getpc_b64 s[48:49]\n"
      "L3_ua:\n"
      "s_add_u32 s48, s48, L3_sa-L3_ua\n"
      "s_addc_u32 s49, s49, 0\n"
      "s_getpc_b64 s[50:51]\n"
      "L3_ub:\n"
      "s_add_u32 s50, s50, L3_sb-L3_ub\n"
      "s_addc_u32 s51, s51, 0\n"
      "s_mov_b32 s58, 1\n"
      "s_ashr_i32 s81, s26, 16\n"
      "s_sext_i32_i16 s26, s26\n"
      "s_ashr_i32 s53, s19, 16\n"
      "s_and_b32 s76, s19, 0xffff\n"
      "s_mov_b32 s19, -200\n"
      "s_waitcnt lgkmcnt(0)\n"
      "s_memrealtime s[68:69]\n"
      "s_nop 4\n"
      "s_memrealtime s[70:71]\n"
      "s_nop 4\n"
      "s_memrealtime s[72:73]\n"
      "s_nop 4\n"
      "s_memrealtime s[74:75]\n"
      "s_memtime s[42:43]\n"
      "s_waitcnt lgkmcnt(0)\n"
      "s_add_u32 s68, s68, s70\n"
      "s_add_u32 s72, s72, s74\n"
      "s_add_u32 s68, s68, s72\n"
      "s_mul_i32 s68, s68, 6\n"
      "s_sub_u32 s40, s68, s42\n"
      "s_sub_u32 s41, s68, 0x4000\n"
      "v_mov_b32 v9, 0\n"
      "v_mov_b64 v[32:33], 0\n"
      "v_mov_b64 v[34:35], 0\n"
      "v_mov_b64 v[36:37], 0\n"
      "v_mov_b64 v[38:39], 0\n"
      "v_mov_b64 v[40:41], 0\n"
      "v_mov_b64 v[42:43], 0\n"
      "v_mov_b64 v[44:45], 0\n"
      "v_mov_b64 v[46:47], 0\n"
      "v_mov_b64 v[48:49], 0\n"
      "v_mov_b64 v[50:51], 0\n"
      "v_mov_b64 v[52:53], 0\n"
      "v_mov_b64 v[54:55], 0\n"
      "s_waitcnt vmcnt(0) lgkmcnt(0)\n"
      "s_branch L3_sa\n"
      ".p2align 6\n"
      "L3_loop:\n"
      ".rept 160\n"
      "s_nop 1\n"
      ".endr\n"
      "L3_sa:\n"
      "s_barrier\n"
      "buffer_load_dwordx4 v[0:3], v124, s[8:11], s31 offen sc1\n"
      "buffer_load_dwordx4 v[4:7], v124, s[8:11], s62 offen sc1\n"
      "v_mov_b32 v10, 0\n"
      "ds_read_b32 v8, v10 offset:58368\n"
      "s_nop 15\n"
      "buffer_load_dwordx4 v[80:83], v109, s[8:11], s15 offen sc1\n"
      "buffer_load_dwordx2 v[84:85], v109, s[8:11], s15 offen offset:1032 sc1\n"
      "buffer_load_dwordx2 v[86:87], v109, s[8:11], s15 offen offset:1024 sc1\n"
      "s_nop 15\n"
      "s_waitcnt lgkmcnt(0)\n"
      "v_add_u32 v8, s40, v8\n"
      "buffer_store_dwordx2 v[8:9], v122, s[8:11], s16 offen sc1\n"
      "buffer_load_dwordx4 v[92:95], v109, s[8:11], s84 offen offset:0 sc1\n"
      "buffer_load_dwordx4 v[96:99], v109, s[8:11], s84 offen offset:1024 sc1\n"
      "buffer_store_dwordx2 v[126:127], v120, s[8:11], s79 offen sc1\n"
      "v_mov_b32 v64, v70\n"
      "v_alignbit_b32 v65, v71, v71, 16\n"
      "v_mov_b32 v66, -1\n"
      "v_mov_b32 v67, -1\n"
      "v_mov_b32 v68, s52\n"
      "v_mov_b32 v69, s67\n"
      "v_permlane32_swap_b32 v64, v66\n"
      "v_permlane32_swap_b32 v65, v67\n"
      "v_mov_b32_dpp v68, v69 quad_perm:[0,1,2,3] row_mask:0xc bank_mask:0xf\n"
      "v_add_u32 v68, v68, v109\n"
      "s_waitcnt vmcnt(8)\n"
      "L3_wrA:\n"
      "ds_write_b128 v112, v[0:3]\n"
      "v_smfmac_f32_16x16x64_bf16 v[32:35], v[0:3], v[128:135], v108\n"
      "v_max3_u32 v56, v0, v1, v2\n"
      "v_max_u32 v56, v56, v3\n"
      "v_smfmac_f32_16x16x64_bf16 v[36:39], v[0:3], v[136:143], v108\n"
      "s_waitcnt vmcnt(7)\n"
      "ds_write_b128 v112, v[4:7] offset:1024\n"
      "v_smfmac_f32_16x16x64_bf16 v[40:43], v[0:3], a[96:103], v108\n"
      "v_max3_u32 v56, v56, v4, v5\n"
      "v_max3_u32 v56, v56, v6, v7\n"
      "v_smfmac_f32_16x16x64_bf16 v[44:47], v[0:3], a[104:111], v108\n"
      "v_cmp_eq_u32 vcc, -1, v56\n"
      "v_smfmac_f32_16x16x64_bf16 v[32:35], v[4:7], v[144:151], v108\n"
      "s_nop 3\n"
      "s_cbranch_vccnz L3_retryA\n"
      "L3_okA:\n"
      "s_waitcnt lgkmcnt(0)\n"
      "s_barrier\n"
      "ds_read_b128 v[8:11], v113 offset:0\n"
      "ds_read_b128 v[12:15], v113 offset:1024\n"
      "v_smfmac_f32_16x16x64_bf16 v[36:39], v[4:7], v[152:159], v108\n"
      "ds_read_b128 v[16:19], v114 offset:0\n"
      "ds_read_b128 v[20:23], v114 offset:1024\n"
      "v_smfmac_f32_16x16x64_bf16 v[40:43], v[4:7], a[112:119], v108\n"
      "ds_read_b128 v[24:27], v115 offset:0\n"
      "ds_read_b128 v[28:31], v115 offset:1024\n"
      "v_smfmac_f32_16x16x64_bf16 v[44:47], v[4:7], a[120:127], v108\n"
      "v_smfmac_f32_16x16x64_bf16 v[48:51], v[0:3], a[224:231], v108\n"
      "v_smfmac_f32_16x16x64_bf16 v[52:55], v[0:3], a[232:239], v108\n"
      "v_smfmac_f32_16x16x64_bf16 v[48:51], v[4:7], a[240:247], v108\n"
      "v_smfmac_f32_16x16x64_bf16 v[52:55], v[4:7], a[248:255], v108\n"
      "s_waitcnt lgkmcnt(5)\n"
      "v_smfmac_f32_16x16x64_bf16 v[32:35], v[8:11], a[0:7], v108\n"
      "v_smfmac_f32_16x16x64_bf16 v[36:39], v[8:11], a[8:15], v108\n"
      "ds_read_b128 v[128:131], v109 offset:49152\n"
      "v_xor_b32 v76, 16, v116\n"
      "s_waitcnt lgkmcnt(5)\n"
      "v_smfmac_f32_16x16x64_bf16 v[32:35], v[12:15], a[16:23], v108\n"
      "ds_read_b128 v[132:135], v109 offset:50176\n"
      "s_waitcnt lgkmcnt(5)\n"
      "v_smfmac_f32_16x16x64_bf16 v[32:35], v[16:19], a[32:39], v108\n"
      "ds_read_b128 v[136:139], v109 offset:51200\n"
      "s_waitcnt lgkmcnt(5)\n"
      "v_smfmac_f32_16x16x64_bf16 v[32:35], v[20:23], a[48:55], v108\n"
      "ds_read_b128 v[140:143], v109 offset:52224\n"
      "s_waitcnt lgkmcnt(5)\n"
      "v_smfmac_f32_16x16x64_bf16 v[32:35], v[24:27], a[64:71], v108\n"
      "ds_read_b128 v[144:147], v109 offset:53248\n"
      "s_waitcnt lgkmcnt(5)\n"
      "v_smfmac_f32_16x16x64_bf16 v[32:35], v[28:31], a[80:87], v108\n"
      "ds_read_b128 v[148:151], v109 offset:54272\n"
      "s_nop 0\n"
      "v_smfmac_f32_16x16x64_bf16 v[36:39], v[12:15], a[24:31], v108\n"
      "ds_read_b128 v[152:155], v109 offset:55296\n"
      "s_nop 1\n"
      "v_smfmac_f32_16x16x64_bf16 v[36:39], v[16:19], a[40:47], v108\n"
      "ds_read_b128 v[156:159], v109 offset:56320\n"
      "v_add_f32 v32, v32, v34\n"
      "v_smfmac_f32_16x16x64_bf16 v[36:39], v[20:23], a[56:63], v108\n"
      "ds_read_b128 v[72:75], v109 offset:57344\n"
      "v_add_f32 v33, v33, v35\n"
      "v_smfmac_f32_16x16x64_bf16 v[36:39], v[24:27], a[72:79], v108\n"
      "v_mul_f32 v32, s65, v32\n"
      "v_smfmac_f32_16x16x64_bf16 v[36:39], v[28:31], a[88:95], v108\n"
      "s_nop 1\n"
      "v_mul_f32 v33, s65, v33\n"
      "v_smfmac_f32_16x16x64_bf16 v[40:43], v[8:11], a[128:135], v108\n"
      "s_nop 1\n"
      "v_cvt_pk_bf16_f32 v32, v32, v33\n"
      "v_smfmac_f32_16x16x64_bf16 v[40:43], v[12:15], a[144:151], v108\n"
      "ds_write_b32 v116, v32\n"
      "v_add_f32 v36, v36, v38\n"
      "v_add_f32 v37, v37, v39\n"
      "v_smfmac_f32_16x16x64_bf16 v[40:43], v[16:19], a[160:167], v108\n"
      "v_mul_f32 v36, s65, v36\n"
      "v_mul_f32 v37, s65, v37\n"
      "v_smfmac_f32_16x16x64_bf16 v[40:43], v[20:23], a[176:183], v108\n"
      "v_cvt_pk_bf16_f32 v33, v36, v37\n"
      "v_smfmac_f32_16x16x64_bf16 v[40:43], v[24:27], a[192:199], v108\n"
      "ds_write_b32 v76, v33\n"
      "v_xor_b32 v77, 16, v118\n"
      "v_smfmac_f32_16x16x64_bf16 v[40:43], v[28:31], a[208:215], v108\n"
      "buffer_store_dwordx4 v[64:67], v68, s[8:11], 0 offen sc1\n"
      "s_nop 2\n"
      "v_smfmac_f32_16x16x64_bf16 v[44:47], v[8:11], a[136:143], v108\n"
      "s_nop 2\n"
      "v_smfmac_f32_16x16x64_bf16 v[44:47], v[12:15], a[152:159], v108\n"
      "v_add_f32 v40, v40, v42\n"
      "v_smfmac_f32_16x16x64_bf16 v[44:47], v[16:19], a[168:175], v108\n"
      "s_waitcnt lgkmcnt(0)\n"
      "s_barrier\n"
      "ds_read_b64_tr_b16 v[56:57], v118 offset:40960\n"
      "v_add_f32 v41, v41, v43\n"
      "v_smfmac_f32_16x16x64_bf16 v[44:47], v[20:23], a[184:191], v108\n"
      "ds_read_b64_tr_b16 v[58:59], v118 offset:41472\n"
      "ds_read_b64_tr_b16 v[60:61], v118 offset:41984\n"
      "v_smfmac_f32_16x16x64_bf16 v[44:47], v[24:27], a[200:207], v108\n"
      "ds_read_b64_tr_b16 v[62:63], v118 offset:42496\n"
      "ds_read_b64_tr_b16 v[64:65], v77 offset:40960\n"
      "v_smfmac_f32_16x16x64_bf16 v[44:47], v[28:31], a[216:223], v108\n"
      "ds_read_b64_tr_b16 v[66:67], v77 offset:41472\n"
      "ds_read_b64_tr_b16 v[68:69], v77 offset:41984\n"
      "v_cvt_pk_bf16_f32 v40, v40, v41\n"
      "v_smfmac_f32_16x16x64_bf16 v[48:51], v[8:11], v[160:167], v108\n"
      "ds_read_b64_tr_b16 v[70:71], v77 offset:42496\n"
      "s_nop 2\n"
      "v_add_f32 v44, v44, v46\n"
      "v_add_f32 v45, v45, v47\n"
      "v_cvt_pk_bf16_f32 v41, v44, v45\n"
      "ds_write_b64 v117, v[40:41]\n"
      "v_smfmac_f32_16x16x64_bf16 v[48:51], v[12:15], v[176:183], v108\n"
      "s_waitcnt lgkmcnt(7)\n"
      "v_mfma_f32_16x16x32_bf16 v[32:35], v[128:131], v[56:59], v[72:75]\n"
      "v_mov_b64 v[42:43], 0\n"
      "v_mov_b64 v[46:47], 0\n"
      "v_mfma_f32_16x16x32_bf16 v[36:39], v[152:155], v[56:59], v[72:75]\n"
      "s_waitcnt lgkmcnt(5)\n"
      "v_mov_b64 v[40:41], v[72:73]\n"
      "v_mfma_f32_16x16x32_bf16 v[32:35], v[132:135], v[60:63], v[32:35]\n"
      "v_mfma_f32_16x16x32_bf16 v[36:39], v[156:159], v[60:63], v[36:39]\n"
      "s_waitcnt lgkmcnt(0)\n"
      "s_barrier\n"
      "ds_read_b64_tr_b16 v[72:73], v118 offset:43008\n"
      "ds_read_b64_tr_b16 v[74:75], v118 offset:43520\n"
      "v_mfma_f32_16x16x32_bf16 v[32:35], v[136:139], v[64:67], v[32:35]\n"
      "ds_read_b64_tr_b16 v[76:77], v118 offset:44032\n"
      "ds_read_b64_tr_b16 v[78:79], v118 offset:44544\n"
      "v_mfma_f32_16x16x32_bf16 v[36:39], v[144:147], v[64:67], v[36:39]\n"
      "v_mfma_f32_16x16x32_bf16 v[32:35], v[140:143], v[68:71], v[32:35]\n"
      "v_mfma_f32_16x16x32_bf16 v[36:39], v[148:151], v[68:71], v[36:39]\n"
      "s_nop 2\n"
      "v_smfmac_f32_16x16x64_bf16 v[48:51], v[16:19], v[192:199], v108\n"
      "s_nop 2\n"
      "v_smfmac_f32_16x16x64_bf16 v[48:51], v[20:23], v[208:215], v108\n"
      "v_cndmask_b32_e64 v32, v32, v36, s[38:39]\n"
      "v_smfmac_f32_16x16x64_bf16 v[48:51], v[24:27], v[224:231], v108\n"
      "v_cndmask_b32_e64 v33, v33, v37, s[38:39]\n"
      "s_waitcnt lgkmcnt(2)\n"
      "v_mfma_f32_16x16x32_bf16 v[40:43], v[72:75], v[56:59], v[40:43]\n"
      "s_waitcnt lgkmcnt(0)\n"
      "v_cndmask_b32_e64 v34, v34, v38, s[38:39]\n"
      "v_cndmask_b32_e64 v35, v35, v39, s[38:39]\n"
      "v_mfma_f32_16x16x32_bf16 v[40:43], v[76:79], v[60:63], v[40:43]\n"
      "v_exp_f32 v36, v32\n"
      "v_smfmac_f32_16x16x64_bf16 v[48:51], v[28:31], v[240:247], v108\n"
      "s_nop 1\n"
      "v_exp_f32 v37, v33\n"
      "v_smfmac_f32_16x16x64_bf16 v[52:55], v[8:11], v[168:175], v108\n"
      "v_exp_f32 v38, v34\n"
      "v_smfmac_f32_16x16x64_bf16 v[52:55], v[12:15], v[184:191], v108\n"
      "v_add_f32_dpp v68, v42, v40 row_ror:8 row_mask:0xf bank_mask:0x3\n"
      "v_smfmac_f32_16x16x64_bf16 v[52:55], v[16:19], v[200:207], v108\n"
      "s_memrealtime s[68:69]\n"
      "v_exp_f32 v39, v35\n"
      "v_add_f32_dpp v68, v41, v43 row_ror:8 row_mask:0xf bank_mask:0xc\n"
      "v_smfmac_f32_16x16x64_bf16 v[52:55], v[20:23], v[216:223], v108\n"
      "s_memrealtime s[70:71]\n"
      "v_cvt_pk_bf16_f32 v64, v36, v37\n"
      "v_exp_f32 v69, v68\n"
      "v_smfmac_f32_16x16x64_bf16 v[52:55], v[24:27], v[232:239], v108\n"
      "s_memrealtime s[72:73]\n"
      "v_cvt_pk_bf16_f32 v65, v38, v39\n"
      "v_add_f32 v48, v48, v50\n"
      "v_smfmac_f32_16x16x64_bf16 v[52:55], v[28:31], v[248:255], v108\n"
      "s_memrealtime s[74:75]\n"
      "v_mov_b32_dpp v40, v69 row_ror:8 row_mask:0xf bank_mask:0xf\n"
      "v_mov_b32_dpp v66, v64 row_ror:8 row_mask:0xf bank_mask:0xf\n"
      "v_mov_b32_dpp v67, v65 row_ror:8 row_mask:0xf bank_mask:0xf\n"
      "v_cvt_pk_bf16_f32 v40, v69, v40\n"
      "v_add_f32 v49, v49, v51\n"
      "v_mfma_f32_16x16x32_bf16 v[0:3], v[104:107], v[64:67], 0\n"
      "v_cndmask_b32_e64 v44, v40, 0, s[38:39]\n"
      "v_cndmask_b32_e64 v45, 0, v40, s[38:39]\n"
      "v_add_f32 v52, v52, v54\n"
      "v_add_f32 v53, v53, v55\n"
      "v_mfma_f32_16x16x32_bf16 v[0:3], v[104:107], v[44:47], v[0:3]\n"
      "v_cvt_pk_bf16_f32 v70, v48, v49\n"
      "v_cvt_pk_bf16_f32 v71, v53, v52\n"
      "s_nop 1\n"
      "v_mfma_f32_16x16x32_bf16 v[4:7], v[70:73], v[44:47], 0\n"
      "s_waitcnt vmcnt(5)\n"
      "s_nop 1\n"
      "v_mfma_f32_16x16x32_bf16 v[24:27], v[80:83], v[64:67], v[4:7]\n"
      "s_nop 0\n"
      "v_rcp_f32 v12, v0\n"
      "v_mfma_f32_16x16x32_bf16 v[28:31], v[84:87], v[64:67], v[4:7]\n"
      "s_nop 0\n"
      "v_subrev_u32 v13, 0x21800000, v0\n"
      "v_cmp_le_u32 vcc, 0x3c000000, v13\n"
      "s_nop 0\n"
      "s_nop 0\n"
      "v_mul_f32 v8, v24, v12\n"
      "v_cndmask_b32 v13, v111, v125, vcc\n"
      "v_mul_f32 v9, v25, v12\n"
      "v_mul_f32 v10, v26, v12\n"
      "v_mul_f32 v11, v27, v12\n"
      "v_mul_f32_dpp v8, v28, v12 quad_perm:[0,1,2,3] row_mask:0xf bank_mask:0xc\n"
      "v_mul_f32_dpp v9, v29, v12 quad_perm:[0,1,2,3] row_mask:0xf bank_mask:0xc\n"
      "v_mul_f32_dpp v10, v30, v12 quad_perm:[0,1,2,3] row_mask:0xf bank_mask:0xc\n"
      "v_mul_f32_dpp v11, v31, v12 quad_perm:[0,1,2,3] row_mask:0xf bank_mask:0xc\n"
      "v_cvt_pk_bf16_f32 v14, v8, v9\n"
      "v_cvt_pk_bf16_f32 v15, v10, v11\n"
      "buffer_store_dwordx2 v[14:15], v13, s[8:11], s63 offen sc1\n"
      "s_cbranch_vccnz L3_exact\n"
      "L3_out:\n"
      "buffer_load_dwordx2 v[16:17], v119, s[8:11], s16 offen offset:0 sc1\n"
      "buffer_load_dwordx2 v[18:19], v119, s[8:11], s16 offen offset:2048 sc1\n"
      "s_add_u32 s30, s41, s23\n"
      "s_cmp_eq_u32 s28, 63\n"
      "s_cbranch_scc1 L3_last\n"
      "s_waitcnt lgkmcnt(0)\n"
      "s_add_u32 s68, s68, s70\n"
      "s_add_u32 s72, s72, s74\n"
      "s_add_u32 s68, s68, s72\n"
      "s_mul_i32 s68, s68, 6\n"
      "s_mov_b32 s80, s68\n"
      "s_sub_u32 s44, s30, s68\n"
      "s_add_u32 s45, s44, 250\n"
      "s_cmp_gt_u32 s45, 1530\n"
      "s_cbranch_scc1 L3_late\n"
      "s_max_i32 s44, s44, 0\n"
      "s_lshr_b32 s44, s44, 1\n"
      "s_and_b32 s44, s44, -4\n"
      "L3_goB:\n"
      "s_sub_u32 s54, s50, s44\n"
      "s_subb_u32 s55, s51, 0\n"
      "s_setpc_b64 s[54:55]\n"
      ".rept 160\n"
      "s_nop 1\n"
      ".endr\n"
      "L3_sb:\n"
      "s_barrier\n"
      "s_mov_b32 s59, s16\n"
      "s_mov_b32 s60, s28\n"
      "ds_read_b128 v[128:131], v123 offset:0\n"
      "ds_read_b128 v[132:135], v123 offset:1024\n"
      "ds_read_b128 v[136:139], v123 offset:2048\n"
      "ds_read_b128 v[140:143], v123 offset:3072\n"
      "ds_read_b128 v[144:147], v123 offset:4096\n"
      "ds_read_b128 v[148:151], v123 offset:5120\n"
      "ds_read_b128 v[152:155], v123 offset:6144\n"
      "ds_read_b128 v[156:159], v123 offset:7168\n"
      "s_sub_u32 s67, s63, 512\n"
      "s_mov_b32 s52, s17\n"
      "s_add_u32 s29, s29, 0x80000\n"
      "s_cmp_eq_u32 s29, 0x180000\n"
      "s_cselect_b32 s29, 0, s29\n"
      "s_add_u32 s63, s29, s14\n"
      "s_add_u32 s28, s28, 1\n"
      "s_and_b32 s31, s28, 3\n"
      "s_lshl_b32 s31, s31, 16\n"
      "s_add_u32 s31, s31, s12\n"
      "s_add_u32 s62, s31, s78\n"
      "s_add_u32 s31, s31, s25\n"
      "s_cmp_lt_u32 s28, 63\n"
      "s_cselect_b32 s54, 0x10000, 0\n"
      "s_cmp_eq_u32 s28, 31\n"
      "s_cselect_b32 s55, s24, 0\n"
      "s_add_u32 s84, s84, s54\n"
      "s_sub_u32 s84, s84, s55\n"
      "s_cmp_eq_u32 s28, 32\n"
      "s_cselect_b32 s55, s24, 0\n"
      "s_add_u32 s54, s55, 0x10000\n"
      "s_sub_u32 s55, 0x10000, s55\n"
      "s_add_u32 s15, s15, s54\n"
      "s_add_u32 s17, s17, s55\n"
      "s_and_b32 s54, s28, 15\n"
      "s_lshl_b32 s54, s54, 14\n"
      "s_add_u32 s16, s54, s21\n"
      "s_waitcnt vmcnt(5) lgkmcnt(2)\n"
      "ds_write_b128 v109, v[92:95] offset:55296\n"
      "ds_write_b128 v109, v[96:99] offset:56320\n"
      "s_waitcnt lgkmcnt(0)\n"
      "s_memrealtime s[68:69]\n"
      "v_mov_b64 v[32:33], 0\n"
      "v_mov_b64 v[34:35], 0\n"
      "v_mov_b64 v[36:37], 0\n"
      "v_mov_b64 v[38:39], 0\n"
      "v_mov_b64 v[40:41], 0\n"
      "s_memrealtime s[70:71]\n"
      "v_mov_b64 v[42:43], 0\n"
      "v_mov_b64 v[44:45], 0\n"
      "v_mov_b64 v[46:47], 0\n"
      "v_mov_b64 v[48:49], 0\n"
      "v_mov_b64 v[50:51], 0\n"
      "s_memrealtime s[72:73]\n"
      "v_mov_b64 v[52:53], 0\n"
      "v_mov_b64 v[54:55], 0\n"
      "s_nop 2\n"
      "s_memrealtime s[74:75]\n"
      "s_memtime s[42:43]\n"
      "s_waitcnt lgkmcnt(0)\n"
      "s_add_u32 s68, s68, s70\n"
      "s_add_u32 s72, s72, s74\n"
      "s_add_u32 s68, s68, s72\n"
      "s_mul_i32 s68, s68, 6\n"
      "s_sub_u32 s40, s68, s42\n"
      "s_cmp_lt_u32 s60, 8\n"
      "s_cselect_b32 s77, 1, 3\n"
      "s_waitcnt vmcnt(0)\n"
      "L3_m:\n"
      "v_subrev_u32 v8, s41, v16\n"
      "v_subrev_u32 v12, s41, v18\n"
      "v_xor_b32 v10, s60, v17\n"
      "v_xor_b32 v11, s60, v19\n"
      "v_max_i32 v8, v8, v12\n"
      "v_or_b32 v12, v10, v11\n"
      "v_and_b32 v12, 0xff, v12\n"
      "v_cmp_ne_u32 vcc, 0, v12\n"
      "v_max_u32 v10, v10, v11\n"
      "v_max_i32_dpp v8, v8, v8 row_shr:1 row_mask:0xf bank_mask:0xf\n"
      "s_nop 0\n"
      "v_max_u32_dpp v10, v10, v10 row_shr:1 row_mask:0xf bank_mask:0xf\n"
      "v_max_i32_dpp v8, v8, v8 row_shr:2 row_mask:0xf bank_mask:0xf\n"
      "s_nop 0\n"
      "v_max_u32_dpp v10, v10, v10 row_shr:2 row_mask:0xf bank_mask:0xf\n"
      "v_max_i32_dpp v8, v8, v8 row_shr:4 row_mask:0xf bank_mask:0xf\n"
      "s_nop 0\n"
      "v_max_u32_dpp v10, v10, v10 row_shr:4 row_mask:0xf bank_mask:0xf\n"
      "v_max_i32_dpp v8, v8, v8 row_shr:8 row_mask:0xf bank_mask:0xf\n"
      "s_nop 0\n"
      "v_max_u32_dpp v10, v10, v10 row_shr:8 row_mask:0xf bank_mask:0xf\n"
      "v_max_i32_dpp v8, v8, v8 row_bcast:15 row_mask:0xa bank_mask:0xf\n"
      "s_nop 0\n"
      "v_max_u32_dpp v10, v10, v10 row_bcast:15 row_mask:0xa bank_mask:0xf\n"
      "v_max_i32_dpp v8, v8, v8 row_bcast:31 row_mask:0xc bank_mask:0xf\n"
      "s_nop 0\n"
      "v_max_u32_dpp v10, v10, v10 row_bcast:31 row_mask:0xc bank_mask:0xf\n"
      "s_cbranch_vccnz L3_mr\n"
      "L3_mk:\n"
      "v_readlane_b32 s45, v8, 63\n"
      "v_readlane_b32 s47, v10, 63\n"
      "s_add_u32 s46, s45, s19\n"
      "s_cmp_gt_i32 s45, s53\n"
      "s_cselect_b32 s46, s46, 0\n"
      "s_add_u32 s41, s41, s46\n"
      "s_add_u32 s30, s41, s23\n"
      "s_lshr_b32 s47, s47, 8\n"
      "s_add_u32 s47, s47, s26\n"
      "s_sub_u32 s47, s47, s23\n"
      "s_add_u32 s45, s45, s81\n"
      "s_cmp_eq_u32 s58, 0\n"
      "s_cselect_b32 s71, 16, 200\n"
      "s_cmp_gt_i32 s47, 300\n"
      "s_cselect_b32 s47, s71, s47\n"
      "s_ashr_i32 s70, s47, s77\n"
      "s_max_i32 s47, s47, s70\n"
      "s_min_i32 s47, s47, 96\n"
      "s_cmp_gt_i32 s45, 300\n"
      "s_cselect_b32 s45, s71, s45\n"
      "s_ashr_i32 s70, s45, s77\n"
      "s_max_i32 s45, s45, s70\n"
      "s_min_i32 s45, s45, 48\n"
      "s_cmp_eq_u32 s58, 0\n"
      "s_cselect_b32 s70, 0x80000000, 0\n"
      "s_max_i32 s47, s47, s70\n"
      "s_max_i32 s45, s45, s70\n"
      "s_cmp_eq_u32 s60, 0\n"
      "s_cselect_b32 s47, 0, s47\n"
      "s_cselect_b32 s45, 0, s45\n"
      "s_cselect_b32 s70, 150, 0\n"
      "s_add_u32 s30, s30, s70\n"
      "s_add_u32 s23, s23, s47\n"
      "s_add_u32 s22, s22, s45\n"
      "s_mov_b32 s58, s46\n"
      "s_mov_b32 s19, s76\n"
      "s_memtime s[42:43]\n"
      "s_sub_u32 s47, s80, s41\n"
      "s_max_i32 s47, s47, 0\n"
      "s_min_i32 s47, s47, 0xfffff\n"
      "s_lshl_b32 s47, s47, 8\n"
      "s_add_u32 s41, s30, s22\n"
      "s_or_b32 s47, s47, s28\n"
      "v_mov_b32 v9, s47\n"
      "L3_remA:\n"
      "s_sub_u32 s44, s41, s40\n"
      "s_waitcnt lgkmcnt(0)\n"
      "s_sub_u32 s44, s44, s42\n"
      "s_max_i32 s44, s44, 0\n"
      "s_lshr_b32 s44, s44, 1\n"
      "s_and_b32 s44, s44, -4\n"
      "s_cmp_gt_i32 s44, 640\n"
      "s_cbranch_scc1 L3_longA\n"
      "s_sub_u32 s54, s48, s44\n"
      "s_subb_u32 s55, s49, 0\n"
      "s_setpc_b64 s[54:55]\n"
      "L3_longA:\n"
      "s_sleep 2\n"
      "s_memrealtime s[68:69]\n"
      "s_nop 4\n"
      "s_memrealtime s[70:71]\n"
      "s_nop 4\n"
      "s_memrealtime s[72:73]\n"
      "s_nop 4\n"
      "s_memrealtime s[74:75]\n"
      "s_memtime s[42:43]\n"
      "s_waitcnt lgkmcnt(0)\n"
      "s_add_u32 s68, s68, s70\n"
      "s_add_u32 s72, s72, s74\n"
      "s_add_u32 s68, s68, s72\n"
      "s_mul_i32 s68, s68, 6\n"
      "s_sub_u32 s40, s68, s42\n"
      "s_cmp_gt_i32 s44, 8000\n"
      "s_cselect_b32 s41, s68, s41\n"
      "s_memtime s[42:43]\n"
      "s_branch L3_remA\n"
      "L3_late:\n"
      "s_waitcnt vmcnt(0)\n"
      "L3_n:\n"
      "v_subrev_u32 v8, s41, v16\n"
      "v_subrev_u32 v12, s41, v18\n"
      "v_xor_b32 v10, s28, v17\n"
      "v_xor_b32 v11, s28, v19\n"
      "v_max_i32 v8, v8, v12\n"
      "v_or_b32 v12, v10, v11\n"
      "v_and_b32 v12, 0xff, v12\n"
      "v_cmp_ne_u32 vcc, 0, v12\n"
      "v_max_u32 v10, v10, v11\n"
      "v_max_i32_dpp v8, v8, v8 row_shr:1 row_mask:0xf bank_mask:0xf\n"
      "s_nop 0\n"
      "v_max_u32_dpp v10, v10, v10 row_shr:1 row_mask:0xf bank_mask:0xf\n"
      "v_max_i32_dpp v8, v8, v8 row_shr:2 row_mask:0xf bank_mask:0xf\n"
      "s_nop 0\n"
      "v_max_u32_dpp v10, v10, v10 row_shr:2 row_mask:0xf bank_mask:0xf\n"
      "v_max_i32_dpp v8, v8, v8 row_shr:4 row_mask:0xf bank_mask:0xf\n"
      "s_nop 0\n"
      "v_max_u32_dpp v10, v10, v10 row_shr:4 row_mask:0xf bank_mask:0xf\n"
      "v_max_i32_dpp v8, v8, v8 row_shr:8 row_mask:0xf bank_mask:0xf\n"
      "s_nop 0\n"
      "v_max_u32_dpp v10, v10, v10 row_shr:8 row_mask:0xf bank_mask:0xf\n"
      "v_max_i32_dpp v8, v8, v8 row_bcast:15 row_mask:0xa bank_mask:0xf\n"
      "s_nop 0\n"
      "v_max_u32_dpp v10, v10, v10 row_bcast:15 row_mask:0xa bank_mask:0xf\n"
      "v_max_i32_dpp v8, v8, v8 row_bcast:31 row_mask:0xc bank_mask:0xf\n"
      "s_nop 0\n"
      "v_max_u32_dpp v10, v10, v10 row_bcast:31 row_mask:0xc bank_mask:0xf\n"
      "s_cbranch_vccnz L3_nr\n"
      "L3_nk:\n"
      "v_readlane_b32 s45, v8, 63\n"
      "v_readlane_b32 s47, v10, 63\n"
      "s_add_u32 s46, s45, s19\n"
      "s_cmp_gt_i32 s45, s53\n"
      "s_cselect_b32 s46, s46, 0\n"
      "s_add_u32 s30, s30, s46\n"
      "s_add_u32 s30, s30, 300\n"
      "L3_longB:\n"
      "s_sleep 1\n"
      "s_memrealtime s[68:69]\n"
      "s_nop 4\n"
      "s_memrealtime s[70:71]\n"
      "s_nop 4\n"
      "s_memrealtime s[72:73]\n"
      "s_nop 4\n"
      "s_memrealtime s[74:75]\n"
      "s_memtime s[42:43]\n"
      "s_waitcnt lgkmcnt(0)\n"
      "s_add_u32 s68, s68, s70\n"
      "s_add_u32 s72, s72, s74\n"
      "s_add_u32 s68, s68, s72\n"
      "s_mul_i32 s68, s68, 6\n"
      "s_sub_u32 s44, s30, s68\n"
      "s_cmp_gt_i32 s44, 16000\n"
      "s_cselect_b32 s44, 0, s44\n"
      "s_max_i32 s44, s44, 0\n"
      "s_lshr_b32 s44, s44, 1\n"
      "s_and_b32 s44, s44, -4\n"
      "s_cmp_gt_i32 s44, 640\n"
      "s_cbranch_scc1 L3_longB\n"
      "s_branch L3_goB\n"
      "L3_mr:\n"
      "s_sub_u32 s64, s64, 1\n"
      "s_cbranch_scc1 L3_mg\n"
      "buffer_load_dwordx2 v[16:17], v119, s[8:11], s59 offen offset:0 sc1\n"
      "buffer_load_dwordx2 v[18:19], v119, s[8:11], s59 offen offset:2048 sc1\n"
      "s_waitcnt vmcnt(0)\n"
      "s_branch L3_m\n"
      "L3_mg:\n"
      "s_mov_b32 s64, 0\n"
      "s_branch L3_mk\n"
      "L3_nr:\n"
      "s_sub_u32 s64, s64, 1\n"
      "s_cbranch_scc1 L3_ng\n"
      "buffer_load_dwordx2 v[16:17], v119, s[8:11], s16 offen offset:0 sc1\n"
      "buffer_load_dwordx2 v[18:19], v119, s[8:11], s16 offen offset:2048 sc1\n"
      "s_waitcnt vmcnt(0)\n"
      "s_branch L3_n\n"
      "L3_ng:\n"
      "s_mov_b32 s64, 0\n"
      "s_branch L3_nk\n"
      "L3_last:\n"
      "s_sub_u32 s67, s63, 512\n"
      "s_mov_b32 s52, s17\n"
      "v_mov_b32 v64, v70\n"
      "v_alignbit_b32 v65, v71, v71, 16\n"
      "v_mov_b32 v66, -1\n"
      "v_mov_b32 v67, -1\n"
      "v_mov_b32 v68, s52\n"
      "v_mov_b32 v69, s67\n"
      "v_permlane32_swap_b32 v64, v66\n"
      "v_permlane32_swap_b32 v65, v67\n"
      "v_mov_b32_dpp v68, v69 quad_perm:[0,1,2,3] row_mask:0xc bank_mask:0xf\n"
      "v_add_u32 v68, v68, v109\n"
      "buffer_store_dwordx4 v[64:67], v68, s[8:11], 0 offen sc1\n"
      "s_waitcnt vmcnt(4)\n"
      "s_barrier\n"
      "s_waitcnt vmcnt(0) lgkmcnt(0)\n"
      "s_barrier\n"
      "s_branch Lend\n"
      "L3_exact:\n"
      "s_nop 7\n"
      "v_max3_f32 v36, v32, v33, v34\n"
      "v_max3_f32 v36, v36, v35, v68\n"
      "s_nop 1\n"
      "v_max_f32_dpp v36, v36, v36 row_ror:8 row_mask:0xf bank_mask:0xf\n"
      "v_mov_b32 v37, v36\n"
      "s_nop 1\n"
      "v_permlane16_swap_b32 v36, v37\n"
      "v_max_f32 v36, v36, v37\n"
      "v_mov_b32 v37, v36\n"
      "s_nop 1\n"
      "v_permlane32_swap_b32 v36, v37\n"
      "v_max_f32 v36, v36, v37\n"
      "v_mov_b32 v41, v36\n"
      "v_sub_f32 v36, v32, v41\n"
      "v_sub_f32 v37, v33, v41\n"
      "v_sub_f32 v38, v34, v41\n"
      "v_sub_f32 v39, v35, v41\n"
      "v_sub_f32 v69, v68, v41\n"
      "v_exp_f32 v36, v36\n"
      "v_exp_f32 v37, v37\n"
      "v_exp_f32 v38, v38\n"
      "v_exp_f32 v39, v39\n"
      "v_exp_f32 v69, v69\n"
      "v_cvt_pk_bf16_f32 v64, v36, v37\n"
      "v_cvt_pk_bf16_f32 v65, v38, v39\n"
      "s_nop 0\n"
      "v_mov_b32_dpp v66, v64 row_ror:8 row_mask:0xf bank_mask:0xf\n"
      "v_mov_b32_dpp v67, v65 row_ror:8 row_mask:0xf bank_mask:0xf\n"
      "v_mov_b32_dpp v40, v69 row_ror:8 row_mask:0xf bank_mask:0xf\n"
      "v_cvt_pk_bf16_f32 v40, v69, v40\n"
      "v_cndmask_b32_e64 v44, v40, 0, s[38:39]\n"
      "v_cndmask_b32_e64 v45, 0, v40, s[38:39]\n"
      "v_mfma_f32_16x16x32_bf16 v[0:3], v[104:107], v[64:67], 0\n"
      "s_nop 0\n"
      "v_mfma_f32_16x16x32_bf16 v[0:3], v[104:107], v[44:47], v[0:3]\n"
      "v_mfma_f32_16x16x32_bf16 v[4:7], v[70:73], v[44:47], 0\n"
      "v_mfma_f32_16x16x32_bf16 v[24:27], v[80:83], v[64:67], v[4:7]\n"
      "v_mfma_f32_16x16x32_bf16 v[28:31], v[84:87], v[64:67], v[4:7]\n"
      "s_nop 4\n"
      "v_rcp_f32 v12, v0\n"
      "v_cndmask_b32 v13, v125, v111, vcc\n"
      "v_mul_f32 v8, v24, v12\n"
      "v_mul_f32 v9, v25, v12\n"
      "v_mul_f32 v10, v26, v12\n"
      "v_mul_f32 v11, v27, v12\n"
      "v_mul_f32_dpp v8, v28, v12 quad_perm:[0,1,2,3] row_mask:0xf bank_mask:0xc\n"
      "v_mul_f32_dpp v9, v29, v12 quad_perm:[0,1,2,3] row_mask:0xf bank_mask:0xc\n"
      "v_mul_f32_dpp v10, v30, v12 quad_perm:[0,1,2,3] row_mask:0xf bank_mask:0xc\n"
      "v_mul_f32_dpp v11, v31, v12 quad_perm:[0,1,2,3] row_mask:0xf bank_mask:0xc\n"
      "v_cvt_pk_bf16_f32 v14, v8, v9\n"
      "v_cvt_pk_bf16_f32 v15, v10, v11\n"
      "buffer_store_dwordx2 v[14:15], v13, s[8:11], s63 offen sc1\n"
      "s_waitcnt vmcnt(1)\n"
      "s_branch L3_out\n"
      "L3_retryA:\n"
      "s_sub_u32 s64, s64, 1\n"
      "s_cbranch_scc1 L3_giveA\n"
      "s_waitcnt lgkmcnt(0)\n"
      "s_nop 7\n"
      "buffer_load_dwordx4 v[0:3], v124, s[8:11], s31 offen sc1\n"
      "buffer_load_dwordx4 v[4:7], v124, s[8:11], s62 offen sc1\n"
      "v_mov_b64 v[32:33], 0\n"
      "v_mov_b64 v[34:35], 0\n"
      "v_mov_b64 v[36:37], 0\n"
      "v_mov_b64 v[38:39], 0\n"
      "v_mov_b64 v[40:41], 0\n"
      "v_mov_b64 v[42:43], 0\n"
      "v_mov_b64 v[44:45], 0\n"
      "v_mov_b64 v[46:47], 0\n"
      "s_waitcnt vmcnt(0)\n"
      "s_branch L3_wrA\n"
      "L3_giveA:\n"
      "s_mov_b32 s64, 0\n"
      "s_branch L3_okA\n"
      "Lfin:\n"
      "s_branch Lend\n"
      "LP0_init:\n"
      "s_mov_b32 s31, s15\n"
      "s_mov_b32 s62, 0x100000\n"
      "s_mov_b32 s28, 0x0\n"
      "buffer_load_dwordx4 a[0:3], v88, s[68:71], s28 offen\n"
      "s_mov_b32 s28, 0x20000\n"
      "buffer_load_dwordx4 a[4:7], v88, s[68:71], s28 offen\n"
      "s_mov_b32 s28, 0x8000\n"
      "buffer_load_dwordx4 a[8:11], v88, s[68:71], s28 offen\n"
      "s_mov_b32 s28, 0x28000\n"
      "buffer_load_dwordx4 a[12:15], v88, s[68:71], s28 offen\n"
      "s_mov_b32 s28, 0x40000\n"
      "buffer_load_dwordx4 a[16:19], v88, s[68:71], s28 offen\n"
      "s_mov_b32 s28, 0x60000\n"
      "buffer_load_dwordx4 a[20:23], v88, s[68:71], s28 offen\n"
      "s_mov_b32 s28, 0x48000\n"
      "buffer_load_dwordx4 a[24:27], v88, s[68:71], s28 offen\n"
      "s_mov_b32 s28, 0x68000\n"
      "buffer_load_dwordx4 a[28:31], v88, s[68:71], s28 offen\n"
      "s_mov_b32 s28, 0x80000\n"
      "buffer_load_dwordx4 a[32:35], v88, s[68:71], s28 offen\n"
      "s_mov_b32 s28, 0xa0000\n"
      "buffer_load_dwordx4 a[36:39], v88, s[68:71], s28 offen\n"
      "s_mov_b32 s28, 0x88000\n"
      "buffer_load_dwordx4 a[40:43], v88, s[68:71], s28 offen\n"
      "s_mov_b32 s28, 0xa8000\n"
      "buffer_load_dwordx4 a[44:47], v88, s[68:71], s28 offen\n"
      "s_mov_b32 s28, 0xc0000\n"
      "buffer_load_dwordx4 a[48:51], v88, s[68:71], s28 offen\n"
      "s_mov_b32 s28, 0xe0000\n"
      "buffer_load_dwordx4 a[52:55], v88, s[68:71], s28 offen\n"
      "s_mov_b32 s28, 0xc8000\n"
      "buffer_load_dwordx4 a[56:59], v88, s[68:71], s28 offen\n"
      "s_mov_b32 s28, 0xe8000\n"
      "buffer_load_dwordx4 a[60:63], v88, s[68:71], s28 offen\n"
      "s_waitcnt vmcnt(12)\n"
      "s_waitcnt lgkmcnt(0)\n"
      "s_add_u32 s85, s85, 1\n"
      "ds_write_b128 v89, a[0:3] offset:0\n"
      "ds_write_b128 v89, a[4:7] offset:1280\n"
      "ds_write_b128 v89, a[8:11] offset:2560\n"
      "ds_write_b128 v89, a[12:15] offset:3840\n"
      "s_mov_b32 s28, 0x100000\n"
      "buffer_load_dwordx4 a[64:67], v88, s[68:71], s28 offen\n"
      "s_mov_b32 s28, 0x120000\n"
      "buffer_load_dwordx4 a[68:71], v88, s[68:71], s28 offen\n"
      "s_mov_b32 s28, 0x108000\n"
      "buffer_load_dwordx4 a[72:75], v88, s[68:71], s28 offen\n"
      "s_mov_b32 s28, 0x128000\n"
      "buffer_load_dwordx4 a[76:79], v88, s[68:71], s28 offen\n"
      "ds_read_b64_tr_b16 a[0:1], v90 offset:0\n"
      "ds_read_b64_tr_b16 a[2:3], v90 offset:2560\n"
      "ds_read_b64_tr_b16 a[4:5], v90 offset:32\n"
      "ds_read_b64_tr_b16 a[6:7], v90 offset:2592\n"
      "ds_read_b64_tr_b16 a[8:9], v90 offset:64\n"
      "ds_read_b64_tr_b16 a[10:11], v90 offset:2624\n"
      "ds_read_b64_tr_b16 a[12:13], v90 offset:96\n"
      "ds_read_b64_tr_b16 a[14:15], v90 offset:2656\n"
      "s_waitcnt lgkmcnt(2)\n"
      "s_waitcnt vmcnt(12)\n"
      "ds_write_b128 v89, a[16:19] offset:0\n"
      "ds_write_b128 v89, a[20:23] offset:1280\n"
      "ds_write_b128 v89, a[24:27] offset:2560\n"
      "ds_write_b128 v89, a[28:31] offset:3840\n"
      "s_mov_b32 s28, 0x140000\n"
      "buffer_load_dwordx4 a[80:83], v88, s[68:71], s28 offen\n"
      "s_mov_b32 s28, 0x160000\n"
      "buffer_load_dwordx4 a[84:87], v88, s[68:71], s28 offen\n"
      "s_mov_b32 s28, 0x148000\n"
      "buffer_load_dwordx4 a[88:91], v88, s[68:71], s28 offen\n"
      "s_mov_b32 s28, 0x168000\n"
      "buffer_load_dwordx4 a[92:95], v88, s[68:71], s28 offen\n"
      "ds_read_b64_tr_b16 a[16:17], v90 offset:0\n"
      "ds_read_b64_tr_b16 a[18:19], v90 offset:2560\n"
      "ds_read_b64_tr_b16 a[20:21], v90 offset:32\n"
      "ds_read_b64_tr_b16 a[22:23], v90 offset:2592\n"
      "ds_read_b64_tr_b16 a[24:25], v90 offset:64\n"
      "ds_read_b64_tr_b16 a[26:27], v90 offset:2624\n"
      "ds_read_b64_tr_b16 a[28:29], v90 offset:96\n"
      "ds_read_b64_tr_b16 a[30:31], v90 offset:2656\n"
      "s_waitcnt lgkmcnt(2)\n"
      "s_waitcnt vmcnt(12)\n"
      "ds_write_b128 v89, a[32:35] offset:0\n"
      "ds_write_b128 v89, a[36:39] offset:1280\n"
      "ds_write_b128 v89, a[40:43] offset:2560\n"
      "ds_write_b128 v89, a[44:47] offset:3840\n"
      "s_mov_b32 s28, 0x180000\n"
      "buffer_load_dwordx4 a[96:99], v88, s[68:71], s28 offen\n"
      "s_mov_b32 s28, 0x1a0000\n"
      "buffer_load_dwordx4 a[100:103], v88, s[68:71], s28 offen\n"
      "s_mov_b32 s28, 0x188000\n"
      "buffer_load_dwordx4 a[104:107], v88, s[68:71], s28 offen\n"
      "s_mov_b32 s28, 0x1a8000\n"
      "buffer_load_dwordx4 a[108:111], v88, s[68:71], s28 offen\n"
      "ds_read_b64_tr_b16 a[32:33], v90 offset:0\n"
      "ds_read_b64_tr_b16 a[34:35], v90 offset:2560\n"
      "ds_read_b64_tr_b16 a[36:37], v90 offset:32\n"
      "ds_read_b64_tr_b16 a[38:39], v90 offset:2592\n"
      "ds_read_b64_tr_b16 a[40:41], v90 offset:64\n"
      "ds_read_b64_tr_b16 a[42:43], v90 offset:2624\n"
      "ds_read_b64_tr_b16 a[44:45], v90 offset:96\n"
      "ds_read_b64_tr_b16 a[46:47], v90 offset:2656\n"
      "s_waitcnt lgkmcnt(2)\n"
      "s_waitcnt vmcnt(12)\n"
      "ds_write_b128 v89, a[48:51] offset:0\n"
      "ds_write_b128 v89, a[52:55] offset:1280\n"
      "ds_write_b128 v89, a[56:59] offset:2560\n"
      "ds_write_b128 v89, a[60:63] offset:3840\n"
      "s_mov_b32 s28, 0x1c0000\n"
      "buffer_load_dwordx4 a[112:115], v88, s[68:71], s28 offen\n"
      "s_mov_b32 s28, 0x1e0000\n"
      "buffer_load_dwordx4 a[116:119], v88, s[68:71], s28 offen\n"
      "s_mov_b32 s28, 0x1c8000\n"
      "buffer_load_dwordx4 a[120:123], v88, s[68:71], s28 offen\n"
      "s_mov_b32 s28, 0x1e8000\n"
      "buffer_load_dwordx4 a[124:127], v88, s[68:71], s28 offen\n"
      "ds_read_b64_tr_b16 a[48:49], v90 offset:0\n"
      "ds_read_b64_tr_b16 a[50:51], v90 offset:2560\n"
      "ds_read_b64_tr_b16 a[52:53], v90 offset:32\n"
      "ds_read_b64_tr_b16 a[54:55], v90 offset:2592\n"
      "ds_read_b64_tr_b16 a[56:57], v90 offset:64\n"
      "ds_read_b64_tr_b16 a[58:59], v90 offset:2624\n"
      "ds_read_b64_tr_b16 a[60:61], v90 offset:96\n"
      "ds_read_b64_tr_b16 a[62:63], v90 offset:2656\n"
      "s_waitcnt lgkmcnt(2)\n"
      "s_waitcnt vmcnt(12)\n"
      "ds_write_b128 v89, a[64:67] offset:0\n"
      "ds_write_b128 v89, a[68:71] offset:1280\n"
      "ds_write_b128 v89, a[72:75] offset:2560\n"
      "ds_write_b128 v89, a[76:79] offset:3840\n"
      "s_mov_b32 s28, 0x200000\n"
      "buffer_load_dwordx4 a[128:131], v88, s[68:71], s28 offen\n"
      "s_mov_b32 s28, 0x220000\n"
      "buffer_load_dwordx4 a[132:135], v88, s[68:71], s28 offen\n"
      "s_mov_b32 s28, 0x208000\n"
      "buffer_load_dwordx4 a[136:139], v88, s[68:71], s28 offen\n"
      "s_mov_b32 s28, 0x228000\n"
      "buffer_load_dwordx4 a[140:143], v88, s[68:71], s28 offen\n"
      "buffer_load_dwordx2 v[0:1], v100, s[8:11], 0 offen sc1\n"
      "ds_read_b64_tr_b16 a[64:65], v90 offset:0\n"
      "ds_read_b64_tr_b16 a[66:67], v90 offset:2560\n"
      "ds_read_b64_tr_b16 a[68:69], v90 offset:32\n"
      "ds_read_b64_tr_b16 a[70:71], v90 offset:2592\n"
      "ds_read_b64_tr_b16 a[72:73], v90 offset:64\n"
      "ds_read_b64_tr_b16 a[74:75], v90 offset:2624\n"
      "ds_read_b64_tr_b16 a[76:77], v90 offset:96\n"
      "ds_read_b64_tr_b16 a[78:79], v90 offset:2656\n"
      "s_waitcnt lgkmcnt(2)\n"
      "s_waitcnt vmcnt(13)\n"
      "ds_write_b128 v89, a[80:83] offset:0\n"
      "ds_write_b128 v89, a[84:87] offset:1280\n"
      "ds_write_b128 v89, a[88:91] offset:2560\n"
      "ds_write_b128 v89, a[92:95] offset:3840\n"
      "s_mov_b32 s28, 0x240000\n"
      "buffer_load_dwordx4 a[144:147], v88, s[68:71], s28 offen\n"
      "s_mov_b32 s28, 0x260000\n"
      "buffer_load_dwordx4 a[148:151], v88, s[68:71], s28 offen\n"
      "s_mov_b32 s28, 0x248000\n"
      "buffer_load_dwordx4 a[152:155], v88, s[68:71], s28 offen\n"
      "s_mov_b32 s28, 0x268000\n"
      "buffer_load_dwordx4 a[156:159], v88, s[68:71], s28 offen\n"
      "ds_read_b64_tr_b16 a[80:81], v90 offset:0\n"
      "ds_read_b64_tr_b16 a[82:83], v90 offset:2560\n"
      "ds_read_b64_tr_b16 a[84:85], v90 offset:32\n"
      "ds_read_b64_tr_b16 a[86:87], v90 offset:2592\n"
      "ds_read_b64_tr_b16 a[88:89], v90 offset:64\n"
      "ds_read_b64_tr_b16 a[90:91], v90 offset:2624\n"
      "ds_read_b64_tr_b16 a[92:93], v90 offset:96\n"
      "ds_read_b64_tr_b16 a[94:95], v90 offset:2656\n"
      "s_waitcnt lgkmcnt(2)\n"
      "s_waitcnt vmcnt(13)\n"
      "ds_write_b128 v89, a[96:99] offset:0\n"
      "ds_write_b128 v89, a[100:103] offset:1280\n"
      "ds_write_b128 v89, a[104:107] offset:2560\n"
      "ds_write_b128 v89, a[108:111] offset:3840\n"
      "s_mov_b32 s28, 0x280000\n"
      "buffer_load_dwordx4 a[160:163], v88, s[68:71], s28 offen\n"
      "s_mov_b32 s28, 0x2a0000\n"
      "buffer_load_dwordx4 a[164:167], v88, s[68:71], s28 offen\n"
      "s_mov_b32 s28, 0x288000\n"
      "buffer_load_dwordx4 a[168:171], v88, s[68:71], s28 offen\n"
      "s_mov_b32 s28, 0x2a8000\n"
      "buffer_load_dwordx4 a[172:175], v88, s[68:71], s28 offen\n"
      "ds_read_b64_tr_b16 a[96:97], v90 offset:0\n"
      "ds_read_b64_tr_b16 a[98:99], v90 offset:2560\n"
      "ds_read_b64_tr_b16 a[100:101], v90 offset:32\n"
      "ds_read_b64_tr_b16 a[102:103], v90 offset:2592\n"
      "ds_read_b64_tr_b16 a[104:105], v90 offset:64\n"
      "ds_read_b64_tr_b16 a[106:107], v90 offset:2624\n"
      "ds_read_b64_tr_b16 a[108:109], v90 offset:96\n"
      "ds_read_b64_tr_b16 a[110:111], v90 offset:2656\n"
      "s_waitcnt lgkmcnt(2)\n"
      "s_waitcnt vmcnt(13)\n"
      "ds_write_b128 v89, a[112:115] offset:0\n"
      "ds_write_b128 v89, a[116:119] offset:1280\n"
      "ds_write_b128 v89, a[120:123] offset:2560\n"
      "ds_write_b128 v89, a[124:127] offset:3840\n"
      "s_mov_b32 s28, 0x2c0000\n"
      "buffer_load_dwordx4 a[176:179], v88, s[68:71], s28 offen\n"
      "s_mov_b32 s28, 0x2e0000\n"
      "buffer_load_dwordx4 a[180:183], v88, s[68:71], s28 offen\n"
      "s_mov_b32 s28, 0x2c8000\n"
      "buffer_load_dwordx4 a[184:187], v88, s[68:71], s28 offen\n"
      "s_mov_b32 s28, 0x2e8000\n"
      "buffer_load_dwordx4 a[188:191], v88, s[68:71], s28 offen\n"
      "ds_read_b64_tr_b16 a[112:113], v90 offset:0\n"
      "ds_read_b64_tr_b16 a[114:115], v90 offset:2560\n"
      "ds_read_b64_tr_b16 a[116:117], v90 offset:32\n"
      "ds_read_b64_tr_b16 a[118:119], v90 offset:2592\n"
      "ds_read_b64_tr_b16 a[120:121], v90 offset:64\n"
      "ds_read_b64_tr_b16 a[122:123], v90 offset:2624\n"
      "ds_read_b64_tr_b16 a[124:125], v90 offset:96\n"
      "ds_read_b64_tr_b16 a[126:127], v90 offset:2656\n"
      "s_waitcnt lgkmcnt(2)\n"
      "s_waitcnt vmcnt(13)\n"
      "ds_write_b128 v89, a[128:131] offset:0\n"
      "ds_write_b128 v89, a[132:135] offset:1280\n"
      "ds_write_b128 v89, a[136:139] offset:2560\n"
      "ds_write_b128 v89, a[140:143] offset:3840\n"
      "s_mov_b32 s28, 0x300000\n"
      "buffer_load_dwordx4 a[192:195], v88, s[68:71], s28 offen\n"
      "s_mov_b32 s28, 0x320000\n"
      "buffer_load_dwordx4 a[196:199], v88, s[68:71], s28 offen\n"
      "s_mov_b32 s28, 0x308000\n"
      "buffer_load_dwordx4 a[200:203], v88, s[68:71], s28 offen\n"
      "s_mov_b32 s28, 0x328000\n"
      "buffer_load_dwordx4 a[204:207], v88, s[68:71], s28 offen\n"
      "ds_read_b64_tr_b16 a[128:129], v90 offset:0\n"
      "ds_read_b64_tr_b16 a[130:131], v90 offset:2560\n"
      "ds_read_b64_tr_b16 a[132:133], v90 offset:32\n"
      "ds_read_b64_tr_b16 a[134:135], v90 offset:2592\n"
      "ds_read_b64_tr_b16 a[136:137], v90 offset:64\n"
      "ds_read_b64_tr_b16 a[138:139], v90 offset:2624\n"
      "ds_read_b64_tr_b16 a[140:141], v90 offset:96\n"
      "ds_read_b64_tr_b16 a[142:143], v90 offset:2656\n"
      "s_waitcnt lgkmcnt(2)\n"
      "s_waitcnt vmcnt(12)\n"
      "ds_write_b128 v89, a[144:147] offset:0\n"
      "ds_write_b128 v89, a[148:151] offset:1280\n"
      "ds_write_b128 v89, a[152:155] offset:2560\n"
      "ds_write_b128 v89, a[156:159] offset:3840\n"
      "s_mov_b32 s28, 0x340000\n"
      "buffer_load_dwordx4 a[208:211], v88, s[68:71], s28 offen\n"
      "s_mov_b32 s28, 0x360000\n"
      "buffer_load_dwordx4 a[212:215], v88, s[68:71], s28 offen\n"
      "s_mov_b32 s28, 0x348000\n"
      "buffer_load_dwordx4 a[216:219], v88, s[68:71], s28 offen\n"
      "s_mov_b32 s28, 0x368000\n"
      "buffer_load_dwordx4 a[220:223], v88, s[68:71], s28 offen\n"
      "s_branch LP0_lnc\n"
      "LP0_ln:\n"
      "buffer_load_dwordx2 v[0:1], v100, s[8:11], 0 offen offset:0 sc1\n"
      "s_sub_u32 s62, s62, 1\n"
      "s_cbranch_scc1 LP0_lng\n"
      "s_waitcnt vmcnt(0)\n"
      "LP0_lnc:\n"
      "v_cmp_ne_u32_e64 s[54:55], s85, v1\n"
      "v_cmp_ge_i32 vcc, -1, v0\n"
      "s_or_b64 vcc, vcc, s[54:55]\n"
      "s_cbranch_vccz LP0_lnk\n"
      "s_sleep 3\n"
      "s_branch LP0_ln\n"
      "LP0_lng:\n"
      "s_mov_b32 s62, 0\n"
      "s_waitcnt vmcnt(0)\n"
      "LP0_lnk:\n"
      "s_mov_b32 m0, s31\n"
      "s_add_u32 s29, s12, 4096\n"
      "buffer_load_dwordx4 v91, s[8:11], s12 offen sc1 lds\n"
      "v_readlane_b32 s55, v0, 63\n"
      "s_and_b32 s55, s55, 1\n"
      "s_lshl_b32 s55, s55, 21\n"
      "s_add_u32 s13, s13, s55\n"
      "buffer_load_dwordx4 v91, s[8:11], s12 offen offset:1024 sc1 lds\n"
      "buffer_load_dwordx4 v91, s[8:11], s12 offen offset:2048 sc1 lds\n"
      "buffer_load_dwordx4 v91, s[8:11], s12 offen offset:3072 sc1 lds\n"
      "s_add_u32 m0, s31, 4096\n"
      "s_nop 0\n"
      "buffer_load_dwordx4 v91, s[8:11], s29 offen sc1 lds\n"
      "buffer_load_dwordx4 v91, s[8:11], s29 offen offset:1024 sc1 lds\n"
      "buffer_load_dwordx4 v91, s[8:11], s29 offen offset:2048 sc1 lds\n"
      "buffer_load_dwordx4 v91, s[8:11], s29 offen offset:3072 sc1 lds\n"
      "ds_read_b64_tr_b16 a[144:145], v90 offset:0\n"
      "ds_read_b64_tr_b16 a[146:147], v90 offset:2560\n"
      "ds_read_b64_tr_b16 a[148:149], v90 offset:32\n"
      "ds_read_b64_tr_b16 a[150:151], v90 offset:2592\n"
      "ds_read_b64_tr_b16 a[152:153], v90 offset:64\n"
      "ds_read_b64_tr_b16 a[154:155], v90 offset:2624\n"
      "ds_read_b64_tr_b16 a[156:157], v90 offset:96\n"
      "ds_read_b64_tr_b16 a[158:159], v90 offset:2656\n"
      "s_waitcnt lgkmcnt(2)\n"
      "s_waitcnt vmcnt(20)\n"
      "ds_write_b128 v89, a[160:163] offset:0\n"
      "ds_write_b128 v89, a[164:167] offset:1280\n"
      "ds_write_b128 v89, a[168:171] offset:2560\n"
      "ds_write_b128 v89, a[172:175] offset:3840\n"
      "s_mov_b32 s28, 0x380000\n"
      "buffer_load_dwordx4 a[224:227], v88, s[68:71], s28 offen\n"
      "s_mov_b32 s28, 0x3a0000\n"
      "buffer_load_dwordx4 a[228:231], v88, s[68:71], s28 offen\n"
      "s_mov_b32 s28, 0x388000\n"
      "buffer_load_dwordx4 a[232:235], v88, s[68:71], s28 offen\n"
      "s_mov_b32 s28, 0x3a8000\n"
      "buffer_load_dwordx4 a[236:239], v88, s[68:71], s28 offen\n"
      "ds_read_b64_tr_b16 a[160:161], v90 offset:0\n"
      "ds_read_b64_tr_b16 a[162:163], v90 offset:2560\n"
      "ds_read_b64_tr_b16 a[164:165], v90 offset:32\n"
      "ds_read_b64_tr_b16 a[166:167], v90 offset:2592\n"
      "ds_read_b64_tr_b16 a[168:169], v90 offset:64\n"
      "ds_read_b64_tr_b16 a[170:171], v90 offset:2624\n"
      "ds_read_b64_tr_b16 a[172:173], v90 offset:96\n"
      "ds_read_b64_tr_b16 a[174:175], v90 offset:2656\n"
      "s_waitcnt lgkmcnt(2)\n"
      "s_waitcnt vmcnt(20)\n"
      "ds_write_b128 v89, a[176:179] offset:0\n"
      "ds_write_b128 v89, a[180:183] offset:1280\n"
      "ds_write_b128 v89, a[184:187] offset:2560\n"
      "ds_write_b128 v89, a[188:191] offset:3840\n"
      "s_mov_b32 s28, 0x3c0000\n"
      "buffer_load_dwordx4 a[240:243], v88, s[68:71], s28 offen\n"
      "s_mov_b32 s28, 0x3e0000\n"
      "buffer_load_dwordx4 a[244:247], v88, s[68:71], s28 offen\n"
      "s_mov_b32 s28, 0x3c8000\n"
      "buffer_load_dwordx4 a[248:251], v88, s[68:71], s28 offen\n"
      "s_mov_b32 s28, 0x3e8000\n"
      "buffer_load_dwordx4 a[252:255], v88, s[68:71], s28 offen\n"
      "s_add_u32 s12, s12, 0x8000\n"
      "s_add_u32 s31, s31, 0x8000\n"
      "s_mov_b32 m0, s31\n"
      "s_add_u32 s29, s12, 4096\n"
      "buffer_load_dwordx4 v91, s[8:11], s12 offen sc1 lds\n"
      "buffer_load_dwordx4 v91, s[8:11], s12 offen offset:1024 sc1 lds\n"
      "buffer_load_dwordx4 v91, s[8:11], s12 offen offset:2048 sc1 lds\n"
      "buffer_load_dwordx4 v91, s[8:11], s12 offen offset:3072 sc1 lds\n"
      "s_add_u32 m0, s31, 4096\n"
      "s_nop 0\n"
      "buffer_load_dwordx4 v91, s[8:11], s29 offen sc1 lds\n"
      "buffer_load_dwordx4 v91, s[8:11], s29 offen offset:1024 sc1 lds\n"
      "buffer_load_dwordx4 v91, s[8:11], s29 offen offset:2048 sc1 lds\n"
      "buffer_load_dwordx4 v91, s[8:11], s29 offen offset:3072 sc1 lds\n"
      "ds_read_b64_tr_b16 a[176:177], v90 offset:0\n"
      "ds_read_b64_tr_b16 a[178:179], v90 offset:2560\n"
      "ds_read_b64_tr_b16 a[180:181], v90 offset:32\n"
      "ds_read_b64_tr_b16 a[182:183], v90 offset:2592\n"
      "ds_read_b64_tr_b16 a[184:185], v90 offset:64\n"
      "ds_read_b64_tr_b16 a[186:187], v90 offset:2624\n"
      "ds_read_b64_tr_b16 a[188:189], v90 offset:96\n"
      "ds_read_b64_tr_b16 a[190:191], v90 offset:2656\n"
      "s_waitcnt lgkmcnt(2)\n"
      "s_waitcnt vmcnt(28)\n"
      "ds_write_b128 v89, a[192:195] offset:0\n"
      "ds_write_b128 v89, a[196:199] offset:1280\n"
      "ds_write_b128 v89, a[200:203] offset:2560\n"
      "ds_write_b128 v89, a[204:207] offset:3840\n"
      "ds_read_b64_tr_b16 a[192:193], v90 offset:0\n"
      "ds_read_b64_tr_b16 a[194:195], v90 offset:2560\n"
      "ds_read_b64_tr_b16 a[196:197], v90 offset:32\n"
      "ds_read_b64_tr_b16 a[198:199], v90 offset:2592\n"
      "ds_read_b64_tr_b16 a[200:201], v90 offset:64\n"
      "ds_read_b64_tr_b16 a[202:203], v90 offset:2624\n"
      "ds_read_b64_tr_b16 a[204:205], v90 offset:96\n"
      "ds_read_b64_tr_b16 a[206:207], v90 offset:2656\n"
      "s_waitcnt lgkmcnt(2)\n"
      "s_waitcnt vmcnt(24)\n"
      "ds_write_b128 v89, a[208:211] offset:0\n"
      "ds_write_b128 v89, a[212:215] offset:1280\n"
      "ds_write_b128 v89, a[216:219] offset:2560\n"
      "ds_write_b128 v89, a[220:223] offset:3840\n"
      "ds_read_b64_tr_b16 a[208:209], v90 offset:0\n"
      "ds_read_b64_tr_b16 a[210:211], v90 offset:2560\n"
      "ds_read_b64_tr_b16 a[212:213], v90 offset:32\n"
      "ds_read_b64_tr_b16 a[214:215], v90 offset:2592\n"
      "ds_read_b64_tr_b16 a[216:217], v90 offset:64\n"
      "ds_read_b64_tr_b16 a[218:219], v90 offset:2624\n"
      "ds_read_b64_tr_b16 a[220:221], v90 offset:96\n"
      "ds_read_b64_tr_b16 a[222:223], v90 offset:2656\n"
      "s_waitcnt lgkmcnt(2)\n"
      "s_waitcnt vmcnt(12)\n"
      "ds_write_b128 v89, a[224:227] offset:0\n"
      "ds_write_b128 v89, a[228:231] offset:1280\n"
      "ds_write_b128 v89, a[232:235] offset:2560\n"
      "ds_write_b128 v89, a[236:239] offset:3840\n"
      "ds_read_b64_tr_b16 a[224:225], v90 offset:0\n"
      "ds_read_b64_tr_b16 a[226:227], v90 offset:2560\n"
      "ds_read_b64_tr_b16 a[228:229], v90 offset:32\n"
      "ds_read_b64_tr_b16 a[230:231], v90 offset:2592\n"
      "ds_read_b64_tr_b16 a[232:233], v90 offset:64\n"
      "ds_read_b64_tr_b16 a[234:235], v90 offset:2624\n"
      "ds_read_b64_tr_b16 a[236:237], v90 offset:96\n"
      "ds_read_b64_tr_b16 a[238:239], v90 offset:2656\n"
      "s_waitcnt lgkmcnt(2)\n"
      "s_waitcnt vmcnt(8)\n"
      "ds_write_b128 v89, a[240:243] offset:0\n"
      "ds_write_b128 v89, a[244:247] offset:1280\n"
      "ds_write_b128 v89, a[248:251] offset:2560\n"
      "ds_write_b128 v89, a[252:255] offset:3840\n"
      "ds_read_b64_tr_b16 a[240:241], v90 offset:0\n"
      "ds_read_b64_tr_b16 a[242:243], v90 offset:2560\n"
      "ds_read_b64_tr_b16 a[244:245], v90 offset:32\n"
      "ds_read_b64_tr_b16 a[246:247], v90 offset:2592\n"
      "ds_read_b64_tr_b16 a[248:249], v90 offset:64\n"
      "ds_read_b64_tr_b16 a[250:251], v90 offset:2624\n"
      "ds_read_b64_tr_b16 a[252:253], v90 offset:96\n"
      "ds_read_b64_tr_b16 a[254:255], v90 offset:2656\n"
      "s_waitcnt lgkmcnt(2)\n"
      "s_mov_b32 s38, 0\n"
      "s_mov_b32 s39, 0\n"
      "s_mov_b32 s40, 0xc0135761\n"
      "s_mov_b32 s41, 0xbdd2d3e8\n"
      "v_mov_b32 v94, v91\n"
      "v_add_u32 v95, 0x8000, v91\n"
      "v_mov_b32 v102, 0\n"
      "v_mov_b32 v103, s85\n"
      "v_mov_b32 v87, s40\n"
      "s_waitcnt lgkmcnt(0)\n"
      "s_barrier\n"
      "ds_read_b128 v[32:35], v94 offset:0\n"
      "ds_read_b128 v[36:39], v94 offset:1024\n"
      "ds_read_b128 v[40:43], v94 offset:2048\n"
      "s_sub_u32 s30, s13, 61440\n"
      ".p2align 6\n"
      "LP0_loop:\n"
      "s_waitcnt lgkmcnt(2)\n"
      "v_mfma_f32_16x16x32_bf16 v[0:3], a[0:3], v[32:35], 0\n"
      "v_mfma_f32_16x16x32_bf16 v[4:7], a[4:7], v[32:35], 0\n"
      "ds_read_b128 v[44:47], v94 offset:3072\n"
      "v_mfma_f32_16x16x32_bf16 v[8:11], a[8:11], v[32:35], 0\n"
      "v_mfma_f32_16x16x32_bf16 v[12:15], a[12:15], v[32:35], 0\n"
      "s_waitcnt lgkmcnt(2)\n"
      "v_mfma_f32_16x16x32_bf16 v[0:3], a[16:19], v[36:39], v[0:3]\n"
      "v_mfma_f32_16x16x32_bf16 v[4:7], a[20:23], v[36:39], v[4:7]\n"
      "ds_read_b128 v[32:35], v94 offset:4096\n"
      "v_mfma_f32_16x16x32_bf16 v[8:11], a[24:27], v[36:39], v[8:11]\n"
      "v_mfma_f32_16x16x32_bf16 v[12:15], a[28:31], v[36:39], v[12:15]\n"
      "v_cvt_pk_bf16_f32 v56, v16, v17\n"
      "s_waitcnt lgkmcnt(2)\n"
      "v_mfma_f32_16x16x32_bf16 v[0:3], a[32:35], v[40:43], v[0:3]\n"
      "v_mfma_f32_16x16x32_bf16 v[4:7], a[36:39], v[40:43], v[4:7]\n"
      "ds_read_b128 v[36:39], v94 offset:5120\n"
      "v_cvt_pk_bf16_f32 v57, v18, v19\n"
      "v_mfma_f32_16x16x32_bf16 v[8:11], a[40:43], v[40:43], v[8:11]\n"
      "v_mfma_f32_16x16x32_bf16 v[12:15], a[44:47], v[40:43], v[12:15]\n"
      "v_cvt_pk_bf16_f32 v58, v24, v25\n"
      "s_waitcnt lgkmcnt(2)\n"
      "v_mfma_f32_16x16x32_bf16 v[0:3], a[48:51], v[44:47], v[0:3]\n"
      "v_mfma_f32_16x16x32_bf16 v[4:7], a[52:55], v[44:47], v[4:7]\n"
      "ds_read_b128 v[40:43], v94 offset:6144\n"
      "v_cvt_pk_bf16_f32 v59, v26, v27\n"
      "v_mfma_f32_16x16x32_bf16 v[8:11], a[56:59], v[44:47], v[8:11]\n"
      "v_mfma_f32_16x16x32_bf16 v[12:15], a[60:63], v[44:47], v[12:15]\n"
      "buffer_store_dwordx4 v[56:59], v93, s[8:11], s30 offen offset:0 sc1\n"
      "s_waitcnt lgkmcnt(2)\n"
      "v_mfma_f32_16x16x32_bf16 v[0:3], a[64:67], v[32:35], v[0:3]\n"
      "v_mfma_f32_16x16x32_bf16 v[4:7], a[68:71], v[32:35], v[4:7]\n"
      "ds_read_b128 v[44:47], v94 offset:7168\n"
      "v_cvt_pk_bf16_f32 v60, v20, v21\n"
      "v_mfma_f32_16x16x32_bf16 v[8:11], a[72:75], v[32:35], v[8:11]\n"
      "v_mfma_f32_16x16x32_bf16 v[12:15], a[76:79], v[32:35], v[12:15]\n"
      "v_cvt_pk_bf16_f32 v61, v22, v23\n"
      "s_waitcnt lgkmcnt(2)\n"
      "v_mfma_f32_16x16x32_bf16 v[0:3], a[80:83], v[36:39], v[0:3]\n"
      "v_mfma_f32_16x16x32_bf16 v[4:7], a[84:87], v[36:39], v[4:7]\n"
      "ds_read_b128 v[32:35], v94 offset:8192\n"
      "v_cvt_pk_bf16_f32 v62, v28, v29\n"
      "v_mfma_f32_16x16x32_bf16 v[8:11], a[88:91], v[36:39], v[8:11]\n"
      "v_mfma_f32_16x16x32_bf16 v[12:15], a[92:95], v[36:39], v[12:15]\n"
      "v_cvt_pk_bf16_f32 v63, v30, v31\n"
      "s_waitcnt lgkmcnt(2)\n"
      "v_mfma_f32_16x16x32_bf16 v[0:3], a[96:99], v[40:43], v[0:3]\n"
      "v_mfma_f32_16x16x32_bf16 v[4:7], a[100:103], v[40:43], v[4:7]\n"
      "ds_read_b128 v[36:39], v94 offset:9216\n"
      "buffer_store_dwordx4 v[60:63], v93, s[8:11], s30 offen offset:2048 sc1\n"
      "v_mfma_f32_16x16x32_bf16 v[8:11], a[104:107], v[40:43], v[8:11]\n"
      "v_mfma_f32_16x16x32_bf16 v[12:15], a[108:111], v[40:43], v[12:15]\n"
      "s_waitcnt lgkmcnt(2)\n"
      "v_mfma_f32_16x16x32_bf16 v[0:3], a[112:115], v[44:47], v[0:3]\n"
      "v_mfma_f32_16x16x32_bf16 v[4:7], a[116:119], v[44:47], v[4:7]\n"
      "ds_read_b128 v[40:43], v94 offset:10240\n"
      "v_mfma_f32_16x16x32_bf16 v[8:11], a[120:123], v[44:47], v[8:11]\n"
      "v_mfma_f32_16x16x32_bf16 v[12:15], a[124:127], v[44:47], v[12:15]\n"
      "s_waitcnt lgkmcnt(2)\n"
      "v_mfma_f32_16x16x32_bf16 v[0:3], a[128:131], v[32:35], v[0:3]\n"
      "v_mfma_f32_16x16x32_bf16 v[4:7], a[132:135], v[32:35], v[4:7]\n"
      "ds_read_b128 v[44:47], v94 offset:11264\n"
      "v_mfma_f32_16x16x32_bf16 v[8:11], a[136:139], v[32:35], v[8:11]\n"
      "v_mfma_f32_16x16x32_bf16 v[12:15], a[140:143], v[32:35], v[12:15]\n"
      "s_waitcnt lgkmcnt(2)\n"
      "v_mfma_f32_16x16x32_bf16 v[0:3], a[144:147], v[36:39], v[0:3]\n"
      "v_mfma_f32_16x16x32_bf16 v[4:7], a[148:151], v[36:39], v[4:7]\n"
      "ds_read_b128 v[32:35], v94 offset:12288\n"
      "v_mfma_f32_16x16x32_bf16 v[8:11], a[152:155], v[36:39], v[8:11]\n"
      "v_mfma_f32_16x16x32_bf16 v[12:15], a[156:159], v[36:39], v[12:15]\n"
      "s_waitcnt lgkmcnt(2)\n"
      "v_mfma_f32_16x16x32_bf16 v[0:3], a[160:163], v[40:43], v[0:3]\n"
      "v_mfma_f32_16x16x32_bf16 v[4:7], a[164:167], v[40:43], v[4:7]\n"
      "ds_read_b128 v[36:39], v94 offset:13312\n"
      "v_mfma_f32_16x16x32_bf16 v[8:11], a[168:171], v[40:43], v[8:11]\n"
      "v_mfma_f32_16x16x32_bf16 v[12:15], a[172:175], v[40:43], v[12:15]\n"
      "s_waitcnt lgkmcnt(2)\n"
      "v_mfma_f32_16x16x32_bf16 v[0:3], a[176:179], v[44:47], v[0:3]\n"
      "v_mfma_f32_16x16x32_bf16 v[4:7], a[180:183], v[44:47], v[4:7]\n"
      "ds_read_b128 v[40:43], v94 offset:14336\n"
      "v_mfma_f32_16x16x32_bf16 v[8:11], a[184:187], v[44:47], v[8:11]\n"
      "v_mfma_f32_16x16x32_bf16 v[12:15], a[188:191], v[44:47], v[12:15]\n"
      "s_waitcnt lgkmcnt(2)\n"
      "v_mfma_f32_16x16x32_bf16 v[0:3], a[192:195], v[32:35], v[0:3]\n"
      "v_mfma_f32_16x16x32_bf16 v[4:7], a[196:199], v[32:35], v[4:7]\n"
      "ds_read_b128 v[44:47], v94 offset:15360\n"
      "v_mfma_f32_16x16x32_bf16 v[8:11], a[200:203], v[32:35], v[8:11]\n"
      "v_mfma_f32_16x16x32_bf16 v[12:15], a[204:207], v[32:35], v[12:15]\n"
      "s_waitcnt lgkmcnt(2)\n"
      "v_mfma_f32_16x16x32_bf16 v[0:3], a[208:211], v[36:39], v[0:3]\n"
      "v_mfma_f32_16x16x32_bf16 v[4:7], a[212:215], v[36:39], v[4:7]\n"
      "ds_read_b128 v[32:35], v94 offset:16384\n"
      "v_mfma_f32_16x16x32_bf16 v[8:11], a[216:219], v[36:39], v[8:11]\n"
      "v_mfma_f32_16x16x32_bf16 v[12:15], a[220:223], v[36:39], v[12:15]\n"
      "s_waitcnt lgkmcnt(2)\n"
      "v_mfma_f32_16x16x32_bf16 v[0:3], a[224:227], v[40:43], v[0:3]\n"
      "v_mfma_f32_16x16x32_bf16 v[4:7], a[228:231], v[40:43], v[4:7]\n"
      "ds_read_b128 v[36:39], v94 offset:17408\n"
      "v_mfma_f32_16x16x32_bf16 v[8:11], a[232:235], v[40:43], v[8:11]\n"
      "v_mfma_f32_16x16x32_bf16 v[12:15], a[236:239], v[40:43], v[12:15]\n"
      "s_waitcnt lgkmcnt(2)\n"
      "v_mfma_f32_16x16x32_bf16 v[0:3], a[240:243], v[44:47], v[0:3]\n"
      "v_mfma_f32_16x16x32_bf16 v[4:7], a[244:247], v[44:47], v[4:7]\n"
      "ds_read_b128 v[40:43], v94 offset:18432\n"
      "v_mfma_f32_16x16x32_bf16 v[8:11], a[248:251], v[44:47], v[8:11]\n"
      "v_mfma_f32_16x16x32_bf16 v[12:15], a[252:255], v[44:47], v[12:15]\n"
      "s_waitcnt lgkmcnt(2)\n"
      "v_mfma_f32_16x16x32_bf16 v[16:19], a[0:3], v[32:35], 0\n"
      "v_mfma_f32_16x16x32_bf16 v[20:23], a[4:7], v[32:35], 0\n"
      "ds_read_b128 v[44:47], v94 offset:19456\n"
      "s_waitcnt vmcnt(0)\n"
      "s_barrier\n"
      "v_mfma_f32_16x16x32_bf16 v[24:27], a[8:11], v[32:35], 0\n"
      "buffer_store_dwordx2 v[102:103], v92, s[8:11], s14 offen sc1\n"
      "v_add_u32 v102, 1, v102\n"
      "v_mfma_f32_16x16x32_bf16 v[28:31], a[12:15], v[32:35], 0\n"
      "s_add_u32 s12, s12, 0x8000\n"
      "s_add_u32 s31, s39, 0x10000\n"
      "s_cmp_ge_u32 s31, 0x18000\n"
      "s_waitcnt lgkmcnt(2)\n"
      "v_mfma_f32_16x16x32_bf16 v[16:19], a[16:19], v[36:39], v[16:19]\n"
      "s_cselect_b32 s28, 0x18000, 0\n"
      "s_sub_u32 s31, s31, s28\n"
      "v_mov_b32 v93, v91\n"
      "v_mfma_f32_16x16x32_bf16 v[20:23], a[20:23], v[36:39], v[20:23]\n"
      "ds_read_b128 v[32:35], v94 offset:20480\n"
      "s_add_u32 s28, s31, s15\n"
      "v_mfma_f32_16x16x32_bf16 v[24:27], a[24:27], v[36:39], v[24:27]\n"
      "s_mov_b32 m0, s28\n"
      "s_add_u32 s29, s12, 4096\n"
      "buffer_load_dwordx4 v91, s[8:11], s12 offen sc1 lds\n"
      "v_mfma_f32_16x16x32_bf16 v[28:31], a[28:31], v[36:39], v[28:31]\n"
      "v_cvt_pk_bf16_f32 v48, v0, v1\n"
      "s_waitcnt lgkmcnt(2)\n"
      "v_mfma_f32_16x16x32_bf16 v[16:19], a[32:35], v[40:43], v[16:19]\n"
      "v_mfma_f32_16x16x32_bf16 v[20:23], a[36:39], v[40:43], v[20:23]\n"
      "ds_read_b128 v[36:39], v94 offset:21504\n"
      "v_cvt_pk_bf16_f32 v49, v2, v3\n"
      "v_mfma_f32_16x16x32_bf16 v[24:27], a[40:43], v[40:43], v[24:27]\n"
      "buffer_load_dwordx4 v91, s[8:11], s12 offen offset:1024 sc1 lds\n"
      "v_mfma_f32_16x16x32_bf16 v[28:31], a[44:47], v[40:43], v[28:31]\n"
      "v_cvt_pk_bf16_f32 v50, v8, v9\n"
      "s_waitcnt lgkmcnt(2)\n"
      "v_mfma_f32_16x16x32_bf16 v[16:19], a[48:51], v[44:47], v[16:19]\n"
      "v_mfma_f32_16x16x32_bf16 v[20:23], a[52:55], v[44:47], v[20:23]\n"
      "ds_read_b128 v[40:43], v94 offset:22528\n"
      "v_cvt_pk_bf16_f32 v51, v10, v11\n"
      "v_mfma_f32_16x16x32_bf16 v[24:27], a[56:59], v[44:47], v[24:27]\n"
      "buffer_load_dwordx4 v91, s[8:11], s12 offen offset:2048 sc1 lds\n"
      "v_mfma_f32_16x16x32_bf16 v[28:31], a[60:63], v[44:47], v[28:31]\n"
      "buffer_store_dwordx4 v[48:51], v91, s[8:11], s13 offen offset:0 sc1\n"
      "s_waitcnt lgkmcnt(2)\n"
      "v_mfma_f32_16x16x32_bf16 v[16:19], a[64:67], v[32:35], v[16:19]\n"
      "v_mfma_f32_16x16x32_bf16 v[20:23], a[68:71], v[32:35], v[20:23]\n"
      "ds_read_b128 v[44:47], v94 offset:23552\n"
      "v_cvt_pk_bf16_f32 v52, v4, v5\n"
      "v_mfma_f32_16x16x32_bf16 v[24:27], a[72:75], v[32:35], v[24:27]\n"
      "buffer_load_dwordx4 v91, s[8:11], s12 offen offset:3072 sc1 lds\n"
      "v_mfma_f32_16x16x32_bf16 v[28:31], a[76:79], v[32:35], v[28:31]\n"
      "v_cvt_pk_bf16_f32 v53, v6, v7\n"
      "s_waitcnt lgkmcnt(2)\n"
      "v_mfma_f32_16x16x32_bf16 v[16:19], a[80:83], v[36:39], v[16:19]\n"
      "v_mfma_f32_16x16x32_bf16 v[20:23], a[84:87], v[36:39], v[20:23]\n"
      "ds_read_b128 v[32:35], v94 offset:24576\n"
      "v_cvt_pk_bf16_f32 v54, v12, v13\n"
      "v_mfma_f32_16x16x32_bf16 v[24:27], a[88:91], v[36:39], v[24:27]\n"
      "s_add_u32 m0, s28, 4096\n"
      "s_nop 0\n"
      "buffer_load_dwordx4 v91, s[8:11], s29 offen sc1 lds\n"
      "v_mfma_f32_16x16x32_bf16 v[28:31], a[92:95], v[36:39], v[28:31]\n"
      "v_cvt_pk_bf16_f32 v55, v14, v15\n"
      "s_waitcnt lgkmcnt(2)\n"
      "v_mfma_f32_16x16x32_bf16 v[16:19], a[96:99], v[40:43], v[16:19]\n"
      "v_mfma_f32_16x16x32_bf16 v[20:23], a[100:103], v[40:43], v[20:23]\n"
      "ds_read_b128 v[36:39], v94 offset:25600\n"
      "buffer_store_dwordx4 v[52:55], v91, s[8:11], s13 offen offset:2048 sc1\n"
      "v_mfma_f32_16x16x32_bf16 v[24:27], a[104:107], v[40:43], v[24:27]\n"
      "buffer_load_dwordx4 v91, s[8:11], s29 offen offset:1024 sc1 lds\n"
      "v_mfma_f32_16x16x32_bf16 v[28:31], a[108:111], v[40:43], v[28:31]\n"
      "s_waitcnt lgkmcnt(2)\n"
      "v_mfma_f32_16x16x32_bf16 v[16:19], a[112:115], v[44:47], v[16:19]\n"
      "v_mfma_f32_16x16x32_bf16 v[20:23], a[116:119], v[44:47], v[20:23]\n"
      "ds_read_b128 v[40:43], v94 offset:26624\n"
      "v_mfma_f32_16x16x32_bf16 v[24:27], a[120:123], v[44:47], v[24:27]\n"
      "buffer_load_dwordx4 v91, s[8:11], s29 offen offset:2048 sc1 lds\n"
      "v_mfma_f32_16x16x32_bf16 v[28:31], a[124:127], v[44:47], v[28:31]\n"
      "s_waitcnt lgkmcnt(2)\n"
      "v_mfma_f32_16x16x32_bf16 v[16:19], a[128:131], v[32:35], v[16:19]\n"
      "v_mfma_f32_16x16x32_bf16 v[20:23], a[132:135], v[32:35], v[20:23]\n"
      "ds_read_b128 v[44:47], v94 offset:27648\n"
      "v_mfma_f32_16x16x32_bf16 v[24:27], a[136:139], v[32:35], v[24:27]\n"
      "buffer_load_dwordx4 v91, s[8:11], s29 offen offset:3072 sc1 lds\n"
      "v_mfma_f32_16x16x32_bf16 v[28:31], a[140:143], v[32:35], v[28:31]\n"
      "s_waitcnt lgkmcnt(2)\n"
      "v_mfma_f32_16x16x32_bf16 v[16:19], a[144:147], v[36:39], v[16:19]\n"
      "v_mfma_f32_16x16x32_bf16 v[20:23], a[148:151], v[36:39], v[20:23]\n"
      "ds_read_b128 v[32:35], v94 offset:28672\n"
      "v_mfma_f32_16x16x32_bf16 v[24:27], a[152:155], v[36:39], v[24:27]\n"
      "v_mfma_f32_16x16x32_bf16 v[28:31], a[156:159], v[36:39], v[28:31]\n"
      "s_waitcnt lgkmcnt(2)\n"
      "v_mfma_f32_16x16x32_bf16 v[16:19], a[160:163], v[40:43], v[16:19]\n"
      "v_mfma_f32_16x16x32_bf16 v[20:23], a[164:167], v[40:43], v[20:23]\n"
      "ds_read_b128 v[36:39], v94 offset:29696\n"
      "v_mfma_f32_16x16x32_bf16 v[24:27], a[168:171], v[40:43], v[24:27]\n"
      "v_mfma_f32_16x16x32_bf16 v[28:31], a[172:175], v[40:43], v[28:31]\n"
      "s_waitcnt lgkmcnt(2)\n"
      "v_mfma_f32_16x16x32_bf16 v[16:19], a[176:179], v[44:47], v[16:19]\n"
      "s_add_u32 s39, s39, 0x8000\n"
      "s_cmp_eq_u32 s39, 0x18000\n"
      "s_cselect_b32 s39, 0, s39\n"
      "v_mfma_f32_16x16x32_bf16 v[20:23], a[180:183], v[44:47], v[20:23]\n"
      "ds_read_b128 v[40:43], v94 offset:30720\n"
      "v_mfma_f32_16x16x32_bf16 v[24:27], a[184:187], v[44:47], v[24:27]\n"
      "v_mfma_f32_16x16x32_bf16 v[28:31], a[188:191], v[44:47], v[28:31]\n"
      "s_cmp_eq_u32 s38, 31\n"
      "s_cselect_b32 s28, s16, 0\n"
      "s_add_u32 s28, s28, 65536\n"
      "s_waitcnt lgkmcnt(2)\n"
      "v_mfma_f32_16x16x32_bf16 v[16:19], a[192:195], v[32:35], v[16:19]\n"
      "v_mfma_f32_16x16x32_bf16 v[20:23], a[196:199], v[32:35], v[20:23]\n"
      "ds_read_b128 v[44:47], v94 offset:31744\n"
      "v_mfma_f32_16x16x32_bf16 v[24:27], a[200:203], v[32:35], v[24:27]\n"
      "v_mfma_f32_16x16x32_bf16 v[28:31], a[204:207], v[32:35], v[28:31]\n"
      "s_add_u32 s30, s13, 4096\n"
      "v_mov_b32 v94, v95\n"
      "s_waitcnt lgkmcnt(2)\n"
      "v_mfma_f32_16x16x32_bf16 v[16:19], a[208:211], v[36:39], v[16:19]\n"
      "v_mfma_f32_16x16x32_bf16 v[20:23], a[212:215], v[36:39], v[20:23]\n"
      "ds_read_b128 v[32:35], v95 offset:0\n"
      "v_mfma_f32_16x16x32_bf16 v[24:27], a[216:219], v[36:39], v[24:27]\n"
      "v_mfma_f32_16x16x32_bf16 v[28:31], a[220:223], v[36:39], v[28:31]\n"
      "s_waitcnt lgkmcnt(2)\n"
      "v_mfma_f32_16x16x32_bf16 v[16:19], a[224:227], v[40:43], v[16:19]\n"
      "v_mfma_f32_16x16x32_bf16 v[20:23], a[228:231], v[40:43], v[20:23]\n"
      "ds_read_b128 v[36:39], v95 offset:1024\n"
      "v_mfma_f32_16x16x32_bf16 v[24:27], a[232:235], v[40:43], v[24:27]\n"
      "v_mfma_f32_16x16x32_bf16 v[28:31], a[236:239], v[40:43], v[28:31]\n"
      "s_waitcnt lgkmcnt(2)\n"
      "v_mfma_f32_16x16x32_bf16 v[16:19], a[240:243], v[44:47], v[16:19]\n"
      "v_mfma_f32_16x16x32_bf16 v[20:23], a[244:247], v[44:47], v[20:23]\n"
      "ds_read_b128 v[40:43], v95 offset:2048\n"
      "v_mfma_f32_16x16x32_bf16 v[24:27], a[248:251], v[44:47], v[24:27]\n"
      "v_add_u32 v95, s31, v91\n"
      "s_add_u32 s38, s38, 1\n"
      "v_mfma_f32_16x16x32_bf16 v[28:31], a[252:255], v[44:47], v[28:31]\n"
      "s_add_u32 s13, s13, s28\n"
      "s_cmp_lt_u32 s38, 64\n"
      "s_cbranch_scc1 LP0_loop\n"
      "v_cvt_pk_bf16_f32 v56, v16, v17\n"
      "v_cvt_pk_bf16_f32 v57, v18, v19\n"
      "v_cvt_pk_bf16_f32 v58, v24, v25\n"
      "v_cvt_pk_bf16_f32 v59, v26, v27\n"
      "buffer_store_dwordx4 v[56:59], v91, s[8:11], s30 offen offset:0 sc1\n"
      "v_cvt_pk_bf16_f32 v60, v20, v21\n"
      "v_cvt_pk_bf16_f32 v61, v22, v23\n"
      "v_cvt_pk_bf16_f32 v62, v28, v29\n"
      "v_cvt_pk_bf16_f32 v63, v30, v31\n"
      "buffer_store_dwordx4 v[60:63], v91, s[8:11], s30 offen offset:2048 sc1\n"
      "s_waitcnt vmcnt(0) lgkmcnt(0)\n"
      "s_barrier\n"
      "v_mov_b32 v102, 0x64\n"
      "buffer_store_dwordx2 v[102:103], v92, s[8:11], s14 offen sc1\n"
      "s_branch Lend\n"
      "LV_init:\n"
      "s_mov_b32 s31, s15\n"
      "s_mov_b32 s62, 0x100000\n"
      "s_mov_b32 s28, 0x0\n"
      "buffer_load_dwordx4 a[0:3], v88, s[68:71], s28 offen\n"
      "s_mov_b32 s28, 0x8000\n"
      "buffer_load_dwordx4 a[4:7], v88, s[68:71], s28 offen\n"
      "s_mov_b32 s28, 0x40000\n"
      "buffer_load_dwordx4 a[8:11], v88, s[68:71], s28 offen\n"
      "s_mov_b32 s28, 0x48000\n"
      "buffer_load_dwordx4 a[12:15], v88, s[68:71], s28 offen\n"
      "s_mov_b32 s28, 0x80000\n"
      "buffer_load_dwordx4 a[16:19], v88, s[68:71], s28 offen\n"
      "s_mov_b32 s28, 0x88000\n"
      "buffer_load_dwordx4 a[20:23], v88, s[68:71], s28 offen\n"
      "s_mov_b32 s28, 0xc0000\n"
      "buffer_load_dwordx4 a[24:27], v88, s[68:71], s28 offen\n"
      "s_mov_b32 s28, 0xc8000\n"
      "buffer_load_dwordx4 a[28:31], v88, s[68:71], s28 offen\n"
      "s_mov_b32 s28, 0x100000\n"
      "buffer_load_dwordx4 a[32:35], v88, s[68:71], s28 offen\n"
      "s_mov_b32 s28, 0x108000\n"
      "buffer_load_dwordx4 a[36:39], v88, s[68:71], s28 offen\n"
      "s_mov_b32 s28, 0x140000\n"
      "buffer_load_dwordx4 a[40:43], v88, s[68:71], s28 offen\n"
      "s_mov_b32 s28, 0x148000\n"
      "buffer_load_dwordx4 a[44:47], v88, s[68:71], s28 offen\n"
      "s_mov_b32 s28, 0x180000\n"
      "buffer_load_dwordx4 a[48:51], v88, s[68:71], s28 offen\n"
      "s_mov_b32 s28, 0x188000\n"
      "buffer_load_dwordx4 a[52:55], v88, s[68:71], s28 offen\n"
      "s_mov_b32 s28, 0x1c0000\n"
      "buffer_load_dwordx4 a[56:59], v88, s[68:71], s28 offen\n"
      "s_mov_b32 s28, 0x1c8000\n"
      "buffer_load_dwordx4 a[60:63], v88, s[68:71], s28 offen\n"
      "s_waitcnt vmcnt(12)\n"
      "s_waitcnt lgkmcnt(0)\n"
      "s_add_u32 s85, s85, 1\n"
      "ds_write_b128 v89, a[0:3] offset:0\n"
      "ds_write_b128 v89, a[4:7] offset:1536\n"
      "ds_write_b128 v89, a[8:11] offset:3072\n"
      "ds_write_b128 v89, a[12:15] offset:4608\n"
      "s_mov_b32 s28, 0x200000\n"
      "buffer_load_dwordx4 a[64:67], v88, s[68:71], s28 offen\n"
      "s_mov_b32 s28, 0x208000\n"
      "buffer_load_dwordx4 a[68:71], v88, s[68:71], s28 offen\n"
      "s_mov_b32 s28, 0x240000\n"
      "buffer_load_dwordx4 a[72:75], v88, s[68:71], s28 offen\n"
      "s_mov_b32 s28, 0x248000\n"
      "buffer_load_dwordx4 a[76:79], v88, s[68:71], s28 offen\n"
      "ds_read_b64_tr_b16 a[0:1], v90 offset:0\n"
      "ds_read_b64_tr_b16 a[4:5], v90 offset:32\n"
      "ds_read_b64_tr_b16 a[2:3], v90 offset:1536\n"
      "ds_read_b64_tr_b16 a[6:7], v90 offset:1568\n"
      "ds_read_b64_tr_b16 a[8:9], v90 offset:3072\n"
      "ds_read_b64_tr_b16 a[12:13], v90 offset:3104\n"
      "ds_read_b64_tr_b16 a[10:11], v90 offset:4608\n"
      "ds_read_b64_tr_b16 a[14:15], v90 offset:4640\n"
      "s_waitcnt lgkmcnt(2)\n"
      "s_waitcnt vmcnt(12)\n"
      "ds_write_b128 v89, a[16:19] offset:0\n"
      "ds_write_b128 v89, a[20:23] offset:1536\n"
      "ds_write_b128 v89, a[24:27] offset:3072\n"
      "ds_write_b128 v89, a[28:31] offset:4608\n"
      "s_mov_b32 s28, 0x280000\n"
      "buffer_load_dwordx4 a[80:83], v88, s[68:71], s28 offen\n"
      "s_mov_b32 s28, 0x288000\n"
      "buffer_load_dwordx4 a[84:87], v88, s[68:71], s28 offen\n"
      "s_mov_b32 s28, 0x2c0000\n"
      "buffer_load_dwordx4 a[88:91], v88, s[68:71], s28 offen\n"
      "s_mov_b32 s28, 0x2c8000\n"
      "buffer_load_dwordx4 a[92:95], v88, s[68:71], s28 offen\n"
      "ds_read_b64_tr_b16 a[16:17], v90 offset:0\n"
      "ds_read_b64_tr_b16 a[20:21], v90 offset:32\n"
      "ds_read_b64_tr_b16 a[18:19], v90 offset:1536\n"
      "ds_read_b64_tr_b16 a[22:23], v90 offset:1568\n"
      "ds_read_b64_tr_b16 a[24:25], v90 offset:3072\n"
      "ds_read_b64_tr_b16 a[28:29], v90 offset:3104\n"
      "ds_read_b64_tr_b16 a[26:27], v90 offset:4608\n"
      "ds_read_b64_tr_b16 a[30:31], v90 offset:4640\n"
      "s_waitcnt lgkmcnt(2)\n"
      "s_waitcnt vmcnt(12)\n"
      "ds_write_b128 v89, a[32:35] offset:0\n"
      "ds_write_b128 v89, a[36:39] offset:1536\n"
      "ds_write_b128 v89, a[40:43] offset:3072\n"
      "ds_write_b128 v89, a[44:47] offset:4608\n"
      "s_mov_b32 s28, 0x300000\n"
      "buffer_load_dwordx4 a[96:99], v88, s[68:71], s28 offen\n"
      "s_mov_b32 s28, 0x308000\n"
      "buffer_load_dwordx4 a[100:103], v88, s[68:71], s28 offen\n"
      "s_mov_b32 s28, 0x340000\n"
      "buffer_load_dwordx4 a[104:107], v88, s[68:71], s28 offen\n"
      "s_mov_b32 s28, 0x348000\n"
      "buffer_load_dwordx4 a[108:111], v88, s[68:71], s28 offen\n"
      "ds_read_b64_tr_b16 a[32:33], v90 offset:0\n"
      "ds_read_b64_tr_b16 a[36:37], v90 offset:32\n"
      "ds_read_b64_tr_b16 a[34:35], v90 offset:1536\n"
      "ds_read_b64_tr_b16 a[38:39], v90 offset:1568\n"
      "ds_read_b64_tr_b16 a[40:41], v90 offset:3072\n"
      "ds_read_b64_tr_b16 a[44:45], v90 offset:3104\n"
      "ds_read_b64_tr_b16 a[42:43], v90 offset:4608\n"
      "ds_read_b64_tr_b16 a[46:47], v90 offset:4640\n"
      "s_waitcnt lgkmcnt(2)\n"
      "s_waitcnt vmcnt(12)\n"
      "ds_write_b128 v89, a[48:51] offset:0\n"
      "ds_write_b128 v89, a[52:55] offset:1536\n"
      "ds_write_b128 v89, a[56:59] offset:3072\n"
      "ds_write_b128 v89, a[60:63] offset:4608\n"
      "s_mov_b32 s28, 0x380000\n"
      "buffer_load_dwordx4 a[112:115], v88, s[68:71], s28 offen\n"
      "s_mov_b32 s28, 0x388000\n"
      "buffer_load_dwordx4 a[116:119], v88, s[68:71], s28 offen\n"
      "s_mov_b32 s28, 0x3c0000\n"
      "buffer_load_dwordx4 a[120:123], v88, s[68:71], s28 offen\n"
      "s_mov_b32 s28, 0x3c8000\n"
      "buffer_load_dwordx4 a[124:127], v88, s[68:71], s28 offen\n"
      "ds_read_b64_tr_b16 a[48:49], v90 offset:0\n"
      "ds_read_b64_tr_b16 a[52:53], v90 offset:32\n"
      "ds_read_b64_tr_b16 a[50:51], v90 offset:1536\n"
      "ds_read_b64_tr_b16 a[54:55], v90 offset:1568\n"
      "ds_read_b64_tr_b16 a[56:57], v90 offset:3072\n"
      "ds_read_b64_tr_b16 a[60:61], v90 offset:3104\n"
      "ds_read_b64_tr_b16 a[58:59], v90 offset:4608\n"
      "ds_read_b64_tr_b16 a[62:63], v90 offset:4640\n"
      "s_waitcnt lgkmcnt(2)\n"
      "s_waitcnt vmcnt(12)\n"
      "ds_write_b128 v89, a[64:67] offset:0\n"
      "ds_write_b128 v89, a[68:71] offset:1536\n"
      "ds_write_b128 v89, a[72:75] offset:3072\n"
      "ds_write_b128 v89, a[76:79] offset:4608\n"
      "s_mov_b32 s28, 0x0\n"
      "buffer_load_dwordx4 a[128:131], v93, s[72:75], s28 offen\n"
      "s_mov_b32 s28, 0x2000\n"
      "buffer_load_dwordx4 a[132:135], v93, s[72:75], s28 offen\n"
      "s_mov_b32 s28, 0x4000\n"
      "buffer_load_dwordx4 a[136:139], v93, s[72:75], s28 offen\n"
      "s_mov_b32 s28, 0x6000\n"
      "buffer_load_dwordx4 a[140:143], v93, s[72:75], s28 offen\n"
      "buffer_load_dwordx2 v[0:1], v100, s[8:11], 0 offen sc1\n"
      "ds_read_b64_tr_b16 a[64:65], v90 offset:0\n"
      "ds_read_b64_tr_b16 a[68:69], v90 offset:32\n"
      "ds_read_b64_tr_b16 a[66:67], v90 offset:1536\n"
      "ds_read_b64_tr_b16 a[70:71], v90 offset:1568\n"
      "ds_read_b64_tr_b16 a[72:73], v90 offset:3072\n"
      "ds_read_b64_tr_b16 a[76:77], v90 offset:3104\n"
      "ds_read_b64_tr_b16 a[74:75], v90 offset:4608\n"
      "ds_read_b64_tr_b16 a[78:79], v90 offset:4640\n"
      "s_waitcnt lgkmcnt(2)\n"
      "s_waitcnt vmcnt(13)\n"
      "ds_write_b128 v89, a[80:83] offset:0\n"
      "ds_write_b128 v89, a[84:87] offset:1536\n"
      "ds_write_b128 v89, a[88:91] offset:3072\n"
      "ds_write_b128 v89, a[92:95] offset:4608\n"
      "s_mov_b32 s28, 0x8000\n"
      "buffer_load_dwordx4 a[144:147], v93, s[72:75], s28 offen\n"
      "s_mov_b32 s28, 0xa000\n"
      "buffer_load_dwordx4 a[148:151], v93, s[72:75], s28 offen\n"
      "s_mov_b32 s28, 0xc000\n"
      "buffer_load_dwordx4 a[152:155], v93, s[72:75], s28 offen\n"
      "s_mov_b32 s28, 0xe000\n"
      "buffer_load_dwordx4 a[156:159], v93, s[72:75], s28 offen\n"
      "ds_read_b64_tr_b16 a[80:81], v90 offset:0\n"
      "ds_read_b64_tr_b16 a[84:85], v90 offset:32\n"
      "ds_read_b64_tr_b16 a[82:83], v90 offset:1536\n"
      "ds_read_b64_tr_b16 a[86:87], v90 offset:1568\n"
      "ds_read_b64_tr_b16 a[88:89], v90 offset:3072\n"
      "ds_read_b64_tr_b16 a[92:93], v90 offset:3104\n"
      "ds_read_b64_tr_b16 a[90:91], v90 offset:4608\n"
      "ds_read_b64_tr_b16 a[94:95], v90 offset:4640\n"
      "s_waitcnt lgkmcnt(2)\n"
      "s_waitcnt vmcnt(13)\n"
      "ds_write_b128 v89, a[96:99] offset:0\n"
      "ds_write_b128 v89, a[100:103] offset:1536\n"
      "ds_write_b128 v89, a[104:107] offset:3072\n"
      "ds_write_b128 v89, a[108:111] offset:4608\n"
      "s_mov_b32 s28, 0x10000\n"
      "buffer_load_dwordx4 a[160:163], v93, s[72:75], s28 offen\n"
      "s_mov_b32 s28, 0x12000\n"
      "buffer_load_dwordx4 a[164:167], v93, s[72:75], s28 offen\n"
      "s_mov_b32 s28, 0x14000\n"
      "buffer_load_dwordx4 a[168:171], v93, s[72:75], s28 offen\n"
      "s_mov_b32 s28, 0x16000\n"
      "buffer_load_dwordx4 a[172:175], v93, s[72:75], s28 offen\n"
      "ds_read_b64_tr_b16 a[96:97], v90 offset:0\n"
      "ds_read_b64_tr_b16 a[100:101], v90 offset:32\n"
      "ds_read_b64_tr_b16 a[98:99], v90 offset:1536\n"
      "ds_read_b64_tr_b16 a[102:103], v90 offset:1568\n"
      "ds_read_b64_tr_b16 a[104:105], v90 offset:3072\n"
      "ds_read_b64_tr_b16 a[108:109], v90 offset:3104\n"
      "ds_read_b64_tr_b16 a[106:107], v90 offset:4608\n"
      "ds_read_b64_tr_b16 a[110:111], v90 offset:4640\n"
      "s_waitcnt lgkmcnt(2)\n"
      "s_waitcnt vmcnt(13)\n"
      "ds_write_b128 v89, a[112:115] offset:0\n"
      "ds_write_b128 v89, a[116:119] offset:1536\n"
      "ds_write_b128 v89, a[120:123] offset:3072\n"
      "ds_write_b128 v89, a[124:127] offset:4608\n"
      "s_mov_b32 s28, 0x18000\n"
      "buffer_load_dwordx4 a[176:179], v93, s[72:75], s28 offen\n"
      "s_mov_b32 s28, 0x1a000\n"
      "buffer_load_dwordx4 a[180:183], v93, s[72:75], s28 offen\n"
      "s_mov_b32 s28, 0x1c000\n"
      "buffer_load_dwordx4 a[184:187], v93, s[72:75], s28 offen\n"
      "s_mov_b32 s28, 0x1e000\n"
      "buffer_load_dwordx4 a[188:191], v93, s[72:75], s28 offen\n"
      "ds_read_b64_tr_b16 a[112:113], v90 offset:0\n"
      "ds_read_b64_tr_b16 a[116:117], v90 offset:32\n"
      "ds_read_b64_tr_b16 a[114:115], v90 offset:1536\n"
      "ds_read_b64_tr_b16 a[118:119], v90 offset:1568\n"
      "ds_read_b64_tr_b16 a[120:121], v90 offset:3072\n"
      "ds_read_b64_tr_b16 a[124:125], v90 offset:3104\n"
      "ds_read_b64_tr_b16 a[122:123], v90 offset:4608\n"
      "ds_read_b64_tr_b16 a[126:127], v90 offset:4640\n"
      "s_waitcnt lgkmcnt(2)\n"
      "s_waitcnt vmcnt(13)\n"
      "ds_write_b128 v94, a[128:131] offset:0\n"
      "ds_write_b128 v94, a[132:135] offset:1280\n"
      "ds_write_b128 v94, a[136:139] offset:2560\n"
      "ds_write_b128 v94, a[140:143] offset:3840\n"
      "s_mov_b32 s28, 0x80\n"
      "buffer_load_dwordx4 a[192:195], v93, s[72:75], s28 offen\n"
      "s_mov_b32 s28, 0x2080\n"
      "buffer_load_dwordx4 a[196:199], v93, s[72:75], s28 offen\n"
      "s_mov_b32 s28, 0x4080\n"
      "buffer_load_dwordx4 a[200:203], v93, s[72:75], s28 offen\n"
      "s_mov_b32 s28, 0x6080\n"
      "buffer_load_dwordx4 a[204:207], v93, s[72:75], s28 offen\n"
      "ds_read_b64_tr_b16 a[128:129], v95 offset:0\n"
      "ds_read_b64_tr_b16 a[130:131], v95 offset:2560\n"
      "ds_read_b64_tr_b16 a[132:133], v95 offset:32\n"
      "ds_read_b64_tr_b16 a[134:135], v95 offset:2592\n"
      "ds_read_b64_tr_b16 a[136:137], v95 offset:64\n"
      "ds_read_b64_tr_b16 a[138:139], v95 offset:2624\n"
      "ds_read_b64_tr_b16 a[140:141], v95 offset:96\n"
      "ds_read_b64_tr_b16 a[142:143], v95 offset:2656\n"
      "s_waitcnt lgkmcnt(2)\n"
      "s_waitcnt vmcnt(12)\n"
      "ds_write_b128 v94, a[144:147] offset:0\n"
      "ds_write_b128 v94, a[148:151] offset:1280\n"
      "ds_write_b128 v94, a[152:155] offset:2560\n"
      "ds_write_b128 v94, a[156:159] offset:3840\n"
      "s_mov_b32 s28, 0x8080\n"
      "buffer_load_dwordx4 a[208:211], v93, s[72:75], s28 offen\n"
      "s_mov_b32 s28, 0xa080\n"
      "buffer_load_dwordx4 a[212:215], v93, s[72:75], s28 offen\n"
      "s_mov_b32 s28, 0xc080\n"
      "buffer_load_dwordx4 a[216:219], v93, s[72:75], s28 offen\n"
      "s_mov_b32 s28, 0xe080\n"
      "buffer_load_dwordx4 a[220:223], v93, s[72:75], s28 offen\n"
      "s_branch LV_lnc\n"
      "LV_ln:\n"
      "buffer_load_dwordx2 v[0:1], v100, s[8:11], 0 offen offset:0 sc1\n"
      "s_sub_u32 s62, s62, 1\n"
      "s_cbranch_scc1 LV_lng\n"
      "s_waitcnt vmcnt(0)\n"
      "LV_lnc:\n"
      "v_cmp_ne_u32_e64 s[54:55], s85, v1\n"
      "v_cmp_ge_i32 vcc, -1, v0\n"
      "s_or_b64 vcc, vcc, s[54:55]\n"
      "s_cbranch_vccz LV_lnk\n"
      "s_sleep 3\n"
      "s_branch LV_ln\n"
      "LV_lng:\n"
      "s_mov_b32 s62, 0\n"
      "s_waitcnt vmcnt(0)\n"
      "LV_lnk:\n"
      "s_mov_b32 m0, s31\n"
      "s_add_u32 s29, s12, 4096\n"
      "buffer_load_dwordx4 v91, s[8:11], s12 offen sc1 lds\n"
      "v_readlane_b32 s55, v0, 63\n"
      "s_and_b32 s55, s55, 1\n"
      "s_lshl_b32 s55, s55, 21\n"
      "s_add_u32 s13, s13, s55\n"
      "buffer_load_dwordx4 v91, s[8:11], s12 offen offset:1024 sc1 lds\n"
      "buffer_load_dwordx4 v91, s[8:11], s12 offen offset:2048 sc1 lds\n"
      "buffer_load_dwordx4 v91, s[8:11], s12 offen offset:3072 sc1 lds\n"
      "s_add_u32 m0, s31, 4096\n"
      "s_nop 0\n"
      "buffer_load_dwordx4 v91, s[8:11], s29 offen sc1 lds\n"
      "buffer_load_dwordx4 v91, s[8:11], s29 offen offset:1024 sc1 lds\n"
      "buffer_load_dwordx4 v91, s[8:11], s29 offen offset:2048 sc1 lds\n"
      "buffer_load_dwordx4 v91, s[8:11], s29 offen offset:3072 sc1 lds\n"
      "ds_read_b64_tr_b16 a[144:145], v95 offset:0\n"
      "ds_read_b64_tr_b16 a[146:147], v95 offset:2560\n"
      "ds_read_b64_tr_b16 a[148:149], v95 offset:32\n"
      "ds_read_b64_tr_b16 a[150:151], v95 offset:2592\n"
      "ds_read_b64_tr_b16 a[152:153], v95 offset:64\n"
      "ds_read_b64_tr_b16 a[154:155], v95 offset:2624\n"
      "ds_read_b64_tr_b16 a[156:157], v95 offset:96\n"
      "ds_read_b64_tr_b16 a[158:159], v95 offset:2656\n"
      "s_waitcnt lgkmcnt(2)\n"
      "s_waitcnt vmcnt(20)\n"
      "ds_write_b128 v94, a[160:163] offset:0\n"
      "ds_write_b128 v94, a[164:167] offset:1280\n"
      "ds_write_b128 v94, a[168:171] offset:2560\n"
      "ds_write_b128 v94, a[172:175] offset:3840\n"
      "s_mov_b32 s28, 0x10080\n"
      "buffer_load_dwordx4 a[224:227], v93, s[72:75], s28 offen\n"
      "s_mov_b32 s28, 0x12080\n"
      "buffer_load_dwordx4 a[228:231], v93, s[72:75], s28 offen\n"
      "s_mov_b32 s28, 0x14080\n"
      "buffer_load_dwordx4 a[232:235], v93, s[72:75], s28 offen\n"
      "s_mov_b32 s28, 0x16080\n"
      "buffer_load_dwordx4 a[236:239], v93, s[72:75], s28 offen\n"
      "ds_read_b64_tr_b16 a[160:161], v95 offset:0\n"
      "ds_read_b64_tr_b16 a[162:163], v95 offset:2560\n"
      "ds_read_b64_tr_b16 a[164:165], v95 offset:32\n"
      "ds_read_b64_tr_b16 a[166:167], v95 offset:2592\n"
      "ds_read_b64_tr_b16 a[168:169], v95 offset:64\n"
      "ds_read_b64_tr_b16 a[170:171], v95 offset:2624\n"
      "ds_read_b64_tr_b16 a[172:173], v95 offset:96\n"
      "ds_read_b64_tr_b16 a[174:175], v95 offset:2656\n"
      "s_waitcnt lgkmcnt(2)\n"
      "s_waitcnt vmcnt(20)\n"
      "ds_write_b128 v94, a[176:179] offset:0\n"
      "ds_write_b128 v94, a[180:183] offset:1280\n"
      "ds_write_b128 v94, a[184:187] offset:2560\n"
      "ds_write_b128 v94, a[188:191] offset:3840\n"
      "s_mov_b32 s28, 0x18080\n"
      "buffer_load_dwordx4 a[240:243], v93, s[72:75], s28 offen\n"
      "s_mov_b32 s28, 0x1a080\n"
      "buffer_load_dwordx4 a[244:247], v93, s[72:75], s28 offen\n"
      "s_mov_b32 s28, 0x1c080\n"
      "buffer_load_dwordx4 a[248:251], v93, s[72:75], s28 offen\n"
      "s_mov_b32 s28, 0x1e080\n"
      "buffer_load_dwordx4 a[252:255], v93, s[72:75], s28 offen\n"
      "s_add_u32 s12, s12, 0x8000\n"
      "s_add_u32 s31, s31, 0x8000\n"
      "s_mov_b32 m0, s31\n"
      "s_add_u32 s29, s12, 4096\n"
      "buffer_load_dwordx4 v91, s[8:11], s12 offen sc1 lds\n"
      "buffer_load_dwordx4 v91, s[8:11], s12 offen offset:1024 sc1 lds\n"
      "buffer_load_dwordx4 v91, s[8:11], s12 offen offset:2048 sc1 lds\n"
      "buffer_load_dwordx4 v91, s[8:11], s12 offen offset:3072 sc1 lds\n"
      "s_add_u32 m0, s31, 4096\n"
      "s_nop 0\n"
      "buffer_load_dwordx4 v91, s[8:11], s29 offen sc1 lds\n"
      "buffer_load_dwordx4 v91, s[8:11], s29 offen offset:1024 sc1 lds\n"
      "buffer_load_dwordx4 v91, s[8:11], s29 offen offset:2048 sc1 lds\n"
      "buffer_load_dwordx4 v91, s[8:11], s29 offen offset:3072 sc1 lds\n"
      "ds_read_b64_tr_b16 a[176:177], v95 offset:0\n"
      "ds_read_b64_tr_b16 a[178:179], v95 offset:2560\n"
      "ds_read_b64_tr_b16 a[180:181], v95 offset:32\n"
      "ds_read_b64_tr_b16 a[182:183], v95 offset:2592\n"
      "ds_read_b64_tr_b16 a[184:185], v95 offset:64\n"
      "ds_read_b64_tr_b16 a[186:187], v95 offset:2624\n"
      "ds_read_b64_tr_b16 a[188:189], v95 offset:96\n"
      "ds_read_b64_tr_b16 a[190:191], v95 offset:2656\n"
      "s_waitcnt lgkmcnt(2)\n"
      "s_waitcnt vmcnt(28)\n"
      "ds_write_b128 v94, a[192:195] offset:0\n"
      "ds_write_b128 v94, a[196:199] offset:1280\n"
      "ds_write_b128 v94, a[200:203] offset:2560\n"
      "ds_write_b128 v94, a[204:207] offset:3840\n"
      "ds_read_b64_tr_b16 a[192:193], v95 offset:0\n"
      "ds_read_b64_tr_b16 a[194:195], v95 offset:2560\n"
      "ds_read_b64_tr_b16 a[196:197], v95 offset:32\n"
      "ds_read_b64_tr_b16 a[198:199], v95 offset:2592\n"
      "ds_read_b64_tr_b16 a[200:201], v95 offset:64\n"
      "ds_read_b64_tr_b16 a[202:203], v95 offset:2624\n"
      "ds_read_b64_tr_b16 a[204:205], v95 offset:96\n"
      "ds_read_b64_tr_b16 a[206:207], v95 offset:2656\n"
      "s_waitcnt lgkmcnt(2)\n"
      "s_waitcnt vmcnt(24)\n"
      "ds_write_b128 v94, a[208:211] offset:0\n"
      "ds_write_b128 v94, a[212:215] offset:1280\n"
      "ds_write_b128 v94, a[216:219] offset:2560\n"
      "ds_write_b128 v94, a[220:223] offset:3840\n"
      "ds_read_b64_tr_b16 a[208:209], v95 offset:0\n"
      "ds_read_b64_tr_b16 a[210:211], v95 offset:2560\n"
      "ds_read_b64_tr_b16 a[212:213], v95 offset:32\n"
      "ds_read_b64_tr_b16 a[214:215], v95 offset:2592\n"
      "ds_read_b64_tr_b16 a[216:217], v95 offset:64\n"
      "ds_read_b64_tr_b16 a[218:219], v95 offset:2624\n"
      "ds_read_b64_tr_b16 a[220:221], v95 offset:96\n"
      "ds_read_b64_tr_b16 a[222:223], v95 offset:2656\n"
      "s_waitcnt lgkmcnt(2)\n"
      "s_waitcnt vmcnt(12)\n"
      "ds_write_b128 v94, a[224:227] offset:0\n"
      "ds_write_b128 v94, a[228:231] offset:1280\n"
      "ds_write_b128 v94, a[232:235] offset:2560\n"
      "ds_write_b128 v94, a[236:239] offset:3840\n"
      "ds_read_b64_tr_b16 a[224:225], v95 offset:0\n"
      "ds_read_b64_tr_b16 a[226:227], v95 offset:2560\n"
      "ds_read_b64_tr_b16 a[228:229], v95 offset:32\n"
      "ds_read_b64_tr_b16 a[230:231], v95 offset:2592\n"
      "ds_read_b64_tr_b16 a[232:233], v95 offset:64\n"
      "ds_read_b64_tr_b16 a[234:235], v95 offset:2624\n"
      "ds_read_b64_tr_b16 a[236:237], v95 offset:96\n"
      "ds_read_b64_tr_b16 a[238:239], v95 offset:2656\n"
      "s_waitcnt lgkmcnt(2)\n"
      "s_waitcnt vmcnt(8)\n"
      "ds_write_b128 v94, a[240:243] offset:0\n"
      "ds_write_b128 v94, a[244:247] offset:1280\n"
      "ds_write_b128 v94, a[248:251] offset:2560\n"
      "ds_write_b128 v94, a[252:255] offset:3840\n"
      "ds_read_b64_tr_b16 a[240:241], v95 offset:0\n"
      "ds_read_b64_tr_b16 a[242:243], v95 offset:2560\n"
      "ds_read_b64_tr_b16 a[244:245], v95 offset:32\n"
      "ds_read_b64_tr_b16 a[246:247], v95 offset:2592\n"
      "ds_read_b64_tr_b16 a[248:249], v95 offset:64\n"
      "ds_read_b64_tr_b16 a[250:251], v95 offset:2624\n"
      "ds_read_b64_tr_b16 a[252:253], v95 offset:96\n"
      "ds_read_b64_tr_b16 a[254:255], v95 offset:2656\n"
      "s_waitcnt lgkmcnt(2)\n"
      "s_mov_b32 s38, 0\n"
      "s_mov_b32 s39, 0x8000\n"
      "s_mov_b32 s40, 0x10000\n"
      "v_mov_b32 v98, v91\n"
      "v_mov_b32 v102, 0\n"
      "v_mov_b32 v103, s85\n"
      "s_waitcnt lgkmcnt(0)\n"
      "s_barrier\n"
      "v_mov_b32 v101, 0x7ffffff0\n"
      "s_mov_b32 s41, s13\n"
      "s_mov_b32 s17, s13\n"
      "ds_read_b128 v[128:131], v98 offset:0\n"
      "ds_read_b128 v[132:135], v98 offset:1024\n"
      "ds_read_b128 v[136:139], v98 offset:2048\n"
      "ds_read_b128 v[140:143], v98 offset:3072\n"
      "ds_read_b128 v[144:147], v98 offset:4096\n"
      "ds_read_b128 v[148:151], v98 offset:5120\n"
      ".p2align 6\n"
      "LV_loop:\n"
      "s_waitcnt lgkmcnt(5)\n"
      "v_mfma_f32_16x16x32_bf16 v[0:3], a[0:3], v[128:131], 0\n"
      "ds_read_b128 v[152:155], v98 offset:6144\n"
      "v_mfma_f32_16x16x32_bf16 v[4:7], a[4:7], v[128:131], 0\n"
      "s_waitcnt lgkmcnt(5)\n"
      "v_mfma_f32_16x16x32_bf16 v[0:3], a[8:11], v[132:135], v[0:3]\n"
      "ds_read_b128 v[156:159], v98 offset:7168\n"
      "v_mfma_f32_16x16x32_bf16 v[4:7], a[12:15], v[132:135], v[4:7]\n"
      "v_cvt_pk_bf16_f32 v250, v24, v25\n"
      "v_cvt_pk_bf16_f32 v251, v26, v27\n"
      "s_waitcnt lgkmcnt(5)\n"
      "v_mfma_f32_16x16x32_bf16 v[0:3], a[16:19], v[136:139], v[0:3]\n"
      "ds_read_b128 v[128:131], v98 offset:8192\n"
      "v_mfma_f32_16x16x32_bf16 v[4:7], a[20:23], v[136:139], v[4:7]\n"
      "buffer_store_dwordx4 v[240:243], v101, s[8:11], s41 offen offset:0 sc1\n"
      "s_waitcnt lgkmcnt(5)\n"
      "v_mfma_f32_16x16x32_bf16 v[0:3], a[24:27], v[140:143], v[0:3]\n"
      "ds_read_b128 v[132:135], v98 offset:9216\n"
      "v_mfma_f32_16x16x32_bf16 v[4:7], a[28:31], v[140:143], v[4:7]\n"
      "v_cvt_pk_bf16_f32 v254, v28, v29\n"
      "v_cvt_pk_bf16_f32 v255, v30, v31\n"
      "s_waitcnt lgkmcnt(5)\n"
      "v_mfma_f32_16x16x32_bf16 v[0:3], a[32:35], v[144:147], v[0:3]\n"
      "ds_read_b128 v[136:139], v98 offset:10240\n"
      "v_mfma_f32_16x16x32_bf16 v[4:7], a[36:39], v[144:147], v[4:7]\n"
      "s_waitcnt lgkmcnt(5)\n"
      "v_mfma_f32_16x16x32_bf16 v[0:3], a[40:43], v[148:151], v[0:3]\n"
      "ds_read_b128 v[140:143], v98 offset:11264\n"
      "v_mfma_f32_16x16x32_bf16 v[4:7], a[44:47], v[148:151], v[4:7]\n"
      "s_add_u32 s12, s12, 0x8000\n"
      "s_add_u32 s31, s40, s15\n"
      "buffer_store_dwordx4 v[244:247], v101, s[8:11], s41 offen offset:1024 sc1\n"
      "s_waitcnt lgkmcnt(5)\n"
      "v_mfma_f32_16x16x32_bf16 v[0:3], a[48:51], v[152:155], v[0:3]\n"
      "ds_read_b128 v[144:147], v98 offset:12288\n"
      "v_mfma_f32_16x16x32_bf16 v[4:7], a[52:55], v[152:155], v[4:7]\n"
      "s_waitcnt lgkmcnt(5)\n"
      "v_mfma_f32_16x16x32_bf16 v[0:3], a[56:59], v[156:159], v[0:3]\n"
      "ds_read_b128 v[148:151], v98 offset:13312\n"
      "v_mfma_f32_16x16x32_bf16 v[4:7], a[60:63], v[156:159], v[4:7]\n"
      "s_waitcnt lgkmcnt(5)\n"
      "v_mfma_f32_16x16x32_bf16 v[0:3], a[64:67], v[128:131], v[0:3]\n"
      "ds_read_b128 v[152:155], v98 offset:14336\n"
      "v_mfma_f32_16x16x32_bf16 v[4:7], a[68:71], v[128:131], v[4:7]\n"
      "buffer_store_dwordx4 v[248:251], v101, s[8:11], s41 offen offset:2048 sc1\n"
      "s_waitcnt lgkmcnt(5)\n"
      "v_mfma_f32_16x16x32_bf16 v[0:3], a[72:75], v[132:135], v[0:3]\n"
      "ds_read_b128 v[156:159], v98 offset:15360\n"
      "v_mfma_f32_16x16x32_bf16 v[4:7], a[76:79], v[132:135], v[4:7]\n"
      "s_waitcnt lgkmcnt(5)\n"
      "v_mfma_f32_16x16x32_bf16 v[0:3], a[80:83], v[136:139], v[0:3]\n"
      "ds_read_b128 v[128:131], v98 offset:16384\n"
      "v_mfma_f32_16x16x32_bf16 v[4:7], a[84:87], v[136:139], v[4:7]\n"
      "s_waitcnt lgkmcnt(5)\n"
      "v_mfma_f32_16x16x32_bf16 v[0:3], a[88:91], v[140:143], v[0:3]\n"
      "ds_read_b128 v[132:135], v98 offset:17408\n"
      "v_mfma_f32_16x16x32_bf16 v[4:7], a[92:95], v[140:143], v[4:7]\n"
      "buffer_store_dwordx4 v[252:255], v101, s[8:11], s41 offen offset:3072 sc1\n"
      "s_waitcnt lgkmcnt(5)\n"
      "v_mfma_f32_16x16x32_bf16 v[0:3], a[96:99], v[144:147], v[0:3]\n"
      "ds_read_b128 v[136:139], v98 offset:18432\n"
      "v_mfma_f32_16x16x32_bf16 v[4:7], a[100:103], v[144:147], v[4:7]\n"
      "s_waitcnt lgkmcnt(5)\n"
      "v_mfma_f32_16x16x32_bf16 v[0:3], a[104:107], v[148:151], v[0:3]\n"
      "ds_read_b128 v[140:143], v98 offset:19456\n"
      "v_mfma_f32_16x16x32_bf16 v[4:7], a[108:111], v[148:151], v[4:7]\n"
      "s_waitcnt lgkmcnt(5)\n"
      "v_mfma_f32_16x16x32_bf16 v[0:3], a[112:115], v[152:155], v[0:3]\n"
      "ds_read_b128 v[144:147], v98 offset:20480\n"
      "v_mfma_f32_16x16x32_bf16 v[4:7], a[116:119], v[152:155], v[4:7]\n"
      "s_mov_b32 m0, s31\n"
      "s_add_u32 s29, s12, 4096\n"
      "buffer_load_dwordx4 v91, s[8:11], s12 offen sc1 lds\n"
      "s_waitcnt lgkmcnt(5)\n"
      "v_mfma_f32_16x16x32_bf16 v[0:3], a[120:123], v[156:159], v[0:3]\n"
      "ds_read_b128 v[148:151], v98 offset:21504\n"
      "v_mfma_f32_16x16x32_bf16 v[4:7], a[124:127], v[156:159], v[4:7]\n"
      "s_waitcnt lgkmcnt(5)\n"
      "v_mfma_f32_16x16x32_bf16 v[8:11], a[0:3], v[128:131], 0\n"
      "ds_read_b128 v[152:155], v98 offset:22528\n"
      "v_mfma_f32_16x16x32_bf16 v[12:15], a[4:7], v[128:131], 0\n"
      "s_waitcnt lgkmcnt(5)\n"
      "v_mfma_f32_16x16x32_bf16 v[8:11], a[8:11], v[132:135], v[8:11]\n"
      "ds_read_b128 v[156:159], v98 offset:23552\n"
      "v_mfma_f32_16x16x32_bf16 v[12:15], a[12:15], v[132:135], v[12:15]\n"
      "s_waitcnt lgkmcnt(5)\n"
      "v_mfma_f32_16x16x32_bf16 v[8:11], a[16:19], v[136:139], v[8:11]\n"
      "ds_read_b128 v[128:131], v98 offset:24576\n"
      "v_mfma_f32_16x16x32_bf16 v[12:15], a[20:23], v[136:139], v[12:15]\n"
      "v_cvt_pk_bf16_f32 v48, v0, v1\n"
      "v_cvt_pk_bf16_f32 v49, v2, v3\n"
      "s_waitcnt lgkmcnt(5)\n"
      "v_mfma_f32_16x16x32_bf16 v[8:11], a[24:27], v[140:143], v[8:11]\n"
      "ds_read_b128 v[132:135], v98 offset:25600\n"
      "v_mfma_f32_16x16x32_bf16 v[12:15], a[28:31], v[140:143], v[12:15]\n"
      "v_cvt_pk_bf16_f32 v50, v4, v5\n"
      "v_cvt_pk_bf16_f32 v51, v6, v7\n"
      "buffer_load_dwordx4 v91, s[8:11], s12 offen offset:1024 sc1 lds\n"
      "s_waitcnt lgkmcnt(5)\n"
      "v_mfma_f32_16x16x32_bf16 v[8:11], a[32:35], v[144:147], v[8:11]\n"
      "ds_read_b128 v[136:139], v98 offset:26624\n"
      "v_mfma_f32_16x16x32_bf16 v[12:15], a[36:39], v[144:147], v[12:15]\n"
      "ds_write_b128 v96, v[48:51] offset:0\n"
      "s_waitcnt lgkmcnt(6)\n"
      "v_mfma_f32_16x16x32_bf16 v[8:11], a[40:43], v[148:151], v[8:11]\n"
      "ds_read_b128 v[140:143], v98 offset:27648\n"
      "v_mfma_f32_16x16x32_bf16 v[12:15], a[44:47], v[148:151], v[12:15]\n"
      "s_waitcnt lgkmcnt(6)\n"
      "v_mfma_f32_16x16x32_bf16 v[8:11], a[48:51], v[152:155], v[8:11]\n"
      "ds_read_b128 v[144:147], v98 offset:28672\n"
      "v_mfma_f32_16x16x32_bf16 v[12:15], a[52:55], v[152:155], v[12:15]\n"
      "s_waitcnt lgkmcnt(6)\n"
      "v_mfma_f32_16x16x32_bf16 v[8:11], a[56:59], v[156:159], v[8:11]\n"
      "ds_read_b128 v[148:151], v98 offset:29696\n"
      "v_mfma_f32_16x16x32_bf16 v[12:15], a[60:63], v[156:159], v[12:15]\n"
      "s_waitcnt lgkmcnt(6)\n"
      "v_mfma_f32_16x16x32_bf16 v[8:11], a[64:67], v[128:131], v[8:11]\n"
      "ds_read_b128 v[152:155], v98 offset:30720\n"
      "v_mfma_f32_16x16x32_bf16 v[12:15], a[68:71], v[128:131], v[12:15]\n"
      "s_waitcnt lgkmcnt(4)\n"
      "s_barrier\n"
      "buffer_load_dwordx4 v91, s[8:11], s12 offen offset:2048 sc1 lds\n"
      "s_waitcnt lgkmcnt(6)\n"
      "v_mfma_f32_16x16x32_bf16 v[8:11], a[72:75], v[132:135], v[8:11]\n"
      "ds_read_b128 v[156:159], v98 offset:31744\n"
      "v_mfma_f32_16x16x32_bf16 v[12:15], a[76:79], v[132:135], v[12:15]\n"
      "ds_read_b128 v[160:163], v97 offset:0\n"
      "s_waitcnt lgkmcnt(7)\n"
      "v_mfma_f32_16x16x32_bf16 v[8:11], a[80:83], v[136:139], v[8:11]\n"
      "v_mfma_f32_16x16x32_bf16 v[12:15], a[84:87], v[136:139], v[12:15]\n"
      "ds_read_b128 v[164:167], v97 offset:1024\n"
      "s_waitcnt lgkmcnt(6)\n"
      "v_mfma_f32_16x16x32_bf16 v[8:11], a[88:91], v[140:143], v[8:11]\n"
      "v_mfma_f32_16x16x32_bf16 v[12:15], a[92:95], v[140:143], v[12:15]\n"
      "ds_read_b128 v[168:171], v97 offset:2048\n"
      "s_waitcnt lgkmcnt(6)\n"
      "v_mfma_f32_16x16x32_bf16 v[8:11], a[96:99], v[144:147], v[8:11]\n"
      "v_mfma_f32_16x16x32_bf16 v[12:15], a[100:103], v[144:147], v[12:15]\n"
      "ds_read_b128 v[172:175], v97 offset:3072\n"
      "s_waitcnt lgkmcnt(6)\n"
      "v_mfma_f32_16x16x32_bf16 v[8:11], a[104:107], v[148:151], v[8:11]\n"
      "v_mfma_f32_16x16x32_bf16 v[12:15], a[108:111], v[148:151], v[12:15]\n"
      "buffer_load_dwordx4 v91, s[8:11], s12 offen offset:3072 sc1 lds\n"
      "s_waitcnt lgkmcnt(5)\n"
      "v_mfma_f32_16x16x32_bf16 v[8:11], a[112:115], v[152:155], v[8:11]\n"
      "v_mfma_f32_16x16x32_bf16 v[12:15], a[116:119], v[152:155], v[12:15]\n"
      "s_waitcnt lgkmcnt(4)\n"
      "v_mfma_f32_16x16x32_bf16 v[8:11], a[120:123], v[156:159], v[8:11]\n"
      "v_mfma_f32_16x16x32_bf16 v[12:15], a[124:127], v[156:159], v[12:15]\n"
      "s_waitcnt lgkmcnt(3)\n"
      "v_mfma_f32_16x16x32_bf16 v[192:195], v[160:163], a[128:131], 0\n"
      "s_waitcnt lgkmcnt(2)\n"
      "v_mfma_f32_16x16x32_bf16 v[192:195], v[164:167], a[144:147], v[192:195]\n"
      "s_waitcnt lgkmcnt(1)\n"
      "v_mfma_f32_16x16x32_bf16 v[192:195], v[168:171], a[160:163], v[192:195]\n"
      "v_add_u32 v98, s39, v91\n"
      "s_mov_b32 s39, s40\n"
      "s_waitcnt lgkmcnt(0)\n"
      "v_mfma_f32_16x16x32_bf16 v[192:195], v[172:175], a[176:179], v[192:195]\n"
      "s_add_u32 s40, s40, 0x8000\n"
      "s_cmp_eq_u32 s40, 0x18000\n"
      "v_mfma_f32_16x16x32_bf16 v[196:199], v[160:163], a[132:135], 0\n"
      "s_cselect_b32 s40, 0, s40\n"
      "v_mov_b32 v101, v91\n"
      "v_mfma_f32_16x16x32_bf16 v[196:199], v[164:167], a[148:151], v[196:199]\n"
      "v_cvt_pk_bf16_f32 v52, v8, v9\n"
      "v_cvt_pk_bf16_f32 v53, v10, v11\n"
      "s_add_u32 m0, s31, 4096\n"
      "s_nop 0\n"
      "buffer_load_dwordx4 v91, s[8:11], s29 offen sc1 lds\n"
      "v_mfma_f32_16x16x32_bf16 v[196:199], v[168:171], a[164:167], v[196:199]\n"
      "v_mfma_f32_16x16x32_bf16 v[196:199], v[172:175], a[180:183], v[196:199]\n"
      "v_cvt_pk_bf16_f32 v54, v12, v13\n"
      "v_cvt_pk_bf16_f32 v55, v14, v15\n"
      "v_mfma_f32_16x16x32_bf16 v[200:203], v[160:163], a[136:139], 0\n"
      "v_mfma_f32_16x16x32_bf16 v[200:203], v[164:167], a[152:155], v[200:203]\n"
      "ds_write_b128 v96, v[52:55] offset:4096\n"
      "v_mfma_f32_16x16x32_bf16 v[200:203], v[168:171], a[168:171], v[200:203]\n"
      "v_cvt_pk_bf16_f32 v224, v192, v193\n"
      "v_cvt_pk_bf16_f32 v225, v194, v195\n"
      "v_mfma_f32_16x16x32_bf16 v[200:203], v[172:175], a[184:187], v[200:203]\n"
      "v_mfma_f32_16x16x32_bf16 v[204:207], v[160:163], a[140:143], 0\n"
      "v_mfma_f32_16x16x32_bf16 v[204:207], v[164:167], a[156:159], v[204:207]\n"
      "v_mfma_f32_16x16x32_bf16 v[204:207], v[168:171], a[172:175], v[204:207]\n"
      "v_cvt_pk_bf16_f32 v228, v196, v197\n"
      "v_cvt_pk_bf16_f32 v229, v198, v199\n"
      "v_mfma_f32_16x16x32_bf16 v[204:207], v[172:175], a[188:191], v[204:207]\n"
      "buffer_load_dwordx4 v91, s[8:11], s29 offen offset:1024 sc1 lds\n"
      "v_mfma_f32_16x16x32_bf16 v[208:211], v[160:163], a[192:195], 0\n"
      "v_mfma_f32_16x16x32_bf16 v[208:211], v[164:167], a[208:211], v[208:211]\n"
      "s_waitcnt lgkmcnt(0)\n"
      "s_barrier\n"
      "v_mfma_f32_16x16x32_bf16 v[208:211], v[168:171], a[224:227], v[208:211]\n"
      "v_cvt_pk_bf16_f32 v232, v200, v201\n"
      "v_cvt_pk_bf16_f32 v233, v202, v203\n"
      "v_mfma_f32_16x16x32_bf16 v[208:211], v[172:175], a[240:243], v[208:211]\n"
      "ds_read_b128 v[176:179], v97 offset:4096\n"
      "v_mfma_f32_16x16x32_bf16 v[212:215], v[160:163], a[196:199], 0\n"
      "v_mfma_f32_16x16x32_bf16 v[212:215], v[164:167], a[212:215], v[212:215]\n"
      "ds_read_b128 v[180:183], v97 offset:5120\n"
      "v_mfma_f32_16x16x32_bf16 v[212:215], v[168:171], a[228:231], v[212:215]\n"
      "v_cvt_pk_bf16_f32 v236, v204, v205\n"
      "v_cvt_pk_bf16_f32 v237, v206, v207\n"
      "v_mfma_f32_16x16x32_bf16 v[212:215], v[172:175], a[244:247], v[212:215]\n"
      "ds_read_b128 v[184:187], v97 offset:6144\n"
      "v_mfma_f32_16x16x32_bf16 v[216:219], v[160:163], a[200:203], 0\n"
      "v_mfma_f32_16x16x32_bf16 v[216:219], v[164:167], a[216:219], v[216:219]\n"
      "ds_read_b128 v[188:191], v97 offset:7168\n"
      "buffer_load_dwordx4 v91, s[8:11], s29 offen offset:2048 sc1 lds\n"
      "v_mfma_f32_16x16x32_bf16 v[216:219], v[168:171], a[232:235], v[216:219]\n"
      "v_cvt_pk_bf16_f32 v240, v208, v209\n"
      "v_cvt_pk_bf16_f32 v241, v210, v211\n"
      "v_mfma_f32_16x16x32_bf16 v[216:219], v[172:175], a[248:251], v[216:219]\n"
      "v_mfma_f32_16x16x32_bf16 v[220:223], v[160:163], a[204:207], 0\n"
      "v_mfma_f32_16x16x32_bf16 v[220:223], v[164:167], a[220:223], v[220:223]\n"
      "v_mfma_f32_16x16x32_bf16 v[220:223], v[168:171], a[236:239], v[220:223]\n"
      "v_cvt_pk_bf16_f32 v244, v212, v213\n"
      "v_cvt_pk_bf16_f32 v245, v214, v215\n"
      "v_mfma_f32_16x16x32_bf16 v[220:223], v[172:175], a[252:255], v[220:223]\n"
      "s_waitcnt lgkmcnt(3)\n"
      "v_mfma_f32_16x16x32_bf16 v[16:19], v[176:179], a[128:131], 0\n"
      "s_waitcnt lgkmcnt(2)\n"
      "v_mfma_f32_16x16x32_bf16 v[16:19], v[180:183], a[144:147], v[16:19]\n"
      "s_add_u32 s41, s13, 4096\n"
      "s_mov_b32 s17, s13\n"
      "s_waitcnt lgkmcnt(1)\n"
      "v_mfma_f32_16x16x32_bf16 v[16:19], v[184:187], a[160:163], v[16:19]\n"
      "v_cvt_pk_bf16_f32 v248, v216, v217\n"
      "v_cvt_pk_bf16_f32 v249, v218, v219\n"
      "s_waitcnt lgkmcnt(0)\n"
      "v_mfma_f32_16x16x32_bf16 v[16:19], v[188:191], a[176:179], v[16:19]\n"
      "buffer_load_dwordx4 v91, s[8:11], s29 offen offset:3072 sc1 lds\n"
      "v_mfma_f32_16x16x32_bf16 v[20:23], v[176:179], a[132:135], 0\n"
      "v_mfma_f32_16x16x32_bf16 v[20:23], v[180:183], a[148:151], v[20:23]\n"
      "v_mfma_f32_16x16x32_bf16 v[20:23], v[184:187], a[164:167], v[20:23]\n"
      "v_cvt_pk_bf16_f32 v252, v220, v221\n"
      "v_cvt_pk_bf16_f32 v253, v222, v223\n"
      "v_mfma_f32_16x16x32_bf16 v[20:23], v[188:191], a[180:183], v[20:23]\n"
      "s_waitcnt vmcnt(8)\n"
      "s_barrier\n"
      "v_mfma_f32_16x16x32_bf16 v[24:27], v[176:179], a[136:139], 0\n"
      "ds_read_b128 v[128:131], v98 offset:0\n"
      "v_mfma_f32_16x16x32_bf16 v[24:27], v[180:183], a[152:155], v[24:27]\n"
      "buffer_store_dwordx2 v[102:103], v92, s[8:11], s14 offen sc1\n"
      "v_mfma_f32_16x16x32_bf16 v[24:27], v[184:187], a[168:171], v[24:27]\n"
      "v_add_u32 v102, 1, v102\n"
      "v_mfma_f32_16x16x32_bf16 v[24:27], v[188:191], a[184:187], v[24:27]\n"
      "v_cvt_pk_bf16_f32 v226, v16, v17\n"
      "v_cvt_pk_bf16_f32 v227, v18, v19\n"
      "v_mfma_f32_16x16x32_bf16 v[28:31], v[176:179], a[140:143], 0\n"
      "ds_read_b128 v[132:135], v98 offset:1024\n"
      "v_mfma_f32_16x16x32_bf16 v[28:31], v[180:183], a[156:159], v[28:31]\n"
      "buffer_store_dwordx4 v[224:227], v91, s[8:11], s17 offen offset:0 sc1\n"
      "v_mfma_f32_16x16x32_bf16 v[28:31], v[184:187], a[172:175], v[28:31]\n"
      "v_mfma_f32_16x16x32_bf16 v[28:31], v[188:191], a[188:191], v[28:31]\n"
      "v_cvt_pk_bf16_f32 v230, v20, v21\n"
      "v_cvt_pk_bf16_f32 v231, v22, v23\n"
      "v_mfma_f32_16x16x32_bf16 v[16:19], v[176:179], a[192:195], 0\n"
      "ds_read_b128 v[136:139], v98 offset:2048\n"
      "v_mfma_f32_16x16x32_bf16 v[16:19], v[180:183], a[208:211], v[16:19]\n"
      "v_mfma_f32_16x16x32_bf16 v[16:19], v[184:187], a[224:227], v[16:19]\n"
      "v_mfma_f32_16x16x32_bf16 v[16:19], v[188:191], a[240:243], v[16:19]\n"
      "buffer_store_dwordx4 v[228:231], v91, s[8:11], s17 offen offset:1024 sc1\n"
      "v_cvt_pk_bf16_f32 v234, v24, v25\n"
      "v_cvt_pk_bf16_f32 v235, v26, v27\n"
      "v_mfma_f32_16x16x32_bf16 v[20:23], v[176:179], a[196:199], 0\n"
      "ds_read_b128 v[140:143], v98 offset:3072\n"
      "v_mfma_f32_16x16x32_bf16 v[20:23], v[180:183], a[212:215], v[20:23]\n"
      "v_mfma_f32_16x16x32_bf16 v[20:23], v[184:187], a[228:231], v[20:23]\n"
      "v_mfma_f32_16x16x32_bf16 v[20:23], v[188:191], a[244:247], v[20:23]\n"
      "v_cvt_pk_bf16_f32 v238, v28, v29\n"
      "v_cvt_pk_bf16_f32 v239, v30, v31\n"
      "v_mfma_f32_16x16x32_bf16 v[24:27], v[176:179], a[200:203], 0\n"
      "ds_read_b128 v[144:147], v98 offset:4096\n"
      "v_mfma_f32_16x16x32_bf16 v[24:27], v[180:183], a[216:219], v[24:27]\n"
      "buffer_store_dwordx4 v[232:235], v91, s[8:11], s17 offen offset:2048 sc1\n"
      "v_mfma_f32_16x16x32_bf16 v[24:27], v[184:187], a[232:235], v[24:27]\n"
      "s_cmp_eq_u32 s38, 31\n"
      "s_cselect_b32 s28, s16, 0\n"
      "v_mfma_f32_16x16x32_bf16 v[24:27], v[188:191], a[248:251], v[24:27]\n"
      "v_cvt_pk_bf16_f32 v242, v16, v17\n"
      "v_cvt_pk_bf16_f32 v243, v18, v19\n"
      "v_mfma_f32_16x16x32_bf16 v[28:31], v[176:179], a[204:207], 0\n"
      "s_add_u32 s13, s13, 0x10000\n"
      "s_add_u32 s13, s13, s28\n"
      "ds_read_b128 v[148:151], v98 offset:5120\n"
      "v_mfma_f32_16x16x32_bf16 v[28:31], v[180:183], a[220:223], v[28:31]\n"
      "v_mfma_f32_16x16x32_bf16 v[28:31], v[184:187], a[236:239], v[28:31]\n"
      "s_add_u32 s38, s38, 1\n"
      "s_cmp_lt_u32 s38, 64\n"
      "v_mfma_f32_16x16x32_bf16 v[28:31], v[188:191], a[252:255], v[28:31]\n"
      "buffer_store_dwordx4 v[236:239], v91, s[8:11], s17 offen offset:3072 sc1\n"
      "v_cvt_pk_bf16_f32 v246, v20, v21\n"
      "v_cvt_pk_bf16_f32 v247, v22, v23\n"
      "s_cbranch_scc1 LV_loop\n"
      "buffer_store_dwordx4 v[240:243], v91, s[8:11], s41 offen offset:0 sc1\n"
      "buffer_store_dwordx4 v[244:247], v91, s[8:11], s41 offen offset:1024 sc1\n"
      "v_cvt_pk_bf16_f32 v250, v24, v25\n"
      "v_cvt_pk_bf16_f32 v251, v26, v27\n"
      "buffer_store_dwordx4 v[248:251], v91, s[8:11], s41 offen offset:2048 sc1\n"
      "v_cvt_pk_bf16_f32 v254, v28, v29\n"
      "v_cvt_pk_bf16_f32 v255, v30, v31\n"
      "buffer_store_dwordx4 v[252:255], v91, s[8:11], s41 offen offset:3072 sc1\n"
      "s_waitcnt vmcnt(0) lgkmcnt(0)\n"
      "s_barrier\n"
      "v_mov_b32 v102, 0x64\n"
      "buffer_store_dwordx2 v[102:103], v92, s[8:11], s14 offen sc1\n"
      "s_branch Lend\n"
      "LP1_init:\n"
      "s_mov_b32 s31, s15\n"
      "s_mov_b32 s62, 0x100000\n"
      "s_mov_b32 s28, 0x0\n"
      "buffer_load_dwordx4 a[0:3], v88, s[68:71], s28 offen\n"
      "s_mov_b32 s28, 0x3800\n"
      "buffer_load_dwordx4 a[4:7], v88, s[68:71], s28 offen\n"
      "s_mov_b32 s28, 0x0\n"
      "buffer_load_dwordx4 a[8:11], v88, s[72:75], s28 offen\n"
      "s_mov_b32 s28, 0x3800\n"
      "buffer_load_dwordx4 a[12:15], v88, s[72:75], s28 offen\n"
      "s_mov_b32 s28, 0x1c000\n"
      "buffer_load_dwordx4 a[16:19], v88, s[68:71], s28 offen\n"
      "s_mov_b32 s28, 0x1f800\n"
      "buffer_load_dwordx4 a[20:23], v88, s[68:71], s28 offen\n"
      "s_mov_b32 s28, 0x1c000\n"
      "buffer_load_dwordx4 a[24:27], v88, s[72:75], s28 offen\n"
      "s_mov_b32 s28, 0x1f800\n"
      "buffer_load_dwordx4 a[28:31], v88, s[72:75], s28 offen\n"
      "s_mov_b32 s28, 0x38000\n"
      "buffer_load_dwordx4 a[32:35], v88, s[68:71], s28 offen\n"
      "s_mov_b32 s28, 0x3b800\n"
      "buffer_load_dwordx4 a[36:39], v88, s[68:71], s28 offen\n"
      "s_mov_b32 s28, 0x38000\n"
      "buffer_load_dwordx4 a[40:43], v88, s[72:75], s28 offen\n"
      "s_mov_b32 s28, 0x3b800\n"
      "buffer_load_dwordx4 a[44:47], v88, s[72:75], s28 offen\n"
      "s_mov_b32 s28, 0x54000\n"
      "buffer_load_dwordx4 a[48:51], v88, s[68:71], s28 offen\n"
      "s_mov_b32 s28, 0x57800\n"
      "buffer_load_dwordx4 a[52:55], v88, s[68:71], s28 offen\n"
      "s_mov_b32 s28, 0x54000\n"
      "buffer_load_dwordx4 a[56:59], v88, s[72:75], s28 offen\n"
      "s_mov_b32 s28, 0x57800\n"
      "buffer_load_dwordx4 a[60:63], v88, s[72:75], s28 offen\n"
      "s_waitcnt vmcnt(12)\n"
      "s_waitcnt lgkmcnt(0)\n"
      "s_add_u32 s85, s85, 1\n"
      "ds_write_b128 v89, a[0:3] offset:0\n"
      "ds_write_b128 v89, a[4:7] offset:1536\n"
      "ds_write_b128 v89, a[8:11] offset:3072\n"
      "ds_write_b128 v89, a[12:15] offset:4608\n"
      "s_mov_b32 s28, 0x70000\n"
      "buffer_load_dwordx4 a[64:67], v88, s[68:71], s28 offen\n"
      "s_mov_b32 s28, 0x73800\n"
      "buffer_load_dwordx4 a[68:71], v88, s[68:71], s28 offen\n"
      "s_mov_b32 s28, 0x70000\n"
      "buffer_load_dwordx4 a[72:75], v88, s[72:75], s28 offen\n"
      "s_mov_b32 s28, 0x73800\n"
      "buffer_load_dwordx4 a[76:79], v88, s[72:75], s28 offen\n"
      "ds_read_b64_tr_b16 a[0:1], v90 offset:0\n"
      "ds_read_b64_tr_b16 a[4:5], v90 offset:32\n"
      "ds_read_b64_tr_b16 a[2:3], v90 offset:1536\n"
      "ds_read_b64_tr_b16 a[6:7], v90 offset:1568\n"
      "ds_read_b64_tr_b16 a[8:9], v90 offset:3072\n"
      "ds_read_b64_tr_b16 a[12:13], v90 offset:3104\n"
      "ds_read_b64_tr_b16 a[10:11], v90 offset:4608\n"
      "ds_read_b64_tr_b16 a[14:15], v90 offset:4640\n"
      "s_waitcnt lgkmcnt(2)\n"
      "s_waitcnt vmcnt(12)\n"
      "ds_write_b128 v89, a[16:19] offset:0\n"
      "ds_write_b128 v89, a[20:23] offset:1536\n"
      "ds_write_b128 v89, a[24:27] offset:3072\n"
      "ds_write_b128 v89, a[28:31] offset:4608\n"
      "s_mov_b32 s28, 0x8c000\n"
      "buffer_load_dwordx4 a[80:83], v88, s[68:71], s28 offen\n"
      "s_mov_b32 s28, 0x8f800\n"
      "buffer_load_dwordx4 a[84:87], v88, s[68:71], s28 offen\n"
      "s_mov_b32 s28, 0x8c000\n"
      "buffer_load_dwordx4 a[88:91], v88, s[72:75], s28 offen\n"
      "s_mov_b32 s28, 0x8f800\n"
      "buffer_load_dwordx4 a[92:95], v88, s[72:75], s28 offen\n"
      "ds_read_b64_tr_b16 a[16:17], v90 offset:0\n"
      "ds_read_b64_tr_b16 a[20:21], v90 offset:32\n"
      "ds_read_b64_tr_b16 a[18:19], v90 offset:1536\n"
      "ds_read_b64_tr_b16 a[22:23], v90 offset:1568\n"
      "ds_read_b64_tr_b16 a[24:25], v90 offset:3072\n"
      "ds_read_b64_tr_b16 a[28:29], v90 offset:3104\n"
      "ds_read_b64_tr_b16 a[26:27], v90 offset:4608\n"
      "ds_read_b64_tr_b16 a[30:31], v90 offset:4640\n"
      "s_waitcnt lgkmcnt(2)\n"
      "s_waitcnt vmcnt(12)\n"
      "ds_write_b128 v89, a[32:35] offset:0\n"
      "ds_write_b128 v89, a[36:39] offset:1536\n"
      "ds_write_b128 v89, a[40:43] offset:3072\n"
      "ds_write_b128 v89, a[44:47] offset:4608\n"
      "s_mov_b32 s28, 0xa8000\n"
      "buffer_load_dwordx4 a[96:99], v88, s[68:71], s28 offen\n"
      "s_mov_b32 s28, 0xab800\n"
      "buffer_load_dwordx4 a[100:103], v88, s[68:71], s28 offen\n"
      "s_mov_b32 s28, 0xa8000\n"
      "buffer_load_dwordx4 a[104:107], v88, s[72:75], s28 offen\n"
      "s_mov_b32 s28, 0xab800\n"
      "buffer_load_dwordx4 a[108:111], v88, s[72:75], s28 offen\n"
      "ds_read_b64_tr_b16 a[32:33], v90 offset:0\n"
      "ds_read_b64_tr_b16 a[36:37], v90 offset:32\n"
      "ds_read_b64_tr_b16 a[34:35], v90 offset:1536\n"
      "ds_read_b64_tr_b16 a[38:39], v90 offset:1568\n"
      "ds_read_b64_tr_b16 a[40:41], v90 offset:3072\n"
      "ds_read_b64_tr_b16 a[44:45], v90 offset:3104\n"
      "ds_read_b64_tr_b16 a[42:43], v90 offset:4608\n"
      "ds_read_b64_tr_b16 a[46:47], v90 offset:4640\n"
      "s_waitcnt lgkmcnt(2)\n"
      "s_waitcnt vmcnt(12)\n"
      "ds_write_b128 v89, a[48:51] offset:0\n"
      "ds_write_b128 v89, a[52:55] offset:1536\n"
      "ds_write_b128 v89, a[56:59] offset:3072\n"
      "ds_write_b128 v89, a[60:63] offset:4608\n"
      "s_mov_b32 s28, 0xc4000\n"
      "buffer_load_dwordx4 a[112:115], v88, s[68:71], s28 offen\n"
      "s_mov_b32 s28, 0xc7800\n"
      "buffer_load_dwordx4 a[116:119], v88, s[68:71], s28 offen\n"
      "s_mov_b32 s28, 0xc4000\n"
      "buffer_load_dwordx4 a[120:123], v88, s[72:75], s28 offen\n"
      "s_mov_b32 s28, 0xc7800\n"
      "buffer_load_dwordx4 a[124:127], v88, s[72:75], s28 offen\n"
      "ds_read_b64_tr_b16 a[48:49], v90 offset:0\n"
      "ds_read_b64_tr_b16 a[52:53], v90 offset:32\n"
      "ds_read_b64_tr_b16 a[50:51], v90 offset:1536\n"
      "ds_read_b64_tr_b16 a[54:55], v90 offset:1568\n"
      "ds_read_b64_tr_b16 a[56:57], v90 offset:3072\n"
      "ds_read_b64_tr_b16 a[60:61], v90 offset:3104\n"
      "ds_read_b64_tr_b16 a[58:59], v90 offset:4608\n"
      "ds_read_b64_tr_b16 a[62:63], v90 offset:4640\n"
      "s_waitcnt lgkmcnt(2)\n"
      "s_waitcnt vmcnt(12)\n"
      "ds_write_b128 v89, a[64:67] offset:0\n"
      "ds_write_b128 v89, a[68:71] offset:1536\n"
      "ds_write_b128 v89, a[72:75] offset:3072\n"
      "ds_write_b128 v89, a[76:79] offset:4608\n"
      "s_mov_b32 s28, 0xe0000\n"
      "buffer_load_dwordx4 a[128:131], v88, s[68:71], s28 offen\n"
      "s_mov_b32 s28, 0xe3800\n"
      "buffer_load_dwordx4 a[132:135], v88, s[68:71], s28 offen\n"
      "s_mov_b32 s28, 0xe0000\n"
      "buffer_load_dwordx4 a[136:139], v88, s[72:75], s28 offen\n"
      "s_mov_b32 s28, 0xe3800\n"
      "buffer_load_dwordx4 a[140:143], v88, s[72:75], s28 offen\n"
      "buffer_load_dwordx2 v[0:1], v100, s[8:11], 0 offen sc1\n"
      "ds_read_b64_tr_b16 a[64:65], v90 offset:0\n"
      "ds_read_b64_tr_b16 a[68:69], v90 offset:32\n"
      "ds_read_b64_tr_b16 a[66:67], v90 offset:1536\n"
      "ds_read_b64_tr_b16 a[70:71], v90 offset:1568\n"
      "ds_read_b64_tr_b16 a[72:73], v90 offset:3072\n"
      "ds_read_b64_tr_b16 a[76:77], v90 offset:3104\n"
      "ds_read_b64_tr_b16 a[74:75], v90 offset:4608\n"
      "ds_read_b64_tr_b16 a[78:79], v90 offset:4640\n"
      "s_waitcnt lgkmcnt(2)\n"
      "s_waitcnt vmcnt(13)\n"
      "ds_write_b128 v89, a[80:83] offset:0\n"
      "ds_write_b128 v89, a[84:87] offset:1536\n"
      "ds_write_b128 v89, a[88:91] offset:3072\n"
      "ds_write_b128 v89, a[92:95] offset:4608\n"
      "s_mov_b32 s28, 0xfc000\n"
      "buffer_load_dwordx4 a[144:147], v88, s[68:71], s28 offen\n"
      "s_mov_b32 s28, 0xff800\n"
      "buffer_load_dwordx4 a[148:151], v88, s[68:71], s28 offen\n"
      "s_mov_b32 s28, 0xfc000\n"
      "buffer_load_dwordx4 a[152:155], v88, s[72:75], s28 offen\n"
      "s_mov_b32 s28, 0xff800\n"
      "buffer_load_dwordx4 a[156:159], v88, s[72:75], s28 offen\n"
      "ds_read_b64_tr_b16 a[80:81], v90 offset:0\n"
      "ds_read_b64_tr_b16 a[84:85], v90 offset:32\n"
      "ds_read_b64_tr_b16 a[82:83], v90 offset:1536\n"
      "ds_read_b64_tr_b16 a[86:87], v90 offset:1568\n"
      "ds_read_b64_tr_b16 a[88:89], v90 offset:3072\n"
      "ds_read_b64_tr_b16 a[92:93], v90 offset:3104\n"
      "ds_read_b64_tr_b16 a[90:91], v90 offset:4608\n"
      "ds_read_b64_tr_b16 a[94:95], v90 offset:4640\n"
      "s_waitcnt lgkmcnt(2)\n"
      "s_waitcnt vmcnt(13)\n"
      "ds_write_b128 v89, a[96:99] offset:0\n"
      "ds_write_b128 v89, a[100:103] offset:1536\n"
      "ds_write_b128 v89, a[104:107] offset:3072\n"
      "ds_write_b128 v89, a[108:111] offset:4608\n"
      "s_mov_b32 s28, 0x118000\n"
      "buffer_load_dwordx4 a[160:163], v88, s[68:71], s28 offen\n"
      "s_mov_b32 s28, 0x11b800\n"
      "buffer_load_dwordx4 a[164:167], v88, s[68:71], s28 offen\n"
      "s_mov_b32 s28, 0x118000\n"
      "buffer_load_dwordx4 a[168:171], v88, s[72:75], s28 offen\n"
      "s_mov_b32 s28, 0x11b800\n"
      "buffer_load_dwordx4 a[172:175], v88, s[72:75], s28 offen\n"
      "ds_read_b64_tr_b16 a[96:97], v90 offset:0\n"
      "ds_read_b64_tr_b16 a[100:101], v90 offset:32\n"
      "ds_read_b64_tr_b16 a[98:99], v90 offset:1536\n"
      "ds_read_b64_tr_b16 a[102:103], v90 offset:1568\n"
      "ds_read_b64_tr_b16 a[104:105], v90 offset:3072\n"
      "ds_read_b64_tr_b16 a[108:109], v90 offset:3104\n"
      "ds_read_b64_tr_b16 a[106:107], v90 offset:4608\n"
      "ds_read_b64_tr_b16 a[110:111], v90 offset:4640\n"
      "s_waitcnt lgkmcnt(2)\n"
      "s_waitcnt vmcnt(13)\n"
      "ds_write_b128 v89, a[112:115] offset:0\n"
      "ds_write_b128 v89, a[116:119] offset:1536\n"
      "ds_write_b128 v89, a[120:123] offset:3072\n"
      "ds_write_b128 v89, a[124:127] offset:4608\n"
      "s_mov_b32 s28, 0x134000\n"
      "buffer_load_dwordx4 a[176:179], v88, s[68:71], s28 offen\n"
      "s_mov_b32 s28, 0x137800\n"
      "buffer_load_dwordx4 a[180:183], v88, s[68:71], s28 offen\n"
      "s_mov_b32 s28, 0x134000\n"
      "buffer_load_dwordx4 a[184:187], v88, s[72:75], s28 offen\n"
      "s_mov_b32 s28, 0x137800\n"
      "buffer_load_dwordx4 a[188:191], v88, s[72:75], s28 offen\n"
      "ds_read_b64_tr_b16 a[112:113], v90 offset:0\n"
      "ds_read_b64_tr_b16 a[116:117], v90 offset:32\n"
      "ds_read_b64_tr_b16 a[114:115], v90 offset:1536\n"
      "ds_read_b64_tr_b16 a[118:119], v90 offset:1568\n"
      "ds_read_b64_tr_b16 a[120:121], v90 offset:3072\n"
      "ds_read_b64_tr_b16 a[124:125], v90 offset:3104\n"
      "ds_read_b64_tr_b16 a[122:123], v90 offset:4608\n"
      "ds_read_b64_tr_b16 a[126:127], v90 offset:4640\n"
      "s_waitcnt lgkmcnt(2)\n"
      "s_waitcnt vmcnt(13)\n"
      "ds_write_b128 v89, a[128:131] offset:0\n"
      "ds_write_b128 v89, a[132:135] offset:1536\n"
      "ds_write_b128 v89, a[136:139] offset:3072\n"
      "ds_write_b128 v89, a[140:143] offset:4608\n"
      "s_mov_b32 s28, 0x150000\n"
      "buffer_load_dwordx4 a[192:195], v88, s[68:71], s28 offen\n"
      "s_mov_b32 s28, 0x153800\n"
      "buffer_load_dwordx4 a[196:199], v88, s[68:71], s28 offen\n"
      "s_mov_b32 s28, 0x150000\n"
      "buffer_load_dwordx4 a[200:203], v88, s[72:75], s28 offen\n"
      "s_mov_b32 s28, 0x153800\n"
      "buffer_load_dwordx4 a[204:207], v88, s[72:75], s28 offen\n"
      "ds_read_b64_tr_b16 a[128:129], v90 offset:0\n"
      "ds_read_b64_tr_b16 a[132:133], v90 offset:32\n"
      "ds_read_b64_tr_b16 a[130:131], v90 offset:1536\n"
      "ds_read_b64_tr_b16 a[134:135], v90 offset:1568\n"
      "ds_read_b64_tr_b16 a[136:137], v90 offset:3072\n"
      "ds_read_b64_tr_b16 a[140:141], v90 offset:3104\n"
      "ds_read_b64_tr_b16 a[138:139], v90 offset:4608\n"
      "ds_read_b64_tr_b16 a[142:143], v90 offset:4640\n"
      "s_waitcnt lgkmcnt(2)\n"
      "s_waitcnt vmcnt(12)\n"
      "ds_write_b128 v89, a[144:147] offset:0\n"
      "ds_write_b128 v89, a[148:151] offset:1536\n"
      "ds_write_b128 v89, a[152:155] offset:3072\n"
      "ds_write_b128 v89, a[156:159] offset:4608\n"
      "s_mov_b32 s28, 0x16c000\n"
      "buffer_load_dwordx4 a[208:211], v88, s[68:71], s28 offen\n"
      "s_mov_b32 s28, 0x16f800\n"
      "buffer_load_dwordx4 a[212:215], v88, s[68:71], s28 offen\n"
      "s_mov_b32 s28, 0x16c000\n"
      "buffer_load_dwordx4 a[216:219], v88, s[72:75], s28 offen\n"
      "s_mov_b32 s28, 0x16f800\n"
      "buffer_load_dwordx4 a[220:223], v88, s[72:75], s28 offen\n"
      "s_branch LP1_lnc\n"
      "LP1_ln:\n"
      "buffer_load_dwordx2 v[0:1], v100, s[8:11], 0 offen offset:0 sc1\n"
      "s_sub_u32 s62, s62, 1\n"
      "s_cbranch_scc1 LP1_lng\n"
      "s_waitcnt vmcnt(0)\n"
      "LP1_lnc:\n"
      "v_cmp_ne_u32_e64 s[54:55], s85, v1\n"
      "v_cmp_ge_i32 vcc, -1, v0\n"
      "s_or_b64 vcc, vcc, s[54:55]\n"
      "s_cbranch_vccz LP1_lnk\n"
      "s_sleep 3\n"
      "s_branch LP1_ln\n"
      "LP1_lng:\n"
      "s_mov_b32 s62, 0\n"
      "s_waitcnt vmcnt(0)\n"
      "LP1_lnk:\n"
      "s_mov_b32 m0, s31\n"
      "s_add_u32 s29, s12, 4096\n"
      "buffer_load_dwordx4 v91, s[8:11], s12 offen sc1 lds\n"
      "buffer_load_dwordx4 v91, s[8:11], s12 offen offset:1024 sc1 lds\n"
      "buffer_load_dwordx4 v91, s[8:11], s12 offen offset:2048 sc1 lds\n"
      "buffer_load_dwordx4 v91, s[8:11], s12 offen offset:3072 sc1 lds\n"
      "s_add_u32 m0, s31, 4096\n"
      "s_nop 0\n"
      "buffer_load_dwordx4 v91, s[8:11], s29 offen sc1 lds\n"
      "buffer_load_dwordx4 v91, s[8:11], s29 offen offset:1024 sc1 lds\n"
      "buffer_load_dwordx4 v91, s[8:11], s29 offen offset:2048 sc1 lds\n"
      "buffer_load_dwordx4 v91, s[8:11], s29 offen offset:3072 sc1 lds\n"
      "ds_read_b64_tr_b16 a[144:145], v90 offset:0\n"
      "ds_read_b64_tr_b16 a[148:149], v90 offset:32\n"
      "ds_read_b64_tr_b16 a[146:147], v90 offset:1536\n"
      "ds_read_b64_tr_b16 a[150:151], v90 offset:1568\n"
      "ds_read_b64_tr_b16 a[152:153], v90 offset:3072\n"
      "ds_read_b64_tr_b16 a[156:157], v90 offset:3104\n"
      "ds_read_b64_tr_b16 a[154:155], v90 offset:4608\n"
      "ds_read_b64_tr_b16 a[158:159], v90 offset:4640\n"
      "s_waitcnt lgkmcnt(2)\n"
      "s_waitcnt vmcnt(20)\n"
      "ds_write_b128 v89, a[160:163] offset:0\n"
      "ds_write_b128 v89, a[164:167] offset:1536\n"
      "ds_write_b128 v89, a[168:171] offset:3072\n"
      "ds_write_b128 v89, a[172:175] offset:4608\n"
      "s_mov_b32 s28, 0x188000\n"
      "buffer_load_dwordx4 a[224:227], v88, s[68:71], s28 offen\n"
      "s_mov_b32 s28, 0x18b800\n"
      "buffer_load_dwordx4 a[228:231], v88, s[68:71], s28 offen\n"
      "s_mov_b32 s28, 0x188000\n"
      "buffer_load_dwordx4 a[232:235], v88, s[72:75], s28 offen\n"
      "s_mov_b32 s28, 0x18b800\n"
      "buffer_load_dwordx4 a[236:239], v88, s[72:75], s28 offen\n"
      "ds_read_b64_tr_b16 a[160:161], v90 offset:0\n"
      "ds_read_b64_tr_b16 a[164:165], v90 offset:32\n"
      "ds_read_b64_tr_b16 a[162:163], v90 offset:1536\n"
      "ds_read_b64_tr_b16 a[166:167], v90 offset:1568\n"
      "ds_read_b64_tr_b16 a[168:169], v90 offset:3072\n"
      "ds_read_b64_tr_b16 a[172:173], v90 offset:3104\n"
      "ds_read_b64_tr_b16 a[170:171], v90 offset:4608\n"
      "ds_read_b64_tr_b16 a[174:175], v90 offset:4640\n"
      "s_waitcnt lgkmcnt(2)\n"
      "s_waitcnt vmcnt(20)\n"
      "ds_write_b128 v89, a[176:179] offset:0\n"
      "ds_write_b128 v89, a[180:183] offset:1536\n"
      "ds_write_b128 v89, a[184:187] offset:3072\n"
      "ds_write_b128 v89, a[188:191] offset:4608\n"
      "s_mov_b32 s28, 0x1a4000\n"
      "buffer_load_dwordx4 a[240:243], v88, s[68:71], s28 offen\n"
      "s_mov_b32 s28, 0x1a7800\n"
      "buffer_load_dwordx4 a[244:247], v88, s[68:71], s28 offen\n"
      "s_mov_b32 s28, 0x1a4000\n"
      "buffer_load_dwordx4 a[248:251], v88, s[72:75], s28 offen\n"
      "s_mov_b32 s28, 0x1a7800\n"
      "buffer_load_dwordx4 a[252:255], v88, s[72:75], s28 offen\n"
      "s_add_u32 s12, s12, 0x8000\n"
      "s_add_u32 s31, s31, 0x8000\n"
      "s_mov_b32 m0, s31\n"
      "s_add_u32 s29, s12, 4096\n"
      "buffer_load_dwordx4 v91, s[8:11], s12 offen sc1 lds\n"
      "buffer_load_dwordx4 v91, s[8:11], s12 offen offset:1024 sc1 lds\n"
      "buffer_load_dwordx4 v91, s[8:11], s12 offen offset:2048 sc1 lds\n"
      "buffer_load_dwordx4 v91, s[8:11], s12 offen offset:3072 sc1 lds\n"
      "s_add_u32 m0, s31, 4096\n"
      "s_nop 0\n"
      "buffer_load_dwordx4 v91, s[8:11], s29 offen sc1 lds\n"
      "buffer_load_dwordx4 v91, s[8:11], s29 offen offset:1024 sc1 lds\n"
      "buffer_load_dwordx4 v91, s[8:11], s29 offen offset:2048 sc1 lds\n"
      "buffer_load_dwordx4 v91, s[8:11], s29 offen offset:3072 sc1 lds\n"
      "ds_read_b64_tr_b16 a[176:177], v90 offset:0\n"
      "ds_read_b64_tr_b16 a[180:181], v90 offset:32\n"
      "ds_read_b64_tr_b16 a[178:179], v90 offset:1536\n"
      "ds_read_b64_tr_b16 a[182:183], v90 offset:1568\n"
      "ds_read_b64_tr_b16 a[184:185], v90 offset:3072\n"
      "ds_read_b64_tr_b16 a[188:189], v90 offset:3104\n"
      "ds_read_b64_tr_b16 a[186:187], v90 offset:4608\n"
      "ds_read_b64_tr_b16 a[190:191], v90 offset:4640\n"
      "s_waitcnt lgkmcnt(2)\n"
      "s_waitcnt vmcnt(28)\n"
      "ds_write_b128 v89, a[192:195] offset:0\n"
      "ds_write_b128 v89, a[196:199] offset:1536\n"
      "ds_write_b128 v89, a[200:203] offset:3072\n"
      "ds_write_b128 v89, a[204:207] offset:4608\n"
      "ds_read_b64_tr_b16 a[192:193], v90 offset:0\n"
      "ds_read_b64_tr_b16 a[196:197], v90 offset:32\n"
      "ds_read_b64_tr_b16 a[194:195], v90 offset:1536\n"
      "ds_read_b64_tr_b16 a[198:199], v90 offset:1568\n"
      "ds_read_b64_tr_b16 a[200:201], v90 offset:3072\n"
      "ds_read_b64_tr_b16 a[204:205], v90 offset:3104\n"
      "ds_read_b64_tr_b16 a[202:203], v90 offset:4608\n"
      "ds_read_b64_tr_b16 a[206:207], v90 offset:4640\n"
      "s_waitcnt lgkmcnt(2)\n"
      "s_waitcnt vmcnt(24)\n"
      "ds_write_b128 v89, a[208:211] offset:0\n"
      "ds_write_b128 v89, a[212:215] offset:1536\n"
      "ds_write_b128 v89, a[216:219] offset:3072\n"
      "ds_write_b128 v89, a[220:223] offset:4608\n"
      "ds_read_b64_tr_b16 a[208:209], v90 offset:0\n"
      "ds_read_b64_tr_b16 a[212:213], v90 offset:32\n"
      "ds_read_b64_tr_b16 a[210:211], v90 offset:1536\n"
      "ds_read_b64_tr_b16 a[214:215], v90 offset:1568\n"
      "ds_read_b64_tr_b16 a[216:217], v90 offset:3072\n"
      "ds_read_b64_tr_b16 a[220:221], v90 offset:3104\n"
      "ds_read_b64_tr_b16 a[218:219], v90 offset:4608\n"
      "ds_read_b64_tr_b16 a[222:223], v90 offset:4640\n"
      "s_waitcnt lgkmcnt(2)\n"
      "s_waitcnt vmcnt(12)\n"
      "ds_write_b128 v89, a[224:227] offset:0\n"
      "ds_write_b128 v89, a[228:231] offset:1536\n"
      "ds_write_b128 v89, a[232:235] offset:3072\n"
      "ds_write_b128 v89, a[236:239] offset:4608\n"
      "ds_read_b64_tr_b16 a[224:225], v90 offset:0\n"
      "ds_read_b64_tr_b16 a[228:229], v90 offset:32\n"
      "ds_read_b64_tr_b16 a[226:227], v90 offset:1536\n"
      "ds_read_b64_tr_b16 a[230:231], v90 offset:1568\n"
      "ds_read_b64_tr_b16 a[232:233], v90 offset:3072\n"
      "ds_read_b64_tr_b16 a[236:237], v90 offset:3104\n"
      "ds_read_b64_tr_b16 a[234:235], v90 offset:4608\n"
      "ds_read_b64_tr_b16 a[238:239], v90 offset:4640\n"
      "s_waitcnt lgkmcnt(2)\n"
      "s_waitcnt vmcnt(8)\n"
      "ds_write_b128 v89, a[240:243] offset:0\n"
      "ds_write_b128 v89, a[244:247] offset:1536\n"
      "ds_write_b128 v89, a[248:251] offset:3072\n"
      "ds_write_b128 v89, a[252:255] offset:4608\n"
      "ds_read_b64_tr_b16 a[240:241], v90 offset:0\n"
      "ds_read_b64_tr_b16 a[244:245], v90 offset:32\n"
      "ds_read_b64_tr_b16 a[242:243], v90 offset:1536\n"
      "ds_read_b64_tr_b16 a[246:247], v90 offset:1568\n"
      "ds_read_b64_tr_b16 a[248:249], v90 offset:3072\n"
      "ds_read_b64_tr_b16 a[252:253], v90 offset:3104\n"
      "ds_read_b64_tr_b16 a[250:251], v90 offset:4608\n"
      "ds_read_b64_tr_b16 a[254:255], v90 offset:4640\n"
      "s_waitcnt lgkmcnt(2)\n"
      "s_mov_b32 s38, 0\n"
      "s_mov_b32 s39, 0\n"
      "s_mov_b32 s40, 0xc0135761\n"
      "s_mov_b32 s41, 0xbdd2d3e8\n"
      "v_mov_b32 v94, v91\n"
      "v_add_u32 v95, 0x8000, v91\n"
      "v_mov_b32 v102, -1\n"
      "v_mov_b32 v103, s85\n"
      "v_mov_b32 v87, s40\n"
      "s_waitcnt lgkmcnt(0)\n"
      "s_barrier\n"
      "ds_read_b128 v[32:35], v94 offset:0\n"
      "ds_read_b128 v[36:39], v94 offset:1024\n"
      "ds_read_b128 v[40:43], v94 offset:2048\n"
      "s_sub_u32 s30, s13, 113664\n"
      ".p2align 6\n"
      "LP1_loop:\n"
      "s_waitcnt lgkmcnt(2)\n"
      "v_mfma_f32_16x16x32_bf16 v[0:3], a[0:3], v[32:35], 0\n"
      "v_mfma_f32_16x16x32_bf16 v[4:7], a[4:7], v[32:35], 0\n"
      "ds_read_b128 v[44:47], v94 offset:3072\n"
      "v_mfma_f32_16x16x32_bf16 v[8:11], a[8:11], v[32:35], 0\n"
      "v_mfma_f32_16x16x32_bf16 v[12:15], a[12:15], v[32:35], 0\n"
      "s_waitcnt lgkmcnt(2)\n"
      "v_mfma_f32_16x16x32_bf16 v[0:3], a[16:19], v[36:39], v[0:3]\n"
      "v_mfma_f32_16x16x32_bf16 v[4:7], a[20:23], v[36:39], v[4:7]\n"
      "ds_read_b128 v[32:35], v94 offset:4096\n"
      "v_mfma_f32_16x16x32_bf16 v[8:11], a[24:27], v[36:39], v[8:11]\n"
      "v_mfma_f32_16x16x32_bf16 v[12:15], a[28:31], v[36:39], v[12:15]\n"
      "v_mul_f32 v64, v16, v16\n"
      "v_mul_f32 v65, v17, v17\n"
      "s_waitcnt lgkmcnt(2)\n"
      "v_mfma_f32_16x16x32_bf16 v[0:3], a[32:35], v[40:43], v[0:3]\n"
      "v_mul_f32 v66, v18, v18\n"
      "v_mul_f32 v67, v19, v19\n"
      "v_mfma_f32_16x16x32_bf16 v[4:7], a[36:39], v[40:43], v[4:7]\n"
      "ds_read_b128 v[36:39], v94 offset:5120\n"
      "v_fma_f32 v64, v64, s41, v87\n"
      "v_mfma_f32_16x16x32_bf16 v[8:11], a[40:43], v[40:43], v[8:11]\n"
      "v_fma_f32 v65, v65, s41, v87\n"
      "v_fma_f32 v66, v66, s41, v87\n"
      "v_mfma_f32_16x16x32_bf16 v[12:15], a[44:47], v[40:43], v[12:15]\n"
      "v_fma_f32 v67, v67, s41, v87\n"
      "v_mul_f32 v64, v64, v16\n"
      "s_waitcnt lgkmcnt(2)\n"
      "v_mfma_f32_16x16x32_bf16 v[0:3], a[48:51], v[44:47], v[0:3]\n"
      "v_mul_f32 v65, v65, v17\n"
      "v_mul_f32 v66, v66, v18\n"
      "v_mfma_f32_16x16x32_bf16 v[4:7], a[52:55], v[44:47], v[4:7]\n"
      "ds_read_b128 v[40:43], v94 offset:6144\n"
      "v_mul_f32 v67, v67, v19\n"
      "v_mfma_f32_16x16x32_bf16 v[8:11], a[56:59], v[44:47], v[8:11]\n"
      "v_exp_f32 v64, v64\n"
      "v_mfma_f32_16x16x32_bf16 v[12:15], a[60:63], v[44:47], v[12:15]\n"
      "v_mul_f32 v24, v16, v24\n"
      "v_exp_f32 v65, v65\n"
      "s_waitcnt lgkmcnt(2)\n"
      "v_mfma_f32_16x16x32_bf16 v[0:3], a[64:67], v[32:35], v[0:3]\n"
      "v_mul_f32 v25, v17, v25\n"
      "v_exp_f32 v66, v66\n"
      "v_mfma_f32_16x16x32_bf16 v[4:7], a[68:71], v[32:35], v[4:7]\n"
      "ds_read_b128 v[44:47], v94 offset:7168\n"
      "v_mul_f32 v26, v18, v26\n"
      "v_mfma_f32_16x16x32_bf16 v[8:11], a[72:75], v[32:35], v[8:11]\n"
      "v_exp_f32 v67, v67\n"
      "v_mfma_f32_16x16x32_bf16 v[12:15], a[76:79], v[32:35], v[12:15]\n"
      "v_mul_f32 v27, v19, v27\n"
      "v_add_f32 v64, 1.0, v64\n"
      "s_waitcnt lgkmcnt(2)\n"
      "v_mfma_f32_16x16x32_bf16 v[0:3], a[80:83], v[36:39], v[0:3]\n"
      "v_add_f32 v65, 1.0, v65\n"
      "v_add_f32 v66, 1.0, v66\n"
      "v_mfma_f32_16x16x32_bf16 v[4:7], a[84:87], v[36:39], v[4:7]\n"
      "ds_read_b128 v[32:35], v94 offset:8192\n"
      "v_add_f32 v67, 1.0, v67\n"
      "v_mfma_f32_16x16x32_bf16 v[8:11], a[88:91], v[36:39], v[8:11]\n"
      "v_rcp_f32 v64, v64\n"
      "v_mfma_f32_16x16x32_bf16 v[12:15], a[92:95], v[36:39], v[12:15]\n"
      "v_rcp_f32 v65, v65\n"
      "s_waitcnt lgkmcnt(2)\n"
      "v_mfma_f32_16x16x32_bf16 v[0:3], a[96:99], v[40:43], v[0:3]\n"
      "v_rcp_f32 v66, v66\n"
      "v_mfma_f32_16x16x32_bf16 v[4:7], a[100:103], v[40:43], v[4:7]\n"
      "ds_read_b128 v[36:39], v94 offset:9216\n"
      "v_rcp_f32 v67, v67\n"
      "v_mfma_f32_16x16x32_bf16 v[8:11], a[104:107], v[40:43], v[8:11]\n"
      "v_mul_f32 v24, v24, v64\n"
      "v_mul_f32 v25, v25, v65\n"
      "v_mfma_f32_16x16x32_bf16 v[12:15], a[108:111], v[40:43], v[12:15]\n"
      "v_mul_f32 v26, v26, v66\n"
      "v_mul_f32 v27, v27, v67\n"
      "s_waitcnt lgkmcnt(2)\n"
      "v_mfma_f32_16x16x32_bf16 v[0:3], a[112:115], v[44:47], v[0:3]\n"
      "v_cvt_pk_bf16_f32 v52, v24, v25\n"
      "v_cvt_pk_bf16_f32 v53, v26, v27\n"
      "v_mfma_f32_16x16x32_bf16 v[4:7], a[116:119], v[44:47], v[4:7]\n"
      "ds_read_b128 v[40:43], v94 offset:10240\n"
      "v_mul_f32 v68, v20, v20\n"
      "v_mfma_f32_16x16x32_bf16 v[8:11], a[120:123], v[44:47], v[8:11]\n"
      "v_mul_f32 v69, v21, v21\n"
      "v_mul_f32 v70, v22, v22\n"
      "v_mfma_f32_16x16x32_bf16 v[12:15], a[124:127], v[44:47], v[12:15]\n"
      "v_mul_f32 v71, v23, v23\n"
      "v_fma_f32 v68, v68, s41, v87\n"
      "s_waitcnt lgkmcnt(2)\n"
      "v_mfma_f32_16x16x32_bf16 v[0:3], a[128:131], v[32:35], v[0:3]\n"
      "v_fma_f32 v69, v69, s41, v87\n"
      "v_fma_f32 v70, v70, s41, v87\n"
      "v_mfma_f32_16x16x32_bf16 v[4:7], a[132:135], v[32:35], v[4:7]\n"
      "ds_read_b128 v[44:47], v94 offset:11264\n"
      "v_fma_f32 v71, v71, s41, v87\n"
      "v_mfma_f32_16x16x32_bf16 v[8:11], a[136:139], v[32:35], v[8:11]\n"
      "v_mul_f32 v68, v68, v20\n"
      "v_mul_f32 v69, v69, v21\n"
      "v_mfma_f32_16x16x32_bf16 v[12:15], a[140:143], v[32:35], v[12:15]\n"
      "v_mul_f32 v70, v70, v22\n"
      "v_mul_f32 v71, v71, v23\n"
      "s_waitcnt lgkmcnt(2)\n"
      "v_mfma_f32_16x16x32_bf16 v[0:3], a[144:147], v[36:39], v[0:3]\n"
      "v_exp_f32 v68, v68\n"
      "v_mfma_f32_16x16x32_bf16 v[4:7], a[148:151], v[36:39], v[4:7]\n"
      "ds_read_b128 v[32:35], v94 offset:12288\n"
      "v_mul_f32 v28, v20, v28\n"
      "v_mfma_f32_16x16x32_bf16 v[8:11], a[152:155], v[36:39], v[8:11]\n"
      "v_exp_f32 v69, v69\n"
      "v_mfma_f32_16x16x32_bf16 v[12:15], a[156:159], v[36:39], v[12:15]\n"
      "v_mul_f32 v29, v21, v29\n"
      "v_exp_f32 v70, v70\n"
      "s_waitcnt lgkmcnt(2)\n"
      "v_mfma_f32_16x16x32_bf16 v[0:3], a[160:163], v[40:43], v[0:3]\n"
      "v_mul_f32 v30, v22, v30\n"
      "v_exp_f32 v71, v71\n"
      "v_mfma_f32_16x16x32_bf16 v[4:7], a[164:167], v[40:43], v[4:7]\n"
      "ds_read_b128 v[36:39], v94 offset:13312\n"
      "v_mul_f32 v31, v23, v31\n"
      "v_mfma_f32_16x16x32_bf16 v[8:11], a[168:171], v[40:43], v[8:11]\n"
      "v_add_f32 v68, 1.0, v68\n"
      "v_add_f32 v69, 1.0, v69\n"
      "v_mfma_f32_16x16x32_bf16 v[12:15], a[172:175], v[40:43], v[12:15]\n"
      "v_add_f32 v70, 1.0, v70\n"
      "v_add_f32 v71, 1.0, v71\n"
      "s_waitcnt lgkmcnt(2)\n"
      "v_mfma_f32_16x16x32_bf16 v[0:3], a[176:179], v[44:47], v[0:3]\n"
      "v_rcp_f32 v68, v68\n"
      "v_mfma_f32_16x16x32_bf16 v[4:7], a[180:183], v[44:47], v[4:7]\n"
      "ds_read_b128 v[40:43], v94 offset:14336\n"
      "v_rcp_f32 v69, v69\n"
      "v_mfma_f32_16x16x32_bf16 v[8:11], a[184:187], v[44:47], v[8:11]\n"
      "v_rcp_f32 v70, v70\n"
      "v_mfma_f32_16x16x32_bf16 v[12:15], a[188:191], v[44:47], v[12:15]\n"
      "v_rcp_f32 v71, v71\n"
      "s_waitcnt lgkmcnt(2)\n"
      "v_mfma_f32_16x16x32_bf16 v[0:3], a[192:195], v[32:35], v[0:3]\n"
      "v_mul_f32 v28, v28, v68\n"
      "v_mul_f32 v29, v29, v69\n"
      "v_mfma_f32_16x16x32_bf16 v[4:7], a[196:199], v[32:35], v[4:7]\n"
      "ds_read_b128 v[44:47], v94 offset:15360\n"
      "v_mul_f32 v30, v30, v70\n"
      "v_mfma_f32_16x16x32_bf16 v[8:11], a[200:203], v[32:35], v[8:11]\n"
      "v_mul_f32 v31, v31, v71\n"
      "v_cvt_pk_bf16_f32 v54, v28, v29\n"
      "v_mfma_f32_16x16x32_bf16 v[12:15], a[204:207], v[32:35], v[12:15]\n"
      "v_cvt_pk_bf16_f32 v55, v30, v31\n"
      "buffer_store_dwordx4 v[52:55], v93, s[8:11], s30 offen sc1\n"
      "s_waitcnt lgkmcnt(2)\n"
      "v_mfma_f32_16x16x32_bf16 v[0:3], a[208:211], v[36:39], v[0:3]\n"
      "v_mfma_f32_16x16x32_bf16 v[4:7], a[212:215], v[36:39], v[4:7]\n"
      "ds_read_b128 v[32:35], v94 offset:16384\n"
      "v_mfma_f32_16x16x32_bf16 v[8:11], a[216:219], v[36:39], v[8:11]\n"
      "v_mfma_f32_16x16x32_bf16 v[12:15], a[220:223], v[36:39], v[12:15]\n"
      "s_waitcnt lgkmcnt(2)\n"
      "v_mfma_f32_16x16x32_bf16 v[0:3], a[224:227], v[40:43], v[0:3]\n"
      "v_mfma_f32_16x16x32_bf16 v[4:7], a[228:231], v[40:43], v[4:7]\n"
      "ds_read_b128 v[36:39], v94 offset:17408\n"
      "v_mfma_f32_16x16x32_bf16 v[8:11], a[232:235], v[40:43], v[8:11]\n"
      "v_mfma_f32_16x16x32_bf16 v[12:15], a[236:239], v[40:43], v[12:15]\n"
      "s_waitcnt lgkmcnt(2)\n"
      "v_mfma_f32_16x16x32_bf16 v[0:3], a[240:243], v[44:47], v[0:3]\n"
      "v_mfma_f32_16x16x32_bf16 v[4:7], a[244:247], v[44:47], v[4:7]\n"
      "ds_read_b128 v[40:43], v94 offset:18432\n"
      "v_mfma_f32_16x16x32_bf16 v[8:11], a[248:251], v[44:47], v[8:11]\n"
      "v_mfma_f32_16x16x32_bf16 v[12:15], a[252:255], v[44:47], v[12:15]\n"
      "s_waitcnt lgkmcnt(2)\n"
      "v_mfma_f32_16x16x32_bf16 v[16:19], a[0:3], v[32:35], 0\n"
      "v_mfma_f32_16x16x32_bf16 v[20:23], a[4:7], v[32:35], 0\n"
      "ds_read_b128 v[44:47], v94 offset:19456\n"
      "s_waitcnt vmcnt(1)\n"
      "s_barrier\n"
      "v_mfma_f32_16x16x32_bf16 v[24:27], a[8:11], v[32:35], 0\n"
      "buffer_store_dwordx2 v[102:103], v92, s[8:11], s14 offen sc1\n"
      "v_add_u32 v102, 1, v102\n"
      "v_mfma_f32_16x16x32_bf16 v[28:31], a[12:15], v[32:35], 0\n"
      "s_add_u32 s12, s12, 0x8000\n"
      "s_add_u32 s31, s39, 0x10000\n"
      "s_cmp_ge_u32 s31, 0x18000\n"
      "s_waitcnt lgkmcnt(2)\n"
      "v_mfma_f32_16x16x32_bf16 v[16:19], a[16:19], v[36:39], v[16:19]\n"
      "s_cselect_b32 s28, 0x18000, 0\n"
      "s_sub_u32 s31, s31, s28\n"
      "v_mov_b32 v93, v91\n"
      "v_mfma_f32_16x16x32_bf16 v[20:23], a[20:23], v[36:39], v[20:23]\n"
      "ds_read_b128 v[32:35], v94 offset:20480\n"
      "s_add_u32 s28, s31, s15\n"
      "v_mfma_f32_16x16x32_bf16 v[24:27], a[24:27], v[36:39], v[24:27]\n"
      "s_mov_b32 m0, s28\n"
      "s_add_u32 s29, s12, 4096\n"
      "buffer_load_dwordx4 v91, s[8:11], s12 offen sc1 lds\n"
      "v_mfma_f32_16x16x32_bf16 v[28:31], a[28:31], v[36:39], v[28:31]\n"
      "v_mul_f32 v64, v0, v0\n"
      "v_mul_f32 v65, v1, v1\n"
      "s_waitcnt lgkmcnt(2)\n"
      "v_mfma_f32_16x16x32_bf16 v[16:19], a[32:35], v[40:43], v[16:19]\n"
      "v_mul_f32 v66, v2, v2\n"
      "v_mul_f32 v67, v3, v3\n"
      "v_mfma_f32_16x16x32_bf16 v[20:23], a[36:39], v[40:43], v[20:23]\n"
      "ds_read_b128 v[36:39], v94 offset:21504\n"
      "v_fma_f32 v64, v64, s41, v87\n"
      "v_mfma_f32_16x16x32_bf16 v[24:27], a[40:43], v[40:43], v[24:27]\n"
      "buffer_load_dwordx4 v91, s[8:11], s12 offen offset:1024 sc1 lds\n"
      "v_fma_f32 v65, v65, s41, v87\n"
      "v_fma_f32 v66, v66, s41, v87\n"
      "v_mfma_f32_16x16x32_bf16 v[28:31], a[44:47], v[40:43], v[28:31]\n"
      "v_fma_f32 v67, v67, s41, v87\n"
      "v_mul_f32 v64, v64, v0\n"
      "s_waitcnt lgkmcnt(2)\n"
      "v_mfma_f32_16x16x32_bf16 v[16:19], a[48:51], v[44:47], v[16:19]\n"
      "v_mul_f32 v65, v65, v1\n"
      "v_mul_f32 v66, v66, v2\n"
      "v_mfma_f32_16x16x32_bf16 v[20:23], a[52:55], v[44:47], v[20:23]\n"
      "ds_read_b128 v[40:43], v94 offset:22528\n"
      "v_mul_f32 v67, v67, v3\n"
      "v_mfma_f32_16x16x32_bf16 v[24:27], a[56:59], v[44:47], v[24:27]\n"
      "buffer_load_dwordx4 v91, s[8:11], s12 offen offset:2048 sc1 lds\n"
      "v_exp_f32 v64, v64\n"
      "v_mfma_f32_16x16x32_bf16 v[28:31], a[60:63], v[44:47], v[28:31]\n"
      "v_mul_f32 v8, v0, v8\n"
      "v_exp_f32 v65, v65\n"
      "s_waitcnt lgkmcnt(2)\n"
      "v_mfma_f32_16x16x32_bf16 v[16:19], a[64:67], v[32:35], v[16:19]\n"
      "v_mul_f32 v9, v1, v9\n"
      "v_exp_f32 v66, v66\n"
      "v_mfma_f32_16x16x32_bf16 v[20:23], a[68:71], v[32:35], v[20:23]\n"
      "ds_read_b128 v[44:47], v94 offset:23552\n"
      "v_mul_f32 v10, v2, v10\n"
      "v_mfma_f32_16x16x32_bf16 v[24:27], a[72:75], v[32:35], v[24:27]\n"
      "buffer_load_dwordx4 v91, s[8:11], s12 offen offset:3072 sc1 lds\n"
      "v_exp_f32 v67, v67\n"
      "v_mfma_f32_16x16x32_bf16 v[28:31], a[76:79], v[32:35], v[28:31]\n"
      "v_mul_f32 v11, v3, v11\n"
      "v_add_f32 v64, 1.0, v64\n"
      "s_waitcnt lgkmcnt(2)\n"
      "v_mfma_f32_16x16x32_bf16 v[16:19], a[80:83], v[36:39], v[16:19]\n"
      "v_add_f32 v65, 1.0, v65\n"
      "v_add_f32 v66, 1.0, v66\n"
      "v_mfma_f32_16x16x32_bf16 v[20:23], a[84:87], v[36:39], v[20:23]\n"
      "ds_read_b128 v[32:35], v94 offset:24576\n"
      "v_add_f32 v67, 1.0, v67\n"
      "v_mfma_f32_16x16x32_bf16 v[24:27], a[88:91], v[36:39], v[24:27]\n"
      "s_add_u32 m0, s28, 4096\n"
      "s_nop 0\n"
      "buffer_load_dwordx4 v91, s[8:11], s29 offen sc1 lds\n"
      "v_rcp_f32 v64, v64\n"
      "v_mfma_f32_16x16x32_bf16 v[28:31], a[92:95], v[36:39], v[28:31]\n"
      "v_rcp_f32 v65, v65\n"
      "s_waitcnt lgkmcnt(2)\n"
      "v_mfma_f32_16x16x32_bf16 v[16:19], a[96:99], v[40:43], v[16:19]\n"
      "v_rcp_f32 v66, v66\n"
      "v_mfma_f32_16x16x32_bf16 v[20:23], a[100:103], v[40:43], v[20:23]\n"
      "ds_read_b128 v[36:39], v94 offset:25600\n"
      "v_rcp_f32 v67, v67\n"
      "v_mfma_f32_16x16x32_bf16 v[24:27], a[104:107], v[40:43], v[24:27]\n"
      "buffer_load_dwordx4 v91, s[8:11], s29 offen offset:1024 sc1 lds\n"
      "v_mul_f32 v8, v8, v64\n"
      "v_mul_f32 v9, v9, v65\n"
      "v_mfma_f32_16x16x32_bf16 v[28:31], a[108:111], v[40:43], v[28:31]\n"
      "v_mul_f32 v10, v10, v66\n"
      "v_mul_f32 v11, v11, v67\n"
      "s_waitcnt lgkmcnt(2)\n"
      "v_mfma_f32_16x16x32_bf16 v[16:19], a[112:115], v[44:47], v[16:19]\n"
      "v_cvt_pk_bf16_f32 v48, v8, v9\n"
      "v_cvt_pk_bf16_f32 v49, v10, v11\n"
      "v_mfma_f32_16x16x32_bf16 v[20:23], a[116:119], v[44:47], v[20:23]\n"
      "ds_read_b128 v[40:43], v94 offset:26624\n"
      "v_mul_f32 v68, v4, v4\n"
      "v_mfma_f32_16x16x32_bf16 v[24:27], a[120:123], v[44:47], v[24:27]\n"
      "buffer_load_dwordx4 v91, s[8:11], s29 offen offset:2048 sc1 lds\n"
      "v_mul_f32 v69, v5, v5\n"
      "v_mul_f32 v70, v6, v6\n"
      "v_mfma_f32_16x16x32_bf16 v[28:31], a[124:127], v[44:47], v[28:31]\n"
      "v_mul_f32 v71, v7, v7\n"
      "v_fma_f32 v68, v68, s41, v87\n"
      "s_waitcnt lgkmcnt(2)\n"
      "v_mfma_f32_16x16x32_bf16 v[16:19], a[128:131], v[32:35], v[16:19]\n"
      "v_fma_f32 v69, v69, s41, v87\n"
      "v_fma_f32 v70, v70, s41, v87\n"
      "v_mfma_f32_16x16x32_bf16 v[20:23], a[132:135], v[32:35], v[20:23]\n"
      "ds_read_b128 v[44:47], v94 offset:27648\n"
      "v_fma_f32 v71, v71, s41, v87\n"
      "v_mfma_f32_16x16x32_bf16 v[24:27], a[136:139], v[32:35], v[24:27]\n"
      "buffer_load_dwordx4 v91, s[8:11], s29 offen offset:3072 sc1 lds\n"
      "v_mul_f32 v68, v68, v4\n"
      "v_mul_f32 v69, v69, v5\n"
      "v_mfma_f32_16x16x32_bf16 v[28:31], a[140:143], v[32:35], v[28:31]\n"
      "v_mul_f32 v70, v70, v6\n"
      "v_mul_f32 v71, v71, v7\n"
      "s_waitcnt lgkmcnt(2)\n"
      "v_mfma_f32_16x16x32_bf16 v[16:19], a[144:147], v[36:39], v[16:19]\n"
      "v_exp_f32 v68, v68\n"
      "v_mfma_f32_16x16x32_bf16 v[20:23], a[148:151], v[36:39], v[20:23]\n"
      "ds_read_b128 v[32:35], v94 offset:28672\n"
      "v_mul_f32 v12, v4, v12\n"
      "v_mfma_f32_16x16x32_bf16 v[24:27], a[152:155], v[36:39], v[24:27]\n"
      "v_exp_f32 v69, v69\n"
      "v_mfma_f32_16x16x32_bf16 v[28:31], a[156:159], v[36:39], v[28:31]\n"
      "v_mul_f32 v13, v5, v13\n"
      "v_exp_f32 v70, v70\n"
      "s_waitcnt lgkmcnt(2)\n"
      "v_mfma_f32_16x16x32_bf16 v[16:19], a[160:163], v[40:43], v[16:19]\n"
      "v_mul_f32 v14, v6, v14\n"
      "v_exp_f32 v71, v71\n"
      "v_mfma_f32_16x16x32_bf16 v[20:23], a[164:167], v[40:43], v[20:23]\n"
      "ds_read_b128 v[36:39], v94 offset:29696\n"
      "v_mul_f32 v15, v7, v15\n"
      "v_mfma_f32_16x16x32_bf16 v[24:27], a[168:171], v[40:43], v[24:27]\n"
      "v_add_f32 v68, 1.0, v68\n"
      "v_add_f32 v69, 1.0, v69\n"
      "v_mfma_f32_16x16x32_bf16 v[28:31], a[172:175], v[40:43], v[28:31]\n"
      "v_add_f32 v70, 1.0, v70\n"
      "v_add_f32 v71, 1.0, v71\n"
      "s_waitcnt lgkmcnt(2)\n"
      "v_mfma_f32_16x16x32_bf16 v[16:19], a[176:179], v[44:47], v[16:19]\n"
      "v_rcp_f32 v68, v68\n"
      "s_add_u32 s39, s39, 0x8000\n"
      "s_cmp_eq_u32 s39, 0x18000\n"
      "s_cselect_b32 s39, 0, s39\n"
      "v_mfma_f32_16x16x32_bf16 v[20:23], a[180:183], v[44:47], v[20:23]\n"
      "ds_read_b128 v[40:43], v94 offset:30720\n"
      "v_rcp_f32 v69, v69\n"
      "v_mfma_f32_16x16x32_bf16 v[24:27], a[184:187], v[44:47], v[24:27]\n"
      "v_rcp_f32 v70, v70\n"
      "v_mfma_f32_16x16x32_bf16 v[28:31], a[188:191], v[44:47], v[28:31]\n"
      "v_rcp_f32 v71, v71\n"
      "s_cmp_eq_u32 s38, 31\n"
      "s_cselect_b32 s28, s16, 0\n"
      "s_add_u32 s28, s28, 114688\n"
      "s_waitcnt lgkmcnt(2)\n"
      "v_mfma_f32_16x16x32_bf16 v[16:19], a[192:195], v[32:35], v[16:19]\n"
      "v_mul_f32 v12, v12, v68\n"
      "v_mul_f32 v13, v13, v69\n"
      "v_mfma_f32_16x16x32_bf16 v[20:23], a[196:199], v[32:35], v[20:23]\n"
      "ds_read_b128 v[44:47], v94 offset:31744\n"
      "v_mul_f32 v14, v14, v70\n"
      "v_mfma_f32_16x16x32_bf16 v[24:27], a[200:203], v[32:35], v[24:27]\n"
      "v_mul_f32 v15, v15, v71\n"
      "v_cvt_pk_bf16_f32 v50, v12, v13\n"
      "v_mfma_f32_16x16x32_bf16 v[28:31], a[204:207], v[32:35], v[28:31]\n"
      "v_cvt_pk_bf16_f32 v51, v14, v15\n"
      "buffer_store_dwordx4 v[48:51], v91, s[8:11], s13 offen sc1\n"
      "s_add_u32 s30, s13, 1024\n"
      "v_mov_b32 v94, v95\n"
      "s_waitcnt lgkmcnt(2)\n"
      "v_mfma_f32_16x16x32_bf16 v[16:19], a[208:211], v[36:39], v[16:19]\n"
      "v_mfma_f32_16x16x32_bf16 v[20:23], a[212:215], v[36:39], v[20:23]\n"
      "ds_read_b128 v[32:35], v95 offset:0\n"
      "v_mfma_f32_16x16x32_bf16 v[24:27], a[216:219], v[36:39], v[24:27]\n"
      "v_mfma_f32_16x16x32_bf16 v[28:31], a[220:223], v[36:39], v[28:31]\n"
      "s_waitcnt lgkmcnt(2)\n"
      "v_mfma_f32_16x16x32_bf16 v[16:19], a[224:227], v[40:43], v[16:19]\n"
      "v_mfma_f32_16x16x32_bf16 v[20:23], a[228:231], v[40:43], v[20:23]\n"
      "ds_read_b128 v[36:39], v95 offset:1024\n"
      "v_mfma_f32_16x16x32_bf16 v[24:27], a[232:235], v[40:43], v[24:27]\n"
      "v_mfma_f32_16x16x32_bf16 v[28:31], a[236:239], v[40:43], v[28:31]\n"
      "s_waitcnt lgkmcnt(2)\n"
      "v_mfma_f32_16x16x32_bf16 v[16:19], a[240:243], v[44:47], v[16:19]\n"
      "v_mfma_f32_16x16x32_bf16 v[20:23], a[244:247], v[44:47], v[20:23]\n"
      "ds_read_b128 v[40:43], v95 offset:2048\n"
      "v_mfma_f32_16x16x32_bf16 v[24:27], a[248:251], v[44:47], v[24:27]\n"
      "v_add_u32 v95, s31, v91\n"
      "s_add_u32 s38, s38, 1\n"
      "v_mfma_f32_16x16x32_bf16 v[28:31], a[252:255], v[44:47], v[28:31]\n"
      "s_add_u32 s13, s13, s28\n"
      "s_cmp_lt_u32 s38, 64\n"
      "s_cbranch_scc1 LP1_loop\n"
      "v_mul_f32 v64, v16, v16\n"
      "v_mul_f32 v65, v17, v17\n"
      "v_mul_f32 v66, v18, v18\n"
      "v_mul_f32 v67, v19, v19\n"
      "v_fma_f32 v64, v64, s41, v87\n"
      "v_fma_f32 v65, v65, s41, v87\n"
      "v_fma_f32 v66, v66, s41, v87\n"
      "v_fma_f32 v67, v67, s41, v87\n"
      "v_mul_f32 v64, v64, v16\n"
      "v_mul_f32 v65, v65, v17\n"
      "v_mul_f32 v66, v66, v18\n"
      "v_mul_f32 v67, v67, v19\n"
      "v_exp_f32 v64, v64\n"
      "v_mul_f32 v24, v16, v24\n"
      "v_exp_f32 v65, v65\n"
      "v_mul_f32 v25, v17, v25\n"
      "v_exp_f32 v66, v66\n"
      "v_mul_f32 v26, v18, v26\n"
      "v_exp_f32 v67, v67\n"
      "v_mul_f32 v27, v19, v27\n"
      "v_add_f32 v64, 1.0, v64\n"
      "v_add_f32 v65, 1.0, v65\n"
      "v_add_f32 v66, 1.0, v66\n"
      "v_add_f32 v67, 1.0, v67\n"
      "v_rcp_f32 v64, v64\n"
      "v_rcp_f32 v65, v65\n"
      "v_rcp_f32 v66, v66\n"
      "v_rcp_f32 v67, v67\n"
      "v_mul_f32 v24, v24, v64\n"
      "v_mul_f32 v25, v25, v65\n"
      "v_mul_f32 v26, v26, v66\n"
      "v_mul_f32 v27, v27, v67\n"
      "v_cvt_pk_bf16_f32 v52, v24, v25\n"
      "v_cvt_pk_bf16_f32 v53, v26, v27\n"
      "v_mul_f32 v68, v20, v20\n"
      "v_mul_f32 v69, v21, v21\n"
      "v_mul_f32 v70, v22, v22\n"
      "v_mul_f32 v71, v23, v23\n"
      "v_fma_f32 v68, v68, s41, v87\n"
      "v_fma_f32 v69, v69, s41, v87\n"
      "v_fma_f32 v70, v70, s41, v87\n"
      "v_fma_f32 v71, v71, s41, v87\n"
      "v_mul_f32 v68, v68, v20\n"
      "v_mul_f32 v69, v69, v21\n"
      "v_mul_f32 v70, v70, v22\n"
      "v_mul_f32 v71, v71, v23\n"
      "v_exp_f32 v68, v68\n"
      "v_mul_f32 v28, v20, v28\n"
      "v_exp_f32 v69, v69\n"
      "v_mul_f32 v29, v21, v29\n"
      "v_exp_f32 v70, v70\n"
      "v_mul_f32 v30, v22, v30\n"
      "v_exp_f32 v71, v71\n"
      "v_mul_f32 v31, v23, v31\n"
      "v_add_f32 v68, 1.0, v68\n"
      "v_add_f32 v69, 1.0, v69\n"
      "v_add_f32 v70, 1.0, v70\n"
      "v_add_f32 v71, 1.0, v71\n"
      "v_rcp_f32 v68, v68\n"
      "v_rcp_f32 v69, v69\n"
      "v_rcp_f32 v70, v70\n"
      "v_rcp_f32 v71, v71\n"
      "v_mul_f32 v28, v28, v68\n"
      "v_mul_f32 v29, v29, v69\n"
      "v_mul_f32 v30, v30, v70\n"
      "v_mul_f32 v31, v31, v71\n"
      "v_cvt_pk_bf16_f32 v54, v28, v29\n"
      "v_cvt_pk_bf16_f32 v55, v30, v31\n"
      "buffer_store_dwordx4 v[52:55], v91, s[8:11], s30 offen sc1\n"
      "s_waitcnt vmcnt(0) lgkmcnt(0)\n"
      "s_barrier\n"
      "v_mov_b32 v102, 0x64\n"
      "buffer_store_dwordx2 v[102:103], v92, s[8:11], s14 offen sc1\n"
      "s_branch Lend\n"
      "LO_init:\n"
      "v_mov_b32 v240, v88\n"
      "v_mov_b32 v241, v89\n"
      "v_mov_b32 v242, v90\n"
      "v_mov_b32 v243, v91\n"
      "v_mov_b32 v244, v92\n"
      "v_mov_b32 v245, v93\n"
      "v_mov_b32 v246, v94\n"
      "v_mov_b32 v247, v95\n"
      "v_mov_b32 v248, v96\n"
      "v_mov_b32 v249, v97\n"
      "v_mov_b32 v250, v98\n"
      "v_mov_b32 v251, v99\n"
      "s_sleep 127\n"
      "s_mov_b32 s28, 0x0\n"
      "buffer_load_dwordx4 a[0:3], v240, s[68:71], s28 offen\n"
      "s_mov_b32 s28, 0x2000\n"
      "buffer_load_dwordx4 a[4:7], v240, s[68:71], s28 offen\n"
      "s_mov_b32 s28, 0x4000\n"
      "buffer_load_dwordx4 a[8:11], v240, s[68:71], s28 offen\n"
      "s_mov_b32 s28, 0x6000\n"
      "buffer_load_dwordx4 a[12:15], v240, s[68:71], s28 offen\n"
      "s_mov_b32 s28, 0x8000\n"
      "buffer_load_dwordx4 a[16:19], v240, s[68:71], s28 offen\n"
      "s_mov_b32 s28, 0xa000\n"
      "buffer_load_dwordx4 a[20:23], v240, s[68:71], s28 offen\n"
      "s_mov_b32 s28, 0xc000\n"
      "buffer_load_dwordx4 a[24:27], v240, s[68:71], s28 offen\n"
      "s_mov_b32 s28, 0xe000\n"
      "buffer_load_dwordx4 a[28:31], v240, s[68:71], s28 offen\n"
      "s_mov_b32 s28, 0x10000\n"
      "buffer_load_dwordx4 a[32:35], v240, s[68:71], s28 offen\n"
      "s_mov_b32 s28, 0x12000\n"
      "buffer_load_dwordx4 a[36:39], v240, s[68:71], s28 offen\n"
      "s_mov_b32 s28, 0x14000\n"
      "buffer_load_dwordx4 a[40:43], v240, s[68:71], s28 offen\n"
      "s_mov_b32 s28, 0x16000\n"
      "buffer_load_dwordx4 a[44:47], v240, s[68:71], s28 offen\n"
      "s_mov_b32 s28, 0x18000\n"
      "buffer_load_dwordx4 a[48:51], v240, s[68:71], s28 offen\n"
      "s_mov_b32 s28, 0x1a000\n"
      "buffer_load_dwordx4 a[52:55], v240, s[68:71], s28 offen\n"
      "s_mov_b32 s28, 0x1c000\n"
      "buffer_load_dwordx4 a[56:59], v240, s[68:71], s28 offen\n"
      "s_mov_b32 s28, 0x1e000\n"
      "buffer_load_dwordx4 a[60:63], v240, s[68:71], s28 offen\n"
      "s_waitcnt vmcnt(12)\n"
      "s_waitcnt lgkmcnt(0)\n"
      "s_add_u32 s85, s85, 1\n"
      "ds_write_b128 v241, a[0:3] offset:0\n"
      "ds_write_b128 v241, a[4:7] offset:1280\n"
      "ds_write_b128 v241, a[8:11] offset:2560\n"
      "ds_write_b128 v241, a[12:15] offset:3840\n"
      "s_mov_b32 s28, 0x20000\n"
      "buffer_load_dwordx4 a[64:67], v240, s[68:71], s28 offen\n"
      "s_mov_b32 s28, 0x22000\n"
      "buffer_load_dwordx4 a[68:71], v240, s[68:71], s28 offen\n"
      "s_mov_b32 s28, 0x24000\n"
      "buffer_load_dwordx4 a[72:75], v240, s[68:71], s28 offen\n"
      "s_mov_b32 s28, 0x26000\n"
      "buffer_load_dwordx4 a[76:79], v240, s[68:71], s28 offen\n"
      "ds_read_b64_tr_b16 a[0:1], v242 offset:0\n"
      "ds_read_b64_tr_b16 a[2:3], v242 offset:2560\n"
      "ds_read_b64_tr_b16 a[4:5], v242 offset:32\n"
      "ds_read_b64_tr_b16 a[6:7], v242 offset:2592\n"
      "ds_read_b64_tr_b16 a[8:9], v242 offset:64\n"
      "ds_read_b64_tr_b16 a[10:11], v242 offset:2624\n"
      "ds_read_b64_tr_b16 a[12:13], v242 offset:96\n"
      "ds_read_b64_tr_b16 a[14:15], v242 offset:2656\n"
      "s_waitcnt lgkmcnt(2)\n"
      "s_waitcnt vmcnt(12)\n"
      "ds_write_b128 v241, a[16:19] offset:0\n"
      "ds_write_b128 v241, a[20:23] offset:1280\n"
      "ds_write_b128 v241, a[24:27] offset:2560\n"
      "ds_write_b128 v241, a[28:31] offset:3840\n"
      "s_mov_b32 s28, 0x28000\n"
      "buffer_load_dwordx4 a[80:83], v240, s[68:71], s28 offen\n"
      "s_mov_b32 s28, 0x2a000\n"
      "buffer_load_dwordx4 a[84:87], v240, s[68:71], s28 offen\n"
      "s_mov_b32 s28, 0x2c000\n"
      "buffer_load_dwordx4 a[88:91], v240, s[68:71], s28 offen\n"
      "s_mov_b32 s28, 0x2e000\n"
      "buffer_load_dwordx4 a[92:95], v240, s[68:71], s28 offen\n"
      "ds_read_b64_tr_b16 a[16:17], v242 offset:0\n"
      "ds_read_b64_tr_b16 a[18:19], v242 offset:2560\n"
      "ds_read_b64_tr_b16 a[20:21], v242 offset:32\n"
      "ds_read_b64_tr_b16 a[22:23], v242 offset:2592\n"
      "ds_read_b64_tr_b16 a[24:25], v242 offset:64\n"
      "ds_read_b64_tr_b16 a[26:27], v242 offset:2624\n"
      "ds_read_b64_tr_b16 a[28:29], v242 offset:96\n"
      "ds_read_b64_tr_b16 a[30:31], v242 offset:2656\n"
      "s_waitcnt lgkmcnt(2)\n"
      "s_waitcnt vmcnt(12)\n"
      "ds_write_b128 v241, a[32:35] offset:0\n"
      "ds_write_b128 v241, a[36:39] offset:1280\n"
      "ds_write_b128 v241, a[40:43] offset:2560\n"
      "ds_write_b128 v241, a[44:47] offset:3840\n"
      "s_mov_b32 s28, 0x30000\n"
      "buffer_load_dwordx4 a[96:99], v240, s[68:71], s28 offen\n"
      "s_mov_b32 s28, 0x32000\n"
      "buffer_load_dwordx4 a[100:103], v240, s[68:71], s28 offen\n"
      "s_mov_b32 s28, 0x34000\n"
      "buffer_load_dwordx4 a[104:107], v240, s[68:71], s28 offen\n"
      "s_mov_b32 s28, 0x36000\n"
      "buffer_load_dwordx4 a[108:111], v240, s[68:71], s28 offen\n"
      "ds_read_b64_tr_b16 a[32:33], v242 offset:0\n"
      "ds_read_b64_tr_b16 a[34:35], v242 offset:2560\n"
      "ds_read_b64_tr_b16 a[36:37], v242 offset:32\n"
      "ds_read_b64_tr_b16 a[38:39], v242 offset:2592\n"
      "ds_read_b64_tr_b16 a[40:41], v242 offset:64\n"
      "ds_read_b64_tr_b16 a[42:43], v242 offset:2624\n"
      "ds_read_b64_tr_b16 a[44:45], v242 offset:96\n"
      "ds_read_b64_tr_b16 a[46:47], v242 offset:2656\n"
      "s_waitcnt lgkmcnt(2)\n"
      "s_waitcnt vmcnt(12)\n"
      "ds_write_b128 v241, a[48:51] offset:0\n"
      "ds_write_b128 v241, a[52:55] offset:1280\n"
      "ds_write_b128 v241, a[56:59] offset:2560\n"
      "ds_write_b128 v241, a[60:63] offset:3840\n"
      "s_mov_b32 s28, 0x38000\n"
      "buffer_load_dwordx4 a[112:115], v240, s[68:71], s28 offen\n"
      "s_mov_b32 s28, 0x3a000\n"
      "buffer_load_dwordx4 a[116:119], v240, s[68:71], s28 offen\n"
      "s_mov_b32 s28, 0x3c000\n"
      "buffer_load_dwordx4 a[120:123], v240, s[68:71], s28 offen\n"
      "s_mov_b32 s28, 0x3e000\n"
      "buffer_load_dwordx4 a[124:127], v240, s[68:71], s28 offen\n"
      "ds_read_b64_tr_b16 a[48:49], v242 offset:0\n"
      "ds_read_b64_tr_b16 a[50:51], v242 offset:2560\n"
      "ds_read_b64_tr_b16 a[52:53], v242 offset:32\n"
      "ds_read_b64_tr_b16 a[54:55], v242 offset:2592\n"
      "ds_read_b64_tr_b16 a[56:57], v242 offset:64\n"
      "ds_read_b64_tr_b16 a[58:59], v242 offset:2624\n"
      "ds_read_b64_tr_b16 a[60:61], v242 offset:96\n"
      "ds_read_b64_tr_b16 a[62:63], v242 offset:2656\n"
      "s_waitcnt lgkmcnt(2)\n"
      "s_waitcnt vmcnt(12)\n"
      "ds_write_b128 v241, a[64:67] offset:0\n"
      "ds_write_b128 v241, a[68:71] offset:1280\n"
      "ds_write_b128 v241, a[72:75] offset:2560\n"
      "ds_write_b128 v241, a[76:79] offset:3840\n"
      "s_mov_b32 s28, 0x40000\n"
      "buffer_load_dwordx4 a[128:131], v240, s[68:71], s28 offen\n"
      "s_mov_b32 s28, 0x42000\n"
      "buffer_load_dwordx4 a[132:135], v240, s[68:71], s28 offen\n"
      "s_mov_b32 s28, 0x44000\n"
      "buffer_load_dwordx4 a[136:139], v240, s[68:71], s28 offen\n"
      "s_mov_b32 s28, 0x46000\n"
      "buffer_load_dwordx4 a[140:143], v240, s[68:71], s28 offen\n"
      "ds_read_b64_tr_b16 a[64:65], v242 offset:0\n"
      "ds_read_b64_tr_b16 a[66:67], v242 offset:2560\n"
      "ds_read_b64_tr_b16 a[68:69], v242 offset:32\n"
      "ds_read_b64_tr_b16 a[70:71], v242 offset:2592\n"
      "ds_read_b64_tr_b16 a[72:73], v242 offset:64\n"
      "ds_read_b64_tr_b16 a[74:75], v242 offset:2624\n"
      "ds_read_b64_tr_b16 a[76:77], v242 offset:96\n"
      "ds_read_b64_tr_b16 a[78:79], v242 offset:2656\n"
      "s_waitcnt lgkmcnt(2)\n"
      "s_waitcnt vmcnt(12)\n"
      "ds_write_b128 v241, a[80:83] offset:0\n"
      "ds_write_b128 v241, a[84:87] offset:1280\n"
      "ds_write_b128 v241, a[88:91] offset:2560\n"
      "ds_write_b128 v241, a[92:95] offset:3840\n"
      "s_mov_b32 s28, 0x48000\n"
      "buffer_load_dwordx4 a[144:147], v240, s[68:71], s28 offen\n"
      "s_mov_b32 s28, 0x4a000\n"
      "buffer_load_dwordx4 a[148:151], v240, s[68:71], s28 offen\n"
      "s_mov_b32 s28, 0x4c000\n"
      "buffer_load_dwordx4 a[152:155], v240, s[68:71], s28 offen\n"
      "s_mov_b32 s28, 0x4e000\n"
      "buffer_load_dwordx4 a[156:159], v240, s[68:71], s28 offen\n"
      "ds_read_b64_tr_b16 a[80:81], v242 offset:0\n"
      "ds_read_b64_tr_b16 a[82:83], v242 offset:2560\n"
      "ds_read_b64_tr_b16 a[84:85], v242 offset:32\n"
      "ds_read_b64_tr_b16 a[86:87], v242 offset:2592\n"
      "ds_read_b64_tr_b16 a[88:89], v242 offset:64\n"
      "ds_read_b64_tr_b16 a[90:91], v242 offset:2624\n"
      "ds_read_b64_tr_b16 a[92:93], v242 offset:96\n"
      "ds_read_b64_tr_b16 a[94:95], v242 offset:2656\n"
      "s_waitcnt lgkmcnt(2)\n"
      "s_waitcnt vmcnt(12)\n"
      "ds_write_b128 v241, a[96:99] offset:0\n"
      "ds_write_b128 v241, a[100:103] offset:1280\n"
      "ds_write_b128 v241, a[104:107] offset:2560\n"
      "ds_write_b128 v241, a[108:111] offset:3840\n"
      "s_mov_b32 s28, 0x50000\n"
      "buffer_load_dwordx4 a[160:163], v240, s[68:71], s28 offen\n"
      "s_mov_b32 s28, 0x52000\n"
      "buffer_load_dwordx4 a[164:167], v240, s[68:71], s28 offen\n"
      "s_mov_b32 s28, 0x54000\n"
      "buffer_load_dwordx4 a[168:171], v240, s[68:71], s28 offen\n"
      "s_mov_b32 s28, 0x56000\n"
      "buffer_load_dwordx4 a[172:175], v240, s[68:71], s28 offen\n"
      "ds_read_b64_tr_b16 a[96:97], v242 offset:0\n"
      "ds_read_b64_tr_b16 a[98:99], v242 offset:2560\n"
      "ds_read_b64_tr_b16 a[100:101], v242 offset:32\n"
      "ds_read_b64_tr_b16 a[102:103], v242 offset:2592\n"
      "ds_read_b64_tr_b16 a[104:105], v242 offset:64\n"
      "ds_read_b64_tr_b16 a[106:107], v242 offset:2624\n"
      "ds_read_b64_tr_b16 a[108:109], v242 offset:96\n"
      "ds_read_b64_tr_b16 a[110:111], v242 offset:2656\n"
      "s_waitcnt lgkmcnt(2)\n"
      "s_waitcnt vmcnt(12)\n"
      "ds_write_b128 v241, a[112:115] offset:0\n"
      "ds_write_b128 v241, a[116:119] offset:1280\n"
      "ds_write_b128 v241, a[120:123] offset:2560\n"
      "ds_write_b128 v241, a[124:127] offset:3840\n"
      "s_mov_b32 s28, 0x58000\n"
      "buffer_load_dwordx4 a[176:179], v240, s[68:71], s28 offen\n"
      "s_mov_b32 s28, 0x5a000\n"
      "buffer_load_dwordx4 a[180:183], v240, s[68:71], s28 offen\n"
      "s_mov_b32 s28, 0x5c000\n"
      "buffer_load_dwordx4 a[184:187], v240, s[68:71], s28 offen\n"
      "s_mov_b32 s28, 0x5e000\n"
      "buffer_load_dwordx4 a[188:191], v240, s[68:71], s28 offen\n"
      "ds_read_b64_tr_b16 a[112:113], v242 offset:0\n"
      "ds_read_b64_tr_b16 a[114:115], v242 offset:2560\n"
      "ds_read_b64_tr_b16 a[116:117], v242 offset:32\n"
      "ds_read_b64_tr_b16 a[118:119], v242 offset:2592\n"
      "ds_read_b64_tr_b16 a[120:121], v242 offset:64\n"
      "ds_read_b64_tr_b16 a[122:123], v242 offset:2624\n"
      "ds_read_b64_tr_b16 a[124:125], v242 offset:96\n"
      "ds_read_b64_tr_b16 a[126:127], v242 offset:2656\n"
      "s_waitcnt lgkmcnt(2)\n"
      "s_waitcnt vmcnt(12)\n"
      "ds_write_b128 v241, a[128:131] offset:0\n"
      "ds_write_b128 v241, a[132:135] offset:1280\n"
      "ds_write_b128 v241, a[136:139] offset:2560\n"
      "ds_write_b128 v241, a[140:143] offset:3840\n"
      "s_mov_b32 s28, 0x60000\n"
      "buffer_load_dwordx4 a[192:195], v240, s[68:71], s28 offen\n"
      "s_mov_b32 s28, 0x62000\n"
      "buffer_load_dwordx4 a[196:199], v240, s[68:71], s28 offen\n"
      "s_mov_b32 s28, 0x64000\n"
      "buffer_load_dwordx4 a[200:203], v240, s[68:71], s28 offen\n"
      "s_mov_b32 s28, 0x66000\n"
      "buffer_load_dwordx4 a[204:207], v240, s[68:71], s28 offen\n"
      "ds_read_b64_tr_b16 a[128:129], v242 offset:0\n"
      "ds_read_b64_tr_b16 a[130:131], v242 offset:2560\n"
      "ds_read_b64_tr_b16 a[132:133], v242 offset:32\n"
      "ds_read_b64_tr_b16 a[134:135], v242 offset:2592\n"
      "ds_read_b64_tr_b16 a[136:137], v242 offset:64\n"
      "ds_read_b64_tr_b16 a[138:139], v242 offset:2624\n"
      "ds_read_b64_tr_b16 a[140:141], v242 offset:96\n"
      "ds_read_b64_tr_b16 a[142:143], v242 offset:2656\n"
      "s_waitcnt lgkmcnt(2)\n"
      "s_waitcnt vmcnt(12)\n"
      "ds_write_b128 v241, a[144:147] offset:0\n"
      "ds_write_b128 v241, a[148:151] offset:1280\n"
      "ds_write_b128 v241, a[152:155] offset:2560\n"
      "ds_write_b128 v241, a[156:159] offset:3840\n"
      "s_mov_b32 s28, 0x68000\n"
      "buffer_load_dwordx4 a[208:211], v240, s[68:71], s28 offen\n"
      "s_mov_b32 s28, 0x6a000\n"
      "buffer_load_dwordx4 a[212:215], v240, s[68:71], s28 offen\n"
      "s_mov_b32 s28, 0x6c000\n"
      "buffer_load_dwordx4 a[216:219], v240, s[68:71], s28 offen\n"
      "s_mov_b32 s28, 0x6e000\n"
      "buffer_load_dwordx4 a[220:223], v240, s[68:71], s28 offen\n"
      "ds_read_b64_tr_b16 a[144:145], v242 offset:0\n"
      "ds_read_b64_tr_b16 a[146:147], v242 offset:2560\n"
      "ds_read_b64_tr_b16 a[148:149], v242 offset:32\n"
      "ds_read_b64_tr_b16 a[150:151], v242 offset:2592\n"
      "ds_read_b64_tr_b16 a[152:153], v242 offset:64\n"
      "ds_read_b64_tr_b16 a[154:155], v242 offset:2624\n"
      "ds_read_b64_tr_b16 a[156:157], v242 offset:96\n"
      "ds_read_b64_tr_b16 a[158:159], v242 offset:2656\n"
      "s_waitcnt lgkmcnt(2)\n"
      "s_waitcnt vmcnt(12)\n"
      "ds_write_b128 v241, a[160:163] offset:0\n"
      "ds_write_b128 v241, a[164:167] offset:1280\n"
      "ds_write_b128 v241, a[168:171] offset:2560\n"
      "ds_write_b128 v241, a[172:175] offset:3840\n"
      "ds_read_b64_tr_b16 a[160:161], v242 offset:0\n"
      "ds_read_b64_tr_b16 a[162:163], v242 offset:2560\n"
      "ds_read_b64_tr_b16 a[164:165], v242 offset:32\n"
      "ds_read_b64_tr_b16 a[166:167], v242 offset:2592\n"
      "ds_read_b64_tr_b16 a[168:169], v242 offset:64\n"
      "ds_read_b64_tr_b16 a[170:171], v242 offset:2624\n"
      "ds_read_b64_tr_b16 a[172:173], v242 offset:96\n"
      "ds_read_b64_tr_b16 a[174:175], v242 offset:2656\n"
      "s_waitcnt lgkmcnt(2)\n"
      "s_waitcnt vmcnt(8)\n"
      "ds_write_b128 v241, a[176:179] offset:0\n"
      "ds_write_b128 v241, a[180:183] offset:1280\n"
      "ds_write_b128 v241, a[184:187] offset:2560\n"
      "ds_write_b128 v241, a[188:191] offset:3840\n"
      "ds_read_b64_tr_b16 a[176:177], v242 offset:0\n"
      "ds_read_b64_tr_b16 a[178:179], v242 offset:2560\n"
      "ds_read_b64_tr_b16 a[180:181], v242 offset:32\n"
      "ds_read_b64_tr_b16 a[182:183], v242 offset:2592\n"
      "ds_read_b64_tr_b16 a[184:185], v242 offset:64\n"
      "ds_read_b64_tr_b16 a[186:187], v242 offset:2624\n"
      "ds_read_b64_tr_b16 a[188:189], v242 offset:96\n"
      "ds_read_b64_tr_b16 a[190:191], v242 offset:2656\n"
      "s_waitcnt lgkmcnt(2)\n"
      "s_waitcnt vmcnt(4)\n"
      "ds_write_b128 v241, a[192:195] offset:0\n"
      "ds_write_b128 v241, a[196:199] offset:1280\n"
      "ds_write_b128 v241, a[200:203] offset:2560\n"
      "ds_write_b128 v241, a[204:207] offset:3840\n"
      "ds_read_b64_tr_b16 a[192:193], v242 offset:0\n"
      "ds_read_b64_tr_b16 a[194:195], v242 offset:2560\n"
      "ds_read_b64_tr_b16 a[196:197], v242 offset:32\n"
      "ds_read_b64_tr_b16 a[198:199], v242 offset:2592\n"
      "ds_read_b64_tr_b16 a[200:201], v242 offset:64\n"
      "ds_read_b64_tr_b16 a[202:203], v242 offset:2624\n"
      "ds_read_b64_tr_b16 a[204:205], v242 offset:96\n"
      "ds_read_b64_tr_b16 a[206:207], v242 offset:2656\n"
      "s_waitcnt lgkmcnt(2)\n"
      "s_waitcnt vmcnt(0)\n"
      "ds_write_b128 v241, a[208:211] offset:0\n"
      "ds_write_b128 v241, a[212:215] offset:1280\n"
      "ds_write_b128 v241, a[216:219] offset:2560\n"
      "ds_write_b128 v241, a[220:223] offset:3840\n"
      "ds_read_b64_tr_b16 a[208:209], v242 offset:0\n"
      "ds_read_b64_tr_b16 a[210:211], v242 offset:2560\n"
      "ds_read_b64_tr_b16 a[212:213], v242 offset:32\n"
      "ds_read_b64_tr_b16 a[214:215], v242 offset:2592\n"
      "ds_read_b64_tr_b16 a[216:217], v242 offset:64\n"
      "ds_read_b64_tr_b16 a[218:219], v242 offset:2624\n"
      "ds_read_b64_tr_b16 a[220:221], v242 offset:96\n"
      "ds_read_b64_tr_b16 a[222:223], v242 offset:2656\n"
      "s_waitcnt lgkmcnt(2)\n"
      "s_mov_b32 s38, 0x100000\n"
      "s_waitcnt lgkmcnt(0)\n"
      "LO_ln:\n"
      "buffer_load_dwordx2 v[0:1], v100, s[8:11], 0 offen offset:0 sc1\n"
      "s_sub_u32 s38, s38, 1\n"
      "s_cbranch_scc1 LO_lng\n"
      "s_waitcnt vmcnt(0)\n"
      "LO_lnc:\n"
      "v_cmp_ne_u32_e64 s[54:55], s85, v1\n"
      "v_cmp_ge_i32 vcc, -1, v0\n"
      "s_or_b64 vcc, vcc, s[54:55]\n"
      "s_cbranch_vccz LO_lnk\n"
      "s_sleep 3\n"
      "s_branch LO_ln\n"
      "LO_lng:\n"
      "s_mov_b32 s38, 0\n"
      "s_waitcnt vmcnt(0)\n"
      "LO_lnk:\n"
      "v_readlane_b32 s55, v0, 63\n"
      "s_and_b32 s55, s55, 1\n"
      "s_lshl_b32 s55, s55, 21\n"
      "s_add_u32 s14, s14, s55\n"
      "s_add_u32 s15, s15, s55\n"
      "s_add_u32 s17, s17, s55\n"
      "v_mov_b32 v242, v101\n"
      "LO_loop:\n"
      "s_lshr_b32 s28, s12, 5\n"
      "s_lshr_b32 s29, s16, s28\n"
      "s_bitcmp1_b32 s29, 0\n"
      "s_cselect_b32 s40, 0x2000, 0\n"
      "s_add_u32 s40, s40, 0x0\n"
      "s_xor_b32 s58, s40, 0x2000\n"
      "s_bitcmp1_b32 s29, 2\n"
      "s_cselect_b32 s41, 0x2000, 0\n"
      "s_add_u32 s41, s41, 0x400000\n"
      "s_xor_b32 s59, s41, 0x2000\n"
      "s_bitcmp1_b32 s29, 4\n"
      "s_cselect_b32 s42, 0x2000, 0\n"
      "s_add_u32 s42, s42, 0x800000\n"
      "s_xor_b32 s60, s42, 0x2000\n"
      "s_bitcmp1_b32 s29, 6\n"
      "s_cselect_b32 s43, 0x2000, 0\n"
      "s_add_u32 s43, s43, 0xc00000\n"
      "s_xor_b32 s61, s43, 0x2000\n"
      "s_bitcmp1_b32 s29, 8\n"
      "s_cselect_b32 s44, 0x2000, 0\n"
      "s_add_u32 s44, s44, 0x1000000\n"
      "s_xor_b32 s62, s44, 0x2000\n"
      "s_bitcmp1_b32 s29, 10\n"
      "s_cselect_b32 s45, 0x2000, 0\n"
      "s_add_u32 s45, s45, 0x1400000\n"
      "s_xor_b32 s63, s45, 0x2000\n"
      "s_bitcmp1_b32 s29, 12\n"
      "s_cselect_b32 s46, 0x2000, 0\n"
      "s_add_u32 s46, s46, 0x1800000\n"
      "s_xor_b32 s64, s46, 0x2000\n"
      "s_bitcmp1_b32 s29, 14\n"
      "s_cselect_b32 s47, 0x2000, 0\n"
      "s_add_u32 s47, s47, 0x1c00000\n"
      "s_xor_b32 s65, s47, 0x2000\n"
      "LO_w1:\n"
      "buffer_load_dwordx2 v[240:241], v246, s[8:11], s18 offen offset:0 sc1\n"
      "s_sub_u32 s38, s38, 1\n"
      "s_cbranch_scc1 LO_w1g\n"
      "s_waitcnt vmcnt(0)\n"
      "LO_w1c:\n"
      "v_cmp_ne_u32_e64 s[54:55], s85, v241\n"
      "v_cmp_ge_i32 vcc, s12, v240\n"
      "s_or_b64 vcc, vcc, s[54:55]\n"
      "s_cbranch_vccz LO_w1k\n"
      "s_sleep 3\n"
      "s_branch LO_w1\n"
      "LO_w1g:\n"
      "s_mov_b32 s38, 0\n"
      "s_waitcnt vmcnt(0)\n"
      "LO_w1k:\n"
      "s_add_u32 s28, s13, 0\n"
      "buffer_load_dwordx4 v[128:131], v243, s[8:11], s28 offen offset:0 sc1\n"
      "buffer_load_dwordx4 v[132:135], v243, s[8:11], s28 offen offset:1024 sc1\n"
      "buffer_load_dwordx4 v[136:139], v243, s[8:11], s28 offen offset:2048 sc1\n"
      "buffer_load_dwordx4 v[140:143], v243, s[8:11], s28 offen offset:3072 sc1\n"
      "s_add_u32 s28, s13, 4096\n"
      "buffer_load_dwordx4 v[144:147], v243, s[8:11], s28 offen offset:0 sc1\n"
      "buffer_load_dwordx4 v[148:151], v243, s[8:11], s28 offen offset:1024 sc1\n"
      "buffer_load_dwordx4 v[152:155], v243, s[8:11], s28 offen offset:2048 sc1\n"
      "buffer_load_dwordx4 v[156:159], v243, s[8:11], s28 offen offset:3072 sc1\n"
      "s_add_u32 s28, s13, 8192\n"
      "buffer_load_dwordx4 v[160:163], v243, s[8:11], s28 offen offset:0 sc1\n"
      "buffer_load_dwordx4 v[164:167], v243, s[8:11], s28 offen offset:1024 sc1\n"
      "buffer_load_dwordx4 v[168:171], v243, s[8:11], s28 offen offset:2048 sc1\n"
      "buffer_load_dwordx4 v[172:175], v243, s[8:11], s28 offen offset:3072 sc1\n"
      "s_add_u32 s28, s13, 12288\n"
      "buffer_load_dwordx4 v[176:179], v243, s[8:11], s28 offen offset:0 sc1\n"
      "buffer_load_dwordx4 v[180:183], v243, s[8:11], s28 offen offset:1024 sc1\n"
      "buffer_load_dwordx4 v[184:187], v243, s[8:11], s28 offen offset:2048 sc1\n"
      "buffer_load_dwordx4 v[188:191], v243, s[8:11], s28 offen offset:3072 sc1\n"
      "s_add_u32 s28, s13, 16384\n"
      "buffer_load_dwordx4 v[192:195], v243, s[8:11], s28 offen offset:0 sc1\n"
      "buffer_load_dwordx4 v[196:199], v243, s[8:11], s28 offen offset:1024 sc1\n"
      "buffer_load_dwordx4 v[200:203], v243, s[8:11], s28 offen offset:2048 sc1\n"
      "buffer_load_dwordx4 v[204:207], v243, s[8:11], s28 offen offset:3072 sc1\n"
      "s_add_u32 s28, s13, 20480\n"
      "buffer_load_dwordx4 v[208:211], v243, s[8:11], s28 offen offset:0 sc1\n"
      "buffer_load_dwordx4 v[212:215], v243, s[8:11], s28 offen offset:1024 sc1\n"
      "buffer_load_dwordx4 v[216:219], v243, s[8:11], s28 offen offset:2048 sc1\n"
      "buffer_load_dwordx4 v[220:223], v243, s[8:11], s28 offen offset:3072 sc1\n"
      "s_add_u32 s28, s13, 24576\n"
      "buffer_load_dwordx4 v[224:227], v243, s[8:11], s28 offen offset:0 sc1\n"
      "buffer_load_dwordx4 v[228:231], v243, s[8:11], s28 offen offset:1024 sc1\n"
      "buffer_load_dwordx4 v[232:235], v243, s[8:11], s28 offen offset:2048 sc1\n"
      "buffer_load_dwordx4 v[236:239], v243, s[8:11], s28 offen offset:3072 sc1\n"
      "s_barrier\n"
      "s_waitcnt vmcnt(27)\n"
      "v_mfma_f32_16x16x32_bf16 a[224:227], a[0:3], v[128:131], 0\n"
      "v_mfma_f32_16x16x32_bf16 a[228:231], a[4:7], v[128:131], 0\n"
      "v_mfma_f32_16x16x32_bf16 a[232:235], a[8:11], v[128:131], 0\n"
      "v_mfma_f32_16x16x32_bf16 a[236:239], a[12:15], v[128:131], 0\n"
      "s_waitcnt vmcnt(26)\n"
      "v_mfma_f32_16x16x32_bf16 a[240:243], a[0:3], v[132:135], 0\n"
      "v_mfma_f32_16x16x32_bf16 a[244:247], a[4:7], v[132:135], 0\n"
      "v_mfma_f32_16x16x32_bf16 a[248:251], a[8:11], v[132:135], 0\n"
      "v_mfma_f32_16x16x32_bf16 a[252:255], a[12:15], v[132:135], 0\n"
      "s_waitcnt vmcnt(25)\n"
      "v_mfma_f32_16x16x32_bf16 a[224:227], a[16:19], v[136:139], a[224:227]\n"
      "v_mfma_f32_16x16x32_bf16 a[228:231], a[20:23], v[136:139], a[228:231]\n"
      "v_mfma_f32_16x16x32_bf16 a[232:235], a[24:27], v[136:139], a[232:235]\n"
      "v_mfma_f32_16x16x32_bf16 a[236:239], a[28:31], v[136:139], a[236:239]\n"
      "s_waitcnt vmcnt(24)\n"
      "v_mfma_f32_16x16x32_bf16 a[240:243], a[16:19], v[140:143], a[240:243]\n"
      "v_mfma_f32_16x16x32_bf16 a[244:247], a[20:23], v[140:143], a[244:247]\n"
      "v_mfma_f32_16x16x32_bf16 a[248:251], a[24:27], v[140:143], a[248:251]\n"
      "v_mfma_f32_16x16x32_bf16 a[252:255], a[28:31], v[140:143], a[252:255]\n"
      "s_waitcnt vmcnt(23)\n"
      "v_mfma_f32_16x16x32_bf16 a[224:227], a[32:35], v[144:147], a[224:227]\n"
      "v_mfma_f32_16x16x32_bf16 a[228:231], a[36:39], v[144:147], a[228:231]\n"
      "v_mfma_f32_16x16x32_bf16 a[232:235], a[40:43], v[144:147], a[232:235]\n"
      "v_mfma_f32_16x16x32_bf16 a[236:239], a[44:47], v[144:147], a[236:239]\n"
      "s_waitcnt vmcnt(22)\n"
      "v_mfma_f32_16x16x32_bf16 a[240:243], a[32:35], v[148:151], a[240:243]\n"
      "v_mfma_f32_16x16x32_bf16 a[244:247], a[36:39], v[148:151], a[244:247]\n"
      "v_mfma_f32_16x16x32_bf16 a[248:251], a[40:43], v[148:151], a[248:251]\n"
      "v_mfma_f32_16x16x32_bf16 a[252:255], a[44:47], v[148:151], a[252:255]\n"
      "s_waitcnt vmcnt(21)\n"
      "v_mfma_f32_16x16x32_bf16 a[224:227], a[48:51], v[152:155], a[224:227]\n"
      "v_mfma_f32_16x16x32_bf16 a[228:231], a[52:55], v[152:155], a[228:231]\n"
      "v_mfma_f32_16x16x32_bf16 a[232:235], a[56:59], v[152:155], a[232:235]\n"
      "v_mfma_f32_16x16x32_bf16 a[236:239], a[60:63], v[152:155], a[236:239]\n"
      "s_waitcnt vmcnt(20)\n"
      "v_mfma_f32_16x16x32_bf16 a[240:243], a[48:51], v[156:159], a[240:243]\n"
      "v_mfma_f32_16x16x32_bf16 a[244:247], a[52:55], v[156:159], a[244:247]\n"
      "v_mfma_f32_16x16x32_bf16 a[248:251], a[56:59], v[156:159], a[248:251]\n"
      "v_mfma_f32_16x16x32_bf16 a[252:255], a[60:63], v[156:159], a[252:255]\n"
      "s_waitcnt vmcnt(19)\n"
      "v_mfma_f32_16x16x32_bf16 a[224:227], a[64:67], v[160:163], a[224:227]\n"
      "v_mfma_f32_16x16x32_bf16 a[228:231], a[68:71], v[160:163], a[228:231]\n"
      "v_mfma_f32_16x16x32_bf16 a[232:235], a[72:75], v[160:163], a[232:235]\n"
      "v_mfma_f32_16x16x32_bf16 a[236:239], a[76:79], v[160:163], a[236:239]\n"
      "s_waitcnt vmcnt(18)\n"
      "v_mfma_f32_16x16x32_bf16 a[240:243], a[64:67], v[164:167], a[240:243]\n"
      "v_mfma_f32_16x16x32_bf16 a[244:247], a[68:71], v[164:167], a[244:247]\n"
      "v_mfma_f32_16x16x32_bf16 a[248:251], a[72:75], v[164:167], a[248:251]\n"
      "v_mfma_f32_16x16x32_bf16 a[252:255], a[76:79], v[164:167], a[252:255]\n"
      "s_waitcnt vmcnt(17)\n"
      "v_mfma_f32_16x16x32_bf16 a[224:227], a[80:83], v[168:171], a[224:227]\n"
      "v_mfma_f32_16x16x32_bf16 a[228:231], a[84:87], v[168:171], a[228:231]\n"
      "v_mfma_f32_16x16x32_bf16 a[232:235], a[88:91], v[168:171], a[232:235]\n"
      "v_mfma_f32_16x16x32_bf16 a[236:239], a[92:95], v[168:171], a[236:239]\n"
      "s_waitcnt vmcnt(16)\n"
      "v_mfma_f32_16x16x32_bf16 a[240:243], a[80:83], v[172:175], a[240:243]\n"
      "v_mfma_f32_16x16x32_bf16 a[244:247], a[84:87], v[172:175], a[244:247]\n"
      "v_mfma_f32_16x16x32_bf16 a[248:251], a[88:91], v[172:175], a[248:251]\n"
      "v_mfma_f32_16x16x32_bf16 a[252:255], a[92:95], v[172:175], a[252:255]\n"
      "s_waitcnt vmcnt(15)\n"
      "v_mfma_f32_16x16x32_bf16 a[224:227], a[96:99], v[176:179], a[224:227]\n"
      "v_mfma_f32_16x16x32_bf16 a[228:231], a[100:103], v[176:179], a[228:231]\n"
      "v_mfma_f32_16x16x32_bf16 a[232:235], a[104:107], v[176:179], a[232:235]\n"
      "v_mfma_f32_16x16x32_bf16 a[236:239], a[108:111], v[176:179], a[236:239]\n"
      "s_waitcnt vmcnt(14)\n"
      "v_mfma_f32_16x16x32_bf16 a[240:243], a[96:99], v[180:183], a[240:243]\n"
      "v_mfma_f32_16x16x32_bf16 a[244:247], a[100:103], v[180:183], a[244:247]\n"
      "v_mfma_f32_16x16x32_bf16 a[248:251], a[104:107], v[180:183], a[248:251]\n"
      "v_mfma_f32_16x16x32_bf16 a[252:255], a[108:111], v[180:183], a[252:255]\n"
      "s_waitcnt vmcnt(13)\n"
      "v_mfma_f32_16x16x32_bf16 a[224:227], a[112:115], v[184:187], a[224:227]\n"
      "v_mfma_f32_16x16x32_bf16 a[228:231], a[116:119], v[184:187], a[228:231]\n"
      "v_mfma_f32_16x16x32_bf16 a[232:235], a[120:123], v[184:187], a[232:235]\n"
      "v_mfma_f32_16x16x32_bf16 a[236:239], a[124:127], v[184:187], a[236:239]\n"
      "s_waitcnt vmcnt(12)\n"
      "v_mfma_f32_16x16x32_bf16 a[240:243], a[112:115], v[188:191], a[240:243]\n"
      "v_mfma_f32_16x16x32_bf16 a[244:247], a[116:119], v[188:191], a[244:247]\n"
      "v_mfma_f32_16x16x32_bf16 a[248:251], a[120:123], v[188:191], a[248:251]\n"
      "v_mfma_f32_16x16x32_bf16 a[252:255], a[124:127], v[188:191], a[252:255]\n"
      "s_waitcnt vmcnt(11)\n"
      "v_mfma_f32_16x16x32_bf16 a[224:227], a[128:131], v[192:195], a[224:227]\n"
      "v_mfma_f32_16x16x32_bf16 a[228:231], a[132:135], v[192:195], a[228:231]\n"
      "v_mfma_f32_16x16x32_bf16 a[232:235], a[136:139], v[192:195], a[232:235]\n"
      "v_mfma_f32_16x16x32_bf16 a[236:239], a[140:143], v[192:195], a[236:239]\n"
      "s_waitcnt vmcnt(10)\n"
      "v_mfma_f32_16x16x32_bf16 a[240:243], a[128:131], v[196:199], a[240:243]\n"
      "v_mfma_f32_16x16x32_bf16 a[244:247], a[132:135], v[196:199], a[244:247]\n"
      "v_mfma_f32_16x16x32_bf16 a[248:251], a[136:139], v[196:199], a[248:251]\n"
      "v_mfma_f32_16x16x32_bf16 a[252:255], a[140:143], v[196:199], a[252:255]\n"
      "s_waitcnt vmcnt(9)\n"
      "v_mfma_f32_16x16x32_bf16 a[224:227], a[144:147], v[200:203], a[224:227]\n"
      "v_mfma_f32_16x16x32_bf16 a[228:231], a[148:151], v[200:203], a[228:231]\n"
      "v_mfma_f32_16x16x32_bf16 a[232:235], a[152:155], v[200:203], a[232:235]\n"
      "v_mfma_f32_16x16x32_bf16 a[236:239], a[156:159], v[200:203], a[236:239]\n"
      "s_waitcnt vmcnt(8)\n"
      "v_mfma_f32_16x16x32_bf16 a[240:243], a[144:147], v[204:207], a[240:243]\n"
      "v_mfma_f32_16x16x32_bf16 a[244:247], a[148:151], v[204:207], a[244:247]\n"
      "v_mfma_f32_16x16x32_bf16 a[248:251], a[152:155], v[204:207], a[248:251]\n"
      "v_mfma_f32_16x16x32_bf16 a[252:255], a[156:159], v[204:207], a[252:255]\n"
      "s_waitcnt vmcnt(7)\n"
      "v_mfma_f32_16x16x32_bf16 a[224:227], a[160:163], v[208:211], a[224:227]\n"
      "v_mfma_f32_16x16x32_bf16 a[228:231], a[164:167], v[208:211], a[228:231]\n"
      "v_mfma_f32_16x16x32_bf16 a[232:235], a[168:171], v[208:211], a[232:235]\n"
      "v_mfma_f32_16x16x32_bf16 a[236:239], a[172:175], v[208:211], a[236:239]\n"
      "s_waitcnt vmcnt(6)\n"
      "v_mfma_f32_16x16x32_bf16 a[240:243], a[160:163], v[212:215], a[240:243]\n"
      "v_mfma_f32_16x16x32_bf16 a[244:247], a[164:167], v[212:215], a[244:247]\n"
      "v_mfma_f32_16x16x32_bf16 a[248:251], a[168:171], v[212:215], a[248:251]\n"
      "v_mfma_f32_16x16x32_bf16 a[252:255], a[172:175], v[212:215], a[252:255]\n"
      "s_waitcnt vmcnt(5)\n"
      "v_mfma_f32_16x16x32_bf16 a[224:227], a[176:179], v[216:219], a[224:227]\n"
      "v_mfma_f32_16x16x32_bf16 a[228:231], a[180:183], v[216:219], a[228:231]\n"
      "v_mfma_f32_16x16x32_bf16 a[232:235], a[184:187], v[216:219], a[232:235]\n"
      "v_mfma_f32_16x16x32_bf16 a[236:239], a[188:191], v[216:219], a[236:239]\n"
      "s_waitcnt vmcnt(4)\n"
      "v_mfma_f32_16x16x32_bf16 a[240:243], a[176:179], v[220:223], a[240:243]\n"
      "v_mfma_f32_16x16x32_bf16 a[244:247], a[180:183], v[220:223], a[244:247]\n"
      "v_mfma_f32_16x16x32_bf16 a[248:251], a[184:187], v[220:223], a[248:251]\n"
      "v_mfma_f32_16x16x32_bf16 a[252:255], a[188:191], v[220:223], a[252:255]\n"
      "s_waitcnt vmcnt(3)\n"
      "v_mfma_f32_16x16x32_bf16 a[224:227], a[192:195], v[224:227], a[224:227]\n"
      "v_mfma_f32_16x16x32_bf16 a[228:231], a[196:199], v[224:227], a[228:231]\n"
      "v_mfma_f32_16x16x32_bf16 a[232:235], a[200:203], v[224:227], a[232:235]\n"
      "v_mfma_f32_16x16x32_bf16 a[236:239], a[204:207], v[224:227], a[236:239]\n"
      "s_waitcnt vmcnt(2)\n"
      "v_mfma_f32_16x16x32_bf16 a[240:243], a[192:195], v[228:231], a[240:243]\n"
      "v_mfma_f32_16x16x32_bf16 a[244:247], a[196:199], v[228:231], a[244:247]\n"
      "v_mfma_f32_16x16x32_bf16 a[248:251], a[200:203], v[228:231], a[248:251]\n"
      "v_mfma_f32_16x16x32_bf16 a[252:255], a[204:207], v[228:231], a[252:255]\n"
      "s_waitcnt vmcnt(1)\n"
      "v_mfma_f32_16x16x32_bf16 a[224:227], a[208:211], v[232:235], a[224:227]\n"
      "v_mfma_f32_16x16x32_bf16 a[228:231], a[212:215], v[232:235], a[228:231]\n"
      "v_mfma_f32_16x16x32_bf16 a[232:235], a[216:219], v[232:235], a[232:235]\n"
      "v_mfma_f32_16x16x32_bf16 a[236:239], a[220:223], v[232:235], a[236:239]\n"
      "s_waitcnt vmcnt(0)\n"
      "v_mfma_f32_16x16x32_bf16 a[240:243], a[208:211], v[236:239], a[240:243]\n"
      "v_mfma_f32_16x16x32_bf16 a[244:247], a[212:215], v[236:239], a[244:247]\n"
      "v_mfma_f32_16x16x32_bf16 a[248:251], a[216:219], v[236:239], a[248:251]\n"
      "v_mfma_f32_16x16x32_bf16 a[252:255], a[220:223], v[236:239], a[252:255]\n"
      "LO_w2:\n"
      "buffer_load_dwordx2 v[240:241], v247, s[8:11], s19 offen offset:0 sc1\n"
      "s_sub_u32 s38, s38, 1\n"
      "s_cbranch_scc1 LO_w2g\n"
      "s_waitcnt vmcnt(0)\n"
      "LO_w2c:\n"
      "v_cmp_ne_u32_e64 s[54:55], s85, v241\n"
      "v_cmp_ge_i32 vcc, s12, v240\n"
      "s_or_b64 vcc, vcc, s[54:55]\n"
      "s_cbranch_vccz LO_w2k\n"
      "s_sleep 3\n"
      "s_branch LO_w2\n"
      "LO_w2g:\n"
      "s_mov_b32 s38, 0\n"
      "s_waitcnt vmcnt(0)\n"
      "LO_w2k:\n"
      "s_add_u32 s28, s14, s40\n"
      "buffer_load_dwordx4 v[0:3], v243, s[8:11], s28 offen offset:0 sc1\n"
      "buffer_load_dwordx4 v[4:7], v243, s[8:11], s28 offen offset:1024 sc1\n"
      "buffer_load_dwordx4 v[8:11], v243, s[8:11], s28 offen offset:2048 sc1\n"
      "buffer_load_dwordx4 v[12:15], v243, s[8:11], s28 offen offset:3072 sc1\n"
      "s_add_u32 s28, s14, s41\n"
      "buffer_load_dwordx4 v[16:19], v243, s[8:11], s28 offen offset:0 sc1\n"
      "buffer_load_dwordx4 v[20:23], v243, s[8:11], s28 offen offset:1024 sc1\n"
      "buffer_load_dwordx4 v[24:27], v243, s[8:11], s28 offen offset:2048 sc1\n"
      "buffer_load_dwordx4 v[28:31], v243, s[8:11], s28 offen offset:3072 sc1\n"
      "s_add_u32 s28, s14, s42\n"
      "buffer_load_dwordx4 v[32:35], v243, s[8:11], s28 offen offset:0 sc1\n"
      "buffer_load_dwordx4 v[36:39], v243, s[8:11], s28 offen offset:1024 sc1\n"
      "buffer_load_dwordx4 v[40:43], v243, s[8:11], s28 offen offset:2048 sc1\n"
      "buffer_load_dwordx4 v[44:47], v243, s[8:11], s28 offen offset:3072 sc1\n"
      "s_add_u32 s28, s14, s43\n"
      "buffer_load_dwordx4 v[48:51], v243, s[8:11], s28 offen offset:0 sc1\n"
      "buffer_load_dwordx4 v[52:55], v243, s[8:11], s28 offen offset:1024 sc1\n"
      "buffer_load_dwordx4 v[56:59], v243, s[8:11], s28 offen offset:2048 sc1\n"
      "buffer_load_dwordx4 v[60:63], v243, s[8:11], s28 offen offset:3072 sc1\n"
      "s_add_u32 s28, s14, s44\n"
      "buffer_load_dwordx4 v[64:67], v243, s[8:11], s28 offen offset:0 sc1\n"
      "buffer_load_dwordx4 v[68:71], v243, s[8:11], s28 offen offset:1024 sc1\n"
      "buffer_load_dwordx4 v[72:75], v243, s[8:11], s28 offen offset:2048 sc1\n"
      "buffer_load_dwordx4 v[76:79], v243, s[8:11], s28 offen offset:3072 sc1\n"
      "s_add_u32 s28, s14, s45\n"
      "buffer_load_dwordx4 v[80:83], v243, s[8:11], s28 offen offset:0 sc1\n"
      "buffer_load_dwordx4 v[84:87], v243, s[8:11], s28 offen offset:1024 sc1\n"
      "buffer_load_dwordx4 v[88:91], v243, s[8:11], s28 offen offset:2048 sc1\n"
      "buffer_load_dwordx4 v[92:95], v243, s[8:11], s28 offen offset:3072 sc1\n"
      "s_add_u32 s28, s14, s46\n"
      "buffer_load_dwordx4 v[96:99], v243, s[8:11], s28 offen offset:0 sc1\n"
      "buffer_load_dwordx4 v[100:103], v243, s[8:11], s28 offen offset:1024 sc1\n"
      "buffer_load_dwordx4 v[104:107], v243, s[8:11], s28 offen offset:2048 sc1\n"
      "buffer_load_dwordx4 v[108:111], v243, s[8:11], s28 offen offset:3072 sc1\n"
      "s_add_u32 s28, s14, s47\n"
      "buffer_load_dwordx4 v[112:115], v243, s[8:11], s28 offen offset:0 sc1\n"
      "buffer_load_dwordx4 v[116:119], v243, s[8:11], s28 offen offset:1024 sc1\n"
      "buffer_load_dwordx4 v[120:123], v243, s[8:11], s28 offen offset:2048 sc1\n"
      "buffer_load_dwordx4 v[124:127], v243, s[8:11], s28 offen offset:3072 sc1\n"
      "buffer_load_dwordx4 v[252:255], v251, s[72:75], s21 offen\n"
      "s_add_u32 s40, s15, s58\n"
      "s_add_u32 s40, s40, 49152\n"
      "s_add_u32 s58, s17, s58\n"
      "s_add_u32 s58, s58, 32768\n"
      "s_add_u32 s41, s15, s59\n"
      "s_add_u32 s41, s41, 0\n"
      "s_add_u32 s59, s17, s59\n"
      "s_add_u32 s59, s59, 49152\n"
      "s_add_u32 s42, s15, s60\n"
      "s_add_u32 s42, s42, 16384\n"
      "s_add_u32 s60, s17, s60\n"
      "s_add_u32 s60, s60, 0\n"
      "s_add_u32 s43, s15, s61\n"
      "s_add_u32 s43, s43, 32768\n"
      "s_add_u32 s61, s17, s61\n"
      "s_add_u32 s61, s61, 16384\n"
      "s_add_u32 s44, s15, s62\n"
      "s_add_u32 s44, s44, 49152\n"
      "s_add_u32 s62, s17, s62\n"
      "s_add_u32 s62, s62, 32768\n"
      "s_add_u32 s45, s15, s63\n"
      "s_add_u32 s45, s45, 0\n"
      "s_add_u32 s63, s17, s63\n"
      "s_add_u32 s63, s63, 49152\n"
      "s_add_u32 s46, s15, s64\n"
      "s_add_u32 s46, s46, 16384\n"
      "s_add_u32 s64, s17, s64\n"
      "s_add_u32 s64, s64, 0\n"
      "s_add_u32 s47, s15, s65\n"
      "s_add_u32 s47, s47, 32768\n"
      "s_add_u32 s65, s17, s65\n"
      "s_add_u32 s65, s65, 16384\n"
      "LO_w3:\n"
      "buffer_load_dwordx2 v[240:241], v248, s[8:11], s20 offen offset:0 sc1\n"
      "s_sub_u32 s38, s38, 1\n"
      "s_cbranch_scc1 LO_w3g\n"
      "s_waitcnt vmcnt(0)\n"
      "LO_w3c:\n"
      "v_cmp_ne_u32_e64 s[54:55], s85, v241\n"
      "v_cmp_ge_i32 vcc, s12, v240\n"
      "s_or_b64 vcc, vcc, s[54:55]\n"
      "s_cbranch_vccz LO_w3k\n"
      "s_sleep 2\n"
      "s_branch LO_w3\n"
      "LO_w3g:\n"
      "s_mov_b32 s38, 0\n"
      "s_waitcnt vmcnt(0)\n"
      "LO_w3k:\n"
      "buffer_load_dwordx4 v[128:131], v243, s[8:11], s40 offen offset:0 sc1\n"
      "buffer_load_dwordx4 v[132:135], v243, s[8:11], s40 offen offset:1024 sc1\n"
      "buffer_load_dwordx4 v[136:139], v243, s[8:11], s41 offen offset:0 sc1\n"
      "buffer_load_dwordx4 v[140:143], v243, s[8:11], s41 offen offset:1024 sc1\n"
      "buffer_load_dwordx4 v[144:147], v243, s[8:11], s42 offen offset:0 sc1\n"
      "buffer_load_dwordx4 v[148:151], v243, s[8:11], s42 offen offset:1024 sc1\n"
      "buffer_load_dwordx4 v[152:155], v243, s[8:11], s43 offen offset:0 sc1\n"
      "buffer_load_dwordx4 v[156:159], v243, s[8:11], s43 offen offset:1024 sc1\n"
      "buffer_load_dwordx4 v[160:163], v243, s[8:11], s44 offen offset:0 sc1\n"
      "buffer_load_dwordx4 v[164:167], v243, s[8:11], s44 offen offset:1024 sc1\n"
      "buffer_load_dwordx4 v[168:171], v243, s[8:11], s45 offen offset:0 sc1\n"
      "buffer_load_dwordx4 v[172:175], v243, s[8:11], s45 offen offset:1024 sc1\n"
      "buffer_load_dwordx4 v[176:179], v243, s[8:11], s46 offen offset:0 sc1\n"
      "buffer_load_dwordx4 v[180:183], v243, s[8:11], s46 offen offset:1024 sc1\n"
      "buffer_load_dwordx4 v[184:187], v243, s[8:11], s47 offen offset:0 sc1\n"
      "buffer_load_dwordx4 v[188:191], v243, s[8:11], s47 offen offset:1024 sc1\n"
      "buffer_load_dwordx2 v[192:193], v244, s[8:11], s58 offen offset:0 sc1\n"
      "buffer_load_dwordx2 v[200:201], v244, s[8:11], s58 offen offset:512 sc1\n"
      "buffer_load_dword v224, v245, s[8:11], s40 offen offset:2048 sc1\n"
      "buffer_load_dword v228, v245, s[8:11], s40 offen offset:2304 sc1\n"
      "buffer_load_dwordx2 v[194:195], v244, s[8:11], s59 offen offset:0 sc1\n"
      "buffer_load_dwordx2 v[202:203], v244, s[8:11], s59 offen offset:512 sc1\n"
      "buffer_load_dword v226, v245, s[8:11], s41 offen offset:2048 sc1\n"
      "buffer_load_dword v230, v245, s[8:11], s41 offen offset:2304 sc1\n"
      "s_waitcnt vmcnt(23)\n"
      "v_mfma_f32_16x16x32_bf16 a[224:227], v[0:3], v[128:131], a[224:227]\n"
      "v_mfma_f32_16x16x32_bf16 a[228:231], v[4:7], v[128:131], a[228:231]\n"
      "buffer_load_dwordx2 v[196:197], v244, s[8:11], s60 offen offset:0 sc1\n"
      "v_mfma_f32_16x16x32_bf16 a[232:235], v[8:11], v[128:131], a[232:235]\n"
      "v_mfma_f32_16x16x32_bf16 a[236:239], v[12:15], v[128:131], a[236:239]\n"
      "buffer_load_dwordx2 v[204:205], v244, s[8:11], s60 offen offset:512 sc1\n"
      "s_waitcnt vmcnt(24)\n"
      "v_mfma_f32_16x16x32_bf16 a[240:243], v[0:3], v[132:135], a[240:243]\n"
      "v_mfma_f32_16x16x32_bf16 a[244:247], v[4:7], v[132:135], a[244:247]\n"
      "buffer_load_dword v225, v245, s[8:11], s42 offen offset:2048 sc1\n"
      "v_mfma_f32_16x16x32_bf16 a[248:251], v[8:11], v[132:135], a[248:251]\n"
      "v_mfma_f32_16x16x32_bf16 a[252:255], v[12:15], v[132:135], a[252:255]\n"
      "buffer_load_dword v229, v245, s[8:11], s42 offen offset:2304 sc1\n"
      "s_waitcnt vmcnt(25)\n"
      "v_mfma_f32_16x16x32_bf16 a[224:227], v[16:19], v[136:139], a[224:227]\n"
      "v_mfma_f32_16x16x32_bf16 a[228:231], v[20:23], v[136:139], a[228:231]\n"
      "buffer_load_dwordx2 v[198:199], v244, s[8:11], s61 offen offset:0 sc1\n"
      "v_mfma_f32_16x16x32_bf16 a[232:235], v[24:27], v[136:139], a[232:235]\n"
      "v_mfma_f32_16x16x32_bf16 a[236:239], v[28:31], v[136:139], a[236:239]\n"
      "buffer_load_dwordx2 v[206:207], v244, s[8:11], s61 offen offset:512 sc1\n"
      "s_waitcnt vmcnt(26)\n"
      "v_mfma_f32_16x16x32_bf16 a[240:243], v[16:19], v[140:143], a[240:243]\n"
      "v_mfma_f32_16x16x32_bf16 a[244:247], v[20:23], v[140:143], a[244:247]\n"
      "buffer_load_dword v227, v245, s[8:11], s43 offen offset:2048 sc1\n"
      "v_mfma_f32_16x16x32_bf16 a[248:251], v[24:27], v[140:143], a[248:251]\n"
      "v_mfma_f32_16x16x32_bf16 a[252:255], v[28:31], v[140:143], a[252:255]\n"
      "buffer_load_dword v231, v245, s[8:11], s43 offen offset:2304 sc1\n"
      "s_waitcnt vmcnt(27)\n"
      "v_mfma_f32_16x16x32_bf16 a[224:227], v[32:35], v[144:147], a[224:227]\n"
      "v_mfma_f32_16x16x32_bf16 a[228:231], v[36:39], v[144:147], a[228:231]\n"
      "buffer_load_dwordx2 v[208:209], v244, s[8:11], s62 offen offset:0 sc1\n"
      "v_mfma_f32_16x16x32_bf16 a[232:235], v[40:43], v[144:147], a[232:235]\n"
      "v_mfma_f32_16x16x32_bf16 a[236:239], v[44:47], v[144:147], a[236:239]\n"
      "buffer_load_dwordx2 v[216:217], v244, s[8:11], s62 offen offset:512 sc1\n"
      "s_waitcnt vmcnt(28)\n"
      "v_mfma_f32_16x16x32_bf16 a[240:243], v[32:35], v[148:151], a[240:243]\n"
      "v_mfma_f32_16x16x32_bf16 a[244:247], v[36:39], v[148:151], a[244:247]\n"
      "buffer_load_dword v232, v245, s[8:11], s44 offen offset:2048 sc1\n"
      "v_mfma_f32_16x16x32_bf16 a[248:251], v[40:43], v[148:151], a[248:251]\n"
      "v_mfma_f32_16x16x32_bf16 a[252:255], v[44:47], v[148:151], a[252:255]\n"
      "buffer_load_dword v236, v245, s[8:11], s44 offen offset:2304 sc1\n"
      "s_waitcnt vmcnt(29)\n"
      "v_mfma_f32_16x16x32_bf16 a[224:227], v[48:51], v[152:155], a[224:227]\n"
      "v_mfma_f32_16x16x32_bf16 a[228:231], v[52:55], v[152:155], a[228:231]\n"
      "buffer_load_dwordx2 v[210:211], v244, s[8:11], s63 offen offset:0 sc1\n"
      "v_mfma_f32_16x16x32_bf16 a[232:235], v[56:59], v[152:155], a[232:235]\n"
      "v_mfma_f32_16x16x32_bf16 a[236:239], v[60:63], v[152:155], a[236:239]\n"
      "buffer_load_dwordx2 v[218:219], v244, s[8:11], s63 offen offset:512 sc1\n"
      "s_waitcnt vmcnt(30)\n"
      "v_mfma_f32_16x16x32_bf16 a[240:243], v[48:51], v[156:159], a[240:243]\n"
      "v_mfma_f32_16x16x32_bf16 a[244:247], v[52:55], v[156:159], a[244:247]\n"
      "buffer_load_dword v234, v245, s[8:11], s45 offen offset:2048 sc1\n"
      "v_mfma_f32_16x16x32_bf16 a[248:251], v[56:59], v[156:159], a[248:251]\n"
      "v_mfma_f32_16x16x32_bf16 a[252:255], v[60:63], v[156:159], a[252:255]\n"
      "buffer_load_dword v238, v245, s[8:11], s45 offen offset:2304 sc1\n"
      "s_waitcnt vmcnt(31)\n"
      "v_mfma_f32_16x16x32_bf16 a[224:227], v[64:67], v[160:163], a[224:227]\n"
      "v_mfma_f32_16x16x32_bf16 a[228:231], v[68:71], v[160:163], a[228:231]\n"
      "buffer_load_dwordx2 v[212:213], v244, s[8:11], s64 offen offset:0 sc1\n"
      "v_mfma_f32_16x16x32_bf16 a[232:235], v[72:75], v[160:163], a[232:235]\n"
      "v_mfma_f32_16x16x32_bf16 a[236:239], v[76:79], v[160:163], a[236:239]\n"
      "buffer_load_dwordx2 v[220:221], v244, s[8:11], s64 offen offset:512 sc1\n"
      "s_waitcnt vmcnt(32)\n"
      "v_mfma_f32_16x16x32_bf16 a[240:243], v[64:67], v[164:167], a[240:243]\n"
      "v_mfma_f32_16x16x32_bf16 a[244:247], v[68:71], v[164:167], a[244:247]\n"
      "buffer_load_dword v233, v245, s[8:11], s46 offen offset:2048 sc1\n"
      "v_mfma_f32_16x16x32_bf16 a[248:251], v[72:75], v[164:167], a[248:251]\n"
      "v_mfma_f32_16x16x32_bf16 a[252:255], v[76:79], v[164:167], a[252:255]\n"
      "buffer_load_dword v237, v245, s[8:11], s46 offen offset:2304 sc1\n"
      "s_waitcnt vmcnt(33)\n"
      "v_mfma_f32_16x16x32_bf16 a[224:227], v[80:83], v[168:171], a[224:227]\n"
      "v_mfma_f32_16x16x32_bf16 a[228:231], v[84:87], v[168:171], a[228:231]\n"
      "buffer_load_dwordx2 v[214:215], v244, s[8:11], s65 offen offset:0 sc1\n"
      "v_mfma_f32_16x16x32_bf16 a[232:235], v[88:91], v[168:171], a[232:235]\n"
      "v_mfma_f32_16x16x32_bf16 a[236:239], v[92:95], v[168:171], a[236:239]\n"
      "buffer_load_dwordx2 v[222:223], v244, s[8:11], s65 offen offset:512 sc1\n"
      "s_waitcnt vmcnt(34)\n"
      "v_mfma_f32_16x16x32_bf16 a[240:243], v[80:83], v[172:175], a[240:243]\n"
      "v_mfma_f32_16x16x32_bf16 a[244:247], v[84:87], v[172:175], a[244:247]\n"
      "buffer_load_dword v235, v245, s[8:11], s47 offen offset:2048 sc1\n"
      "v_mfma_f32_16x16x32_bf16 a[248:251], v[88:91], v[172:175], a[248:251]\n"
      "v_mfma_f32_16x16x32_bf16 a[252:255], v[92:95], v[172:175], a[252:255]\n"
      "buffer_load_dword v239, v245, s[8:11], s47 offen offset:2304 sc1\n"
      "s_waitcnt vmcnt(35)\n"
      "v_mfma_f32_16x16x32_bf16 a[224:227], v[96:99], v[176:179], a[224:227]\n"
      "v_mfma_f32_16x16x32_bf16 a[228:231], v[100:103], v[176:179], a[228:231]\n"
      "v_mfma_f32_16x16x32_bf16 a[232:235], v[104:107], v[176:179], a[232:235]\n"
      "v_mfma_f32_16x16x32_bf16 a[236:239], v[108:111], v[176:179], a[236:239]\n"
      "s_waitcnt vmcnt(34)\n"
      "v_mfma_f32_16x16x32_bf16 a[240:243], v[96:99], v[180:183], a[240:243]\n"
      "v_mfma_f32_16x16x32_bf16 a[244:247], v[100:103], v[180:183], a[244:247]\n"
      "v_mfma_f32_16x16x32_bf16 a[248:251], v[104:107], v[180:183], a[248:251]\n"
      "v_mfma_f32_16x16x32_bf16 a[252:255], v[108:111], v[180:183], a[252:255]\n"
      "s_waitcnt vmcnt(16)\n"
      "v_swap_b32 v193, v196\n"
      "v_swap_b32 v195, v198\n"
      "v_swap_b32 v201, v204\n"
      "v_swap_b32 v203, v206\n"
      "v_mfma_f32_16x16x32_bf16 a[224:227], v[192:195], v[224:227], a[224:227]\n"
      "v_mfma_f32_16x16x32_bf16 a[228:231], v[196:199], v[224:227], a[228:231]\n"
      "v_mfma_f32_16x16x32_bf16 a[232:235], v[200:203], v[224:227], a[232:235]\n"
      "v_mfma_f32_16x16x32_bf16 a[236:239], v[204:207], v[224:227], a[236:239]\n"
      "v_mfma_f32_16x16x32_bf16 a[240:243], v[192:195], v[228:231], a[240:243]\n"
      "v_mfma_f32_16x16x32_bf16 a[244:247], v[196:199], v[228:231], a[244:247]\n"
      "v_mfma_f32_16x16x32_bf16 a[248:251], v[200:203], v[228:231], a[248:251]\n"
      "v_mfma_f32_16x16x32_bf16 a[252:255], v[204:207], v[228:231], a[252:255]\n"
      "s_waitcnt vmcnt(33)\n"
      "v_mfma_f32_16x16x32_bf16 a[224:227], v[112:115], v[184:187], a[224:227]\n"
      "v_mfma_f32_16x16x32_bf16 a[228:231], v[116:119], v[184:187], a[228:231]\n"
      "v_mfma_f32_16x16x32_bf16 a[232:235], v[120:123], v[184:187], a[232:235]\n"
      "v_mfma_f32_16x16x32_bf16 a[236:239], v[124:127], v[184:187], a[236:239]\n"
      "s_waitcnt vmcnt(32)\n"
      "v_mfma_f32_16x16x32_bf16 a[240:243], v[112:115], v[188:191], a[240:243]\n"
      "v_mfma_f32_16x16x32_bf16 a[244:247], v[116:119], v[188:191], a[244:247]\n"
      "v_mfma_f32_16x16x32_bf16 a[248:251], v[120:123], v[188:191], a[248:251]\n"
      "v_mfma_f32_16x16x32_bf16 a[252:255], v[124:127], v[188:191], a[252:255]\n"
      "s_waitcnt vmcnt(0)\n"
      "v_swap_b32 v209, v212\n"
      "v_swap_b32 v211, v214\n"
      "v_swap_b32 v217, v220\n"
      "v_swap_b32 v219, v222\n"
      "v_mfma_f32_16x16x32_bf16 a[224:227], v[208:211], v[232:235], a[224:227]\n"
      "v_mfma_f32_16x16x32_bf16 a[228:231], v[212:215], v[232:235], a[228:231]\n"
      "v_mfma_f32_16x16x32_bf16 a[232:235], v[216:219], v[232:235], a[232:235]\n"
      "v_mfma_f32_16x16x32_bf16 a[236:239], v[220:223], v[232:235], a[236:239]\n"
      "v_mfma_f32_16x16x32_bf16 a[240:243], v[208:211], v[236:239], a[240:243]\n"
      "v_mfma_f32_16x16x32_bf16 a[244:247], v[212:215], v[236:239], a[244:247]\n"
      "v_mfma_f32_16x16x32_bf16 a[248:251], v[216:219], v[236:239], a[248:251]\n"
      "v_mfma_f32_16x16x32_bf16 a[252:255], v[220:223], v[236:239], a[252:255]\n"
      "s_nop 0\n"
      "ds_write_b128 v249, a[224:227] offset:0\n"
      "ds_write_b128 v249, a[228:231] offset:64\n"
      "ds_write_b128 v249, a[232:235] offset:128\n"
      "ds_write_b128 v249, a[236:239] offset:192\n"
      "ds_write_b128 v249, a[240:243] offset:4352\n"
      "ds_write_b128 v249, a[244:247] offset:4416\n"
      "ds_write_b128 v249, a[248:251] offset:4480\n"
      "ds_write_b128 v249, a[252:255] offset:4544\n"
      "s_waitcnt lgkmcnt(0)\n"
      "s_barrier\n"
      "ds_read_b128 v[0:3], v250 offset:0\n"
      "ds_read_b128 v[4:7], v250 offset:16\n"
      "ds_read_b128 v[8:11], v250 offset:8704\n"
      "ds_read_b128 v[12:15], v250 offset:8720\n"
      "ds_read_b128 v[16:19], v250 offset:17408\n"
      "ds_read_b128 v[20:23], v250 offset:17424\n"
      "ds_read_b128 v[24:27], v250 offset:26112\n"
      "ds_read_b128 v[28:31], v250 offset:26128\n"
      "v_lshlrev_b32 v32, 16, v252\n"
      "v_and_b32 v33, 0xffff0000, v252\n"
      "v_lshlrev_b32 v34, 16, v253\n"
      "v_and_b32 v35, 0xffff0000, v253\n"
      "v_lshlrev_b32 v36, 16, v254\n"
      "v_and_b32 v37, 0xffff0000, v254\n"
      "v_lshlrev_b32 v38, 16, v255\n"
      "v_and_b32 v39, 0xffff0000, v255\n"
      "s_waitcnt lgkmcnt(6)\n"
      "v_add_f32 v32, v32, v0\n"
      "v_add_f32 v33, v33, v1\n"
      "v_add_f32 v34, v34, v2\n"
      "v_add_f32 v35, v35, v3\n"
      "v_add_f32 v36, v36, v4\n"
      "v_add_f32 v37, v37, v5\n"
      "v_add_f32 v38, v38, v6\n"
      "v_add_f32 v39, v39, v7\n"
      "s_waitcnt lgkmcnt(4)\n"
      "v_add_f32 v32, v32, v8\n"
      "v_add_f32 v33, v33, v9\n"
      "v_add_f32 v34, v34, v10\n"
      "v_add_f32 v35, v35, v11\n"
      "v_add_f32 v36, v36, v12\n"
      "v_add_f32 v37, v37, v13\n"
      "v_add_f32 v38, v38, v14\n"
      "v_add_f32 v39, v39, v15\n"
      "s_waitcnt lgkmcnt(2)\n"
      "v_add_f32 v32, v32, v16\n"
      "v_add_f32 v33, v33, v17\n"
      "v_add_f32 v34, v34, v18\n"
      "v_add_f32 v35, v35, v19\n"
      "v_add_f32 v36, v36, v20\n"
      "v_add_f32 v37, v37, v21\n"
      "v_add_f32 v38, v38, v22\n"
      "v_add_f32 v39, v39, v23\n"
      "s_waitcnt lgkmcnt(0)\n"
      "v_add_f32 v32, v32, v24\n"
      "v_add_f32 v33, v33, v25\n"
      "v_add_f32 v34, v34, v26\n"
      "v_add_f32 v35, v35, v27\n"
      "v_add_f32 v36, v36, v28\n"
      "v_add_f32 v37, v37, v29\n"
      "v_add_f32 v38, v38, v30\n"
      "v_add_f32 v39, v39, v31\n"
      "v_cvt_pk_bf16_f32 v40, v32, v33\n"
      "v_cvt_pk_bf16_f32 v41, v34, v35\n"
      "v_cvt_pk_bf16_f32 v42, v36, v37\n"
      "v_cvt_pk_bf16_f32 v43, v38, v39\n"
      "buffer_store_dwordx4 v[40:43], v251, s[76:79], s21 offen\n"
      "s_add_u32 s13, s13, 688128\n"
      "s_add_u32 s14, s14, 393216\n"
      "s_add_u32 s15, s15, 393216\n"
      "s_add_u32 s17, s17, 393216\n"
      "s_add_u32 s21, s21, 196608\n"
      "s_add_u32 s12, s12, 6\n"
      "s_cmp_lt_u32 s12, 64\n"
      "s_cbranch_scc1 LO_loop\n"
      "v_mov_b32 v0, s85\n"
      "buffer_store_dword v0, v242, s[8:11], 0 offen sc1\n"
      "s_branch Lend\n"
      "Lend:\n"
      "s_waitcnt vmcnt(0) lgkmcnt(0)\n"
      : "+{s[12:27]}"(scv), "+{s[68:83]}"(srds), "+{s84}"(s84), "+{v[88:95]}"(v1), "+{v[96:127]}"(v0)
      : "{s[8:11]}"(srd), "i"(0)

      : "v0", "v1", "v2", "v3", "v4", "v5", "v6", "v7", "v8", "v9", "v10", "v11", "v12", "v13", "v14", "v15", "v16", "v17", "v18", "v19",
        "v20", "v21", "v22", "v23", "v24", "v25", "v26", "v27", "v28", "v29", "v30", "v31", "v32", "v33", "v34", "v35", "v36", "v37", "v38", "v39",
        "v40", "v41", "v42", "v43", "v44", "v45", "v46", "v47", "v48", "v49", "v50", "v51", "v52", "v53", "v54", "v55", "v56", "v57", "v58", "v59",
        "v60", "v61", "v62", "v63", "v64", "v65", "v66", "v67", "v68", "v69", "v70", "v71", "v72", "v73", "v74", "v75", "v76", "v77", "v78", "v79",
        "v80", "v81", "v82", "v83", "v84", "v85", "v86", "v87", "v128", "v129", "v130", "v131", "v132", "v133", "v134", "v135", "v136", "v137", "v138", "v139",
        "v140", "v141", "v142", "v143", "v144", "v145", "v146", "v147", "v148", "v149", "v150", "v151", "v152", "v153", "v154", "v155", "v156", "v157", "v158", "v159",
        "v160", "v161", "v162", "v163", "v164", "v165", "v166", "v167", "v168", "v169", "v170", "v171", "v172", "v173", "v174", "v175", "v176", "v177", "v178", "v179",
        "v180", "v181", "v182", "v183", "v184", "v185", "v186", "v187", "v188", "v189", "v190", "v191", "v192", "v193", "v194", "v195", "v196", "v197", "v198", "v199",
        "v200", "v201", "v202", "v203", "v204", "v205", "v206", "v207", "v208", "v209", "v210", "v211", "v212", "v213", "v214", "v215", "v216", "v217", "v218", "v219",
        "v220", "v221", "v222", "v223", "v224", "v225", "v226", "v227", "v228", "v229", "v230", "v231", "v232", "v233", "v234", "v235", "v236", "v237", "v238", "v239",
        "v240", "v241", "v242", "v243", "v244", "v245", "v246", "v247", "v248", "v249", "v250", "v251", "v252", "v253", "v254", "v255", "a0", "a1", "a2", "a3",
        "a4", "a5", "a6", "a7", "a8", "a9", "a10", "a11", "a12", "a13", "a14", "a15", "a16", "a17", "a18", "a19", "a20", "a21", "a22", "a23",
        "a24", "a25", "a26", "a27", "a28", "a29", "a30", "a31", "a32", "a33", "a34", "a35", "a36", "a37", "a38", "a39", "a40", "a41", "a42", "a43",
        "a44", "a45", "a46", "a47", "a48", "a49", "a50", "a51", "a52", "a53", "a54", "a55", "a56", "a57", "a58", "a59", "a60", "a61", "a62", "a63",
        "a64", "a65", "a66", "a67", "a68", "a69", "a70", "a71", "a72", "a73", "a74", "a75", "a76", "a77", "a78", "a79", "a80", "a81", "a82", "a83",
        "a84", "a85", "a86", "a87", "a88", "a89", "a90", "a91", "a92", "a93", "a94", "a95", "a96", "a97", "a98", "a99", "a100", "a101", "a102", "a103",
        "a104", "a105", "a106", "a107", "a108", "a109", "a110", "a111", "a112", "a113", "a114", "a115", "a116", "a117", "a118", "a119", "a120", "a121", "a122", "a123",
        "a124", "a125", "a126", "a127", "a128", "a129", "a130", "a131", "a132", "a133", "a134", "a135", "a136", "a137", "a138", "a139", "a140", "a141", "a142", "a143",
        "a144", "a145", "a146", "a147", "a148", "a149", "a150", "a151", "a152", "a153", "a154", "a155", "a156", "a157", "a158", "a159", "a160", "a161", "a162", "a163",
        "a164", "a165", "a166", "a167", "a168", "a169", "a170", "a171", "a172", "a173", "a174", "a175", "a176", "a177", "a178", "a179", "a180", "a181", "a182", "a183",
        "a184", "a185", "a186", "a187", "a188", "a189", "a190", "a191", "a192", "a193", "a194", "a195", "a196", "a197", "a198", "a199", "a200", "a201", "a202", "a203",
        "a204", "a205", "a206", "a207", "a208", "a209", "a210", "a211", "a212", "a213", "a214", "a215", "a216", "a217", "a218", "a219", "a220", "a221", "a222", "a223",
        "a224", "a225", "a226", "a227", "a228", "a229", "a230", "a231", "a232", "a233", "a234", "a235", "a236", "a237", "a238", "a239", "a240", "a241", "a242", "a243",
        "a244", "a245", "a246", "a247", "a248", "a249", "a250", "a251", "a252", "a253", "a254", "a255", "s28", "s29", "s30", "s31", "s35", "s36", "s37", "s38",
        "s39", "s40", "s41", "s42", "s43", "s44", "s45", "s46", "s47", "s48", "s49", "s50", "s51", "s52", "s53", "s54", "s55", "s58", "s59", "s60",
        "s61", "s62", "s63", "s64", "s65", "s66", "s67", "s85", "vcc", "scc", "memory");
}

#define D_A 1450
#define D_B 2650
#define D_MA 80
#define D_MB 140
#define D_V 130
#define D_N 2000
extern "C" void kernel_launch(void* const* d_in, const int* in_sizes, int n_in, void* d_out, int out_size, void* d_ws, size_t ws_size,
                              hipStream_t stream) {
  (void)in_sizes; (void)n_in; (void)out_size; (void)ws_size;
  Params p;
  p.x = (const u32*)d_in[0]; p.mem0 = (const u32*)d_in[1]; p.wq = (const u32*)d_in[2]; p.wk = (const u32*)d_in[3];
  p.wv = (const u32*)d_in[4]; p.wo = (const u32*)d_in[5]; p.wg = (const u32*)d_in[8]; p.wu = (const u32*)d_in[9];
  p.wd = (const u32*)d_in[10]; p.out = (u32*)d_out; p.ws = (unsigned char*)d_ws;
  p.vis = D_V; p.pen = D_N; p.mB = D_MB; p.dA = D_A; p.dB = D_B; p.mA = D_MA;
  constexpr int LDS = 160 * 1024;
  (void)hipFuncSetAttribute((const void*)k_main, hipFuncAttributeMaxDynamicSharedMemorySize, LDS);
  hipLaunchKernelGGL(k_main, dim3(256), dim3(256), LDS, stream, p);
}
```
